```python
import torch

def _softmax(x, axis=-1):
    return torch.softmax(x, dim=axis)

def _gelu(x):
    return torch.nn.functional.gelu(x, approximate='tanh')

def _silu(x):
    return torch.nn.functional.silu(x)

def _sigmoid(x):
    return torch.sigmoid(x)

def _layernorm(x, axis=-1, eps=1e-05):
    m = x.mean(dim=axis, keepdim=True)
    v = x.var(dim=axis, keepdim=True, unbiased=False)
    return (x - m) / torch.sqrt(v + eps)

def _scatter_add(tgt, idx, src, axis=0):
    return tgt.clone().index_add_(axis, idx, src)

def _segment_sum(src, seg, n):
    out = torch.zeros((n,) + src.shape[1:], dtype=src.dtype)
    return out.index_add_(0, seg, src)

def _pad(x, pw):
    flat = tuple((v for pair in reversed(pw) for v in pair))
    return torch.nn.functional.pad(x, flat)
kernel_name = 'parallel_sum__memory_aug_recurrent_mem_tr__glu_gated_reglu__prompt'

def setup_inputs():

    _w = lambda a: (lambda t: t.to(torch.bfloat16) if t.is_floating_point() else t)(torch.as_tensor(a))

    def _A_setup():
        B, S, Dm = (1, 2048, 640)
        H, Dh = (32, 128)
        M, seg = (8, 32)
        seg = min(seg, S)
        S = S // seg * seg
        return {'x': _w((lambda *s: torch.randn(*s, dtype=torch.bfloat16) * 0.05)(B, S, Dm)), 'mem0': _w((lambda *s: torch.randn(*s, dtype=torch.bfloat16) * 0.2)(B, M, Dm)), 'Wq': _w((lambda *s: torch.randn(*s, dtype=torch.bfloat16) * 1.6)(Dm, H, Dh) * Dm ** (-0.5)), 'Wk': _w((lambda *s: torch.randn(*s, dtype=torch.bfloat16) * 1.6)(Dm, H, Dh) * Dm ** (-0.5)), 'Wv': _w((lambda *s: torch.randn(*s, dtype=torch.bfloat16) * 0.2)(Dm, H, Dh) * Dm ** (-0.5)), 'Wo': _w((lambda *s: torch.randn(*s, dtype=torch.bfloat16) * 0.2)(H, Dh, Dm) * (H * Dh) ** (-0.5)), 'seg_len': seg}
    _A_in = _A_setup()

    def _B_setup():
        B, S, D, F = (1, 2048, 640, 1728)
        return {'x': _w((lambda *s: torch.randn(*s, dtype=torch.bfloat16) * 0.05)(B, S, D)), 'W_gate': _w((lambda *s: torch.randn(*s, dtype=torch.bfloat16) * 0.2)(D, F) * (2.0 / D) ** 0.5), 'W_up': _w((lambda *s: torch.randn(*s, dtype=torch.bfloat16) * 0.2)(D, F) * (2.0 / D) ** 0.5), 'W_down': _w((lambda *s: torch.randn(*s, dtype=torch.bfloat16) * 0.2)(F, D) * (1.0 / F) ** 0.5)}
    _B_in = _B_setup()
    return {'A_x': _A_in['x'], 'A_mem0': _A_in['mem0'], 'A_Wq': _A_in['Wq'], 'A_Wk': _A_in['Wk'], 'A_Wv': _A_in['Wv'], 'A_Wo': _A_in['Wo'], 'A_seg_len': _A_in['seg_len'], 'B_x': _B_in['x'], 'B_W_gate': _B_in['W_gate'], 'B_W_up': _B_in['W_up'], 'B_W_down': _B_in['W_down']}

def reference(A_x, A_mem0, A_Wq, A_Wk, A_Wv, A_Wo, A_seg_len, B_x, B_W_gate, B_W_up, B_W_down):

    def _A_ref(x, mem0, Wq, Wk, Wv, Wo, seg_len):
        B, S, Dm = x.shape
        H, Dh = (Wq.shape[1], Wq.shape[2])
        M = mem0.shape[1]
        scale = Dh ** (-0.5)
        n_seg = S // seg_len
        L = M + seg_len
        idx = torch.arange(L)
        mask = (idx[:, None] < M) | (idx[:, None] >= idx[None, :])
        mem = mem0
        outs = []
        for s in range(n_seg):
            xs = x[:, s * seg_len:(s + 1) * seg_len, :].clone()
            h = torch.concatenate([mem, xs], axis=1)
            q = torch.einsum('btd,dhe->bhte', h, Wq)
            k = torch.einsum('btd,dhe->bhte', h, Wk)
            v = torch.einsum('btd,dhe->bhte', h, Wv)
            scores = torch.einsum('bhqe,bhke->bhqk', q, k) * scale
            scores = torch.where(mask[None, None], scores, -1000000000.0)
            p = _softmax(scores, axis=-1)
            o = torch.einsum('bhqk,bhke->bhqe', p, v)
            o = torch.einsum('bhte,hed->btd', o, Wo)
            mem = o[:, :M, :].clone()
            outs.append(o[:, M:, :])
        return torch.concatenate(outs, axis=1)

    def _B_ref(x, W_gate, W_up, W_down):
        gate = torch.einsum('bsd,df->bsf', x, W_gate)
        up = torch.einsum('bsd,df->bsf', x, W_up)
        gate = gate * (lambda x, dt: x.to(dt))(gate > 0, gate.dtype)
        h = gate * up
        return torch.einsum('bsf,fd->bsd', h, W_down)
    x = A_x
    xn = _layernorm(x, axis=-1)
    return x + _A_ref(xn, A_mem0, A_Wq, A_Wk, A_Wv, A_Wo, A_seg_len) + _B_ref(xn, B_W_gate, B_W_up, B_W_down)
```

```cpp
#include <hip/hip_runtime.h>
#include <hip/hip_bf16.h>
#include <stdint.h>

#define DI __device__ __forceinline__
#define LAS __attribute__((address_space(3)))
typedef unsigned v2u __attribute__((ext_vector_type(2)));
typedef unsigned v4u __attribute__((ext_vector_type(4)));
typedef float f32x2 __attribute__((ext_vector_type(2)));
typedef float f32x4 __attribute__((ext_vector_type(4)));
typedef short s16x4 __attribute__((ext_vector_type(4)));
typedef short s16x8 __attribute__((ext_vector_type(8)));
typedef __bf16 bf16x2 __attribute__((ext_vector_type(2)));
typedef __bf16 bf16x8 __attribute__((ext_vector_type(8)));
typedef __bf16 bf16x16 __attribute__((ext_vector_type(16)));
typedef __amdgpu_buffer_rsrc_t rsrc_t;

constexpr int SEQ = 2048, DM = 640, NH = 32, DH = 128, FF = 1728, NSEG = 64;
constexpr int SC1 = 16;

constexpr unsigned EX_BUF = 163840;
constexpr unsigned EX_BYTES = 3 * EX_BUF;
constexpr unsigned EX_MEM = 0, EX_HINT_H = 10752, EX_HINT_C = 11264, EX_O = 12288;
constexpr unsigned CTL = 524288, CTL_BYTES = 32768;
constexpr unsigned C_TICK = CTL, C_TOTAL = CTL + 64, C_OVF = CTL + 128, C_ABORT = CTL + 256, C_CFG = CTL + 512;
constexpr unsigned C_PF = CTL + 1024;
constexpr unsigned C_OF = CTL + 16384;
constexpr unsigned PROBE = CTL + CTL_BYTES + 32768;
constexpr unsigned W_XN = 2u << 20;
constexpr unsigned W_KX = 6u << 20;
constexpr unsigned W_QX = W_KX + (16u << 20);
constexpr unsigned W_VX = W_QX + (16u << 20);
constexpr unsigned W_OT = W_VX + (16u << 20);
constexpr unsigned W_HD = W_OT + (16u << 20);
constexpr unsigned W_END = W_HD + (8u << 20);

constexpr int LDS_BYTES = 160 * 1024;
constexpr unsigned SPIN_TICKS = 3000000;
constexpr int DELTA = 12;

struct Args {
  const uint16_t* x; const uint16_t* mem0; const uint16_t* wq; const uint16_t* wk; const uint16_t* wv; const uint16_t* wo;
  const uint16_t* wg; const uint16_t* wu; const uint16_t* wd; uint16_t* out; unsigned char* ws;
};

DI unsigned ticks() { unsigned long long t; asm volatile("s_memrealtime %0\n\ts_waitcnt lgkmcnt(0)" : "=s"(t) :: "memory"); return (unsigned)t; }
DI unsigned cycles() { unsigned long long t; asm volatile("s_memtime %0\n\ts_waitcnt lgkmcnt(0)" : "=s"(t) :: "memory"); return (unsigned)t; }
DI unsigned xcc_id() { return (unsigned)__builtin_amdgcn_s_getreg(20 | (3 << 11)) & 7u; }
DI unsigned uni(unsigned v) { return __builtin_amdgcn_readfirstlane(v); }
DI rsrc_t mkrsrc(const void* p, unsigned bytes) { return __builtin_amdgcn_make_buffer_rsrc((void*)p, 0, bytes, 0x00020000); }
DI void bar() { asm volatile("s_waitcnt lgkmcnt(0)\n\ts_barrier" ::: "memory"); }
template <int N> DI void vmwait() { asm volatile("s_waitcnt vmcnt(%0)" :: "i"(N) : "memory"); }
DI void lgkm0() { asm volatile("s_waitcnt lgkmcnt(0)" ::: "memory"); }
DI unsigned pk2(float a, float b) { f32x2 v = {a, b}; return __builtin_bit_cast(unsigned, __builtin_convertvector(v, bf16x2)); }
DI float bflo(unsigned u) { return __uint_as_float(u << 16); }
DI float bfhi(unsigned u) { return __uint_as_float(u & 0xffff0000u); }
DI float rbf(float a) { return bflo(pk2(a, a)); }
DI f32x4 mfma32(bf16x8 a, bf16x8 b, f32x4 c) { return __builtin_amdgcn_mfma_f32_16x16x32_bf16(a, b, c, 0, 0, 0); }
DI f32x4 mfma16(s16x4 a, s16x4 b, f32x4 c) { return __builtin_amdgcn_mfma_f32_16x16x16bf16_1k(a, b, c, 0, 0, 0); }
DI f32x4 smf(v4u a, bf16x16 b, f32x4 c, int idx) { return __builtin_amdgcn_smfmac_f32_16x16x64_bf16(__builtin_bit_cast(bf16x8, a), b, c, idx, 0, 0); }
DI s16x4 trread(unsigned a) { return __builtin_amdgcn_ds_read_tr16_b64_v4i16((LAS s16x4*)a); }
DI bf16x8 cat8(s16x4 a, s16x4 b) { return __builtin_bit_cast(bf16x8, __builtin_shufflevector(a, b, 0, 1, 2, 3, 4, 5, 6, 7)); }
DI bf16x16 cat16(bf16x8 a, bf16x8 b) { return __builtin_shufflevector(a, b, 0, 1, 2, 3, 4, 5, 6, 7, 8, 9, 10, 11, 12, 13, 14, 15); }
DI bf16x8 ldsfrag(unsigned a) { return *(LAS bf16x8*)a; }
DI bf16x8 asfrag(v4u v) { return __builtin_bit_cast(bf16x8, v); }
DI void dma(rsrc_t r, unsigned voff, unsigned soff, unsigned dst) {
  asm volatile("s_mov_b32 m0, %0\n\ts_nop 0\n\tbuffer_load_dwordx4 %1, %2, %3 offen lds" :: "s"(dst), "v"(voff), "s"(r), "s"(soff) : "memory");
}
DI void dma_sc1(rsrc_t r, unsigned voff, unsigned soff, unsigned dst) {
  asm volatile("s_mov_b32 m0, %0\n\ts_nop 0\n\tbuffer_load_dwordx4 %1, %2, %3 offen sc1 lds" :: "s"(dst), "v"(voff), "s"(r), "s"(soff) : "memory");
}
DI void ld16_sc1(v4u& d, rsrc_t r, unsigned voff, unsigned soff) {
  asm volatile("buffer_load_dwordx4 %0, %1, %2, %3 offen sc1" : "=v"(d) : "v"(voff), "s"(r), "s"(soff) : "memory");
}
template <int N> DI void landed(v4u& d) { asm volatile("s_waitcnt vmcnt(%1)" : "+v"(d) : "i"(N) : "memory"); }
DI unsigned ld4_now(rsrc_t r, unsigned voff) {
  unsigned d; asm volatile("buffer_load_dword %0, %1, %2, 0 offen sc1\n\ts_waitcnt vmcnt(0)" : "=&v"(d) : "v"(voff), "s"(r) : "memory"); return d;
}
DI v2u ld8_now(rsrc_t r, unsigned voff) {
  v2u d; asm volatile("buffer_load_dwordx2 %0, %1, %2, 0 offen sc1\n\ts_waitcnt vmcnt(0)" : "=&v"(d) : "v"(voff), "s"(r) : "memory"); return d;
}
DI void st4(unsigned v, rsrc_t r, unsigned voff, unsigned soff) { __builtin_amdgcn_raw_buffer_store_b32(v, r, voff, soff, SC1); }
DI void st8(v2u v, rsrc_t r, unsigned voff, unsigned soff) { __builtin_amdgcn_raw_buffer_store_b64(v, r, voff, soff, SC1); }
DI void st16(v4u v, rsrc_t r, unsigned voff, unsigned soff) { __builtin_amdgcn_raw_buffer_store_b128(v, r, voff, soff, SC1); }
template <class T> DI void pin_a(T& v) { asm volatile("" : "+a"(v)); }
template <class T> DI void pin_v(T& v) { asm volatile("" : "+v"(v)); }
DI void fence_sched() { __builtin_amdgcn_sched_barrier(0); }
DI float max3f(float a, float b, float c) { return __builtin_elementwise_maximum(__builtin_elementwise_maximum(a, b), c); }
DI float xmax16(float v) { auto p = __builtin_amdgcn_permlane16_swap(__float_as_uint(v), __float_as_uint(v), false, false); return __builtin_elementwise_maximum(__uint_as_float(p[0]), __uint_as_float(p[1])); }
DI float xmax32(float v) { auto p = __builtin_amdgcn_permlane32_swap(__float_as_uint(v), __float_as_uint(v), false, false); return __builtin_elementwise_maximum(__uint_as_float(p[0]), __uint_as_float(p[1])); }
DI float xsum16(float v) { auto p = __builtin_amdgcn_permlane16_swap(__float_as_uint(v), __float_as_uint(v), false, false); return __uint_as_float(p[0]) + __uint_as_float(p[1]); }
DI float xsum32(float v) { auto p = __builtin_amdgcn_permlane32_swap(__float_as_uint(v), __float_as_uint(v), false, false); return __uint_as_float(p[0]) + __uint_as_float(p[1]); }
constexpr unsigned DROP = 0xf0000000u;
#define SMF_A(acc, a, w, idx) asm volatile("v_smfmac_f32_16x16x64_bf16 %0, %1, %2, %3" : "+v"(acc) : "v"(a), "a"(w), "v"(idx))
#define SMF_V(acc, a, w, idx) asm volatile("v_smfmac_f32_16x16x64_bf16 %0, %1, %2, %3" : "+v"(acc) : "v"(a), "v"(w), "v"(idx))
#define MF32_0(d, a, b) asm volatile("v_mfma_f32_16x16x32_bf16 %0, %1, %2, 0" : "=&v"(d) : "v"(a), "v"(b))
#define MF32_C(d, a, b, c) asm volatile("v_mfma_f32_16x16x32_bf16 %0, %1, %2, %3" : "=&v"(d) : "v"(a), "v"(b), "v"(c))
#define MF32(d, a, b) asm volatile("v_mfma_f32_16x16x32_bf16 %0, %1, %2, %0" : "+v"(d) : "v"(a), "v"(b))
#define MF16_0(d, a, b) asm volatile("v_mfma_f32_16x16x16_bf16 %0, %1, %2, 0" : "=&v"(d) : "v"(a), "v"(b))
#define MF16(d, a, b) asm volatile("v_mfma_f32_16x16x16_bf16 %0, %1, %2, %0" : "+v"(d) : "v"(a), "v"(b))
#define NOPS(n) asm volatile("s_nop %0" :: "i"((n) - 1))
#define DSR128(d, addr, off) asm volatile("ds_read_b128 %0, %1 offset:%2" : "=v"(d) : "v"(addr), "i"(off) : "memory")
#define DSR64(d, addr, off) asm volatile("ds_read_b64 %0, %1 offset:%2" : "=v"(d) : "v"(addr), "i"(off) : "memory")
#define DSRTR(d, addr, off) asm volatile("ds_read_b64_tr_b16 %0, %1 offset:%2" : "=v"(d) : "v"(addr), "i"(off) : "memory")
#define DSW64(addr, v, off) asm volatile("ds_write_b64 %0, %1 offset:%2" :: "v"(addr), "v"(v), "i"(off) : "memory")
#define DSW32(addr, v, off) asm volatile("ds_write_b32 %0, %1 offset:%2" :: "v"(addr), "v"(v), "i"(off) : "memory")
#define STEP4(CW) asm volatile("s_waitcnt vmcnt(%c15)\n\tv_smfmac_f32_16x16x64_bf16 %0, %5, %6, %10\n\tv_smfmac_f32_16x16x64_bf16 %1, %5, %7, %10\n\t" \
    "v_max3_u32 %4, %4, %11, %12\n\tv_smfmac_f32_16x16x64_bf16 %2, %5, %8, %10\n\tv_max3_u32 %4, %4, %13, %14\n\tv_smfmac_f32_16x16x64_bf16 %3, %5, %9, %10"   \
    : "+v"(c0), "+v"(c1), "+v"(c2), "+v"(c3), "+v"(mx)                                                                                              \
    : "v"(a), "a"(w0), "a"(w1), "a"(w2), CW(w3), "v"(idx), "v"(a.x), "v"(a.y), "v"(a.z), "v"(a.w), "i"(N) : "memory")
template <int N, bool W3A> DI void step4(f32x4& c0, f32x4& c1, f32x4& c2, f32x4& c3, unsigned& mx, const v4u& a, const bf16x16& w0, const bf16x16& w1,
                                         const bf16x16& w2, const bf16x16& w3, int idx) {
  if constexpr (W3A) STEP4("a"); else STEP4("v");
}
template <bool FIRST, bool WFIRST, bool WA> DI void mfw(f32x4& d, const bf16x8& w, const v4u& x) {
#define MFW(OPS, CW) { if constexpr (FIRST) asm volatile("v_mfma_f32_16x16x32_bf16 %0, " OPS ", 0" : "=&v"(d) : CW(w), "v"(x)); \
                       else asm volatile("v_mfma_f32_16x16x32_bf16 %0, " OPS ", %0" : "+v"(d) : CW(w), "v"(x)); }
  if constexpr (WFIRST) { if constexpr (WA) MFW("%1, %2", "a") else MFW("%1, %2", "v") }
  else { if constexpr (WA) MFW("%2, %1", "a") else MFW("%2, %1", "v") }
}
template <int OFF> DI void dsr128(v4u& d, unsigned addr) { DSR128(d, addr, OFF); }
template <int N> DI void lgkm2(v4u& a, v4u& b) { asm volatile("s_waitcnt lgkmcnt(%2)" : "+v"(a), "+v"(b) : "i"(N) : "memory"); }
DI v4u cat4(v2u a, v2u b) { return __builtin_shufflevector(a, b, 0, 1, 2, 3); }
DI unsigned umax3(unsigned a, unsigned b, unsigned c) { return max(max(a, b), c); }
DI unsigned noff(unsigned v) { return min(v, 0xfffffffeu); }
DI unsigned exmap(unsigned lo, unsigned cfg) { unsigned pg = lo >> 12; return (((pg >> 2) * 8 + ((cfg >> (4 * (pg & 3))) & 7)) << 12) | (lo & 4095); }

DI unsigned uld4(rsrc_t r, unsigned soff) {
  unsigned d; asm volatile("buffer_load_dword %0, off, %1, %2 sc1\n\ts_waitcnt vmcnt(0)" : "=&v"(d) : "s"(r), "s"(soff) : "memory"); return uni(d);
}
DI v2u uld8(rsrc_t r, unsigned soff) {
  v2u d; asm volatile("buffer_load_dwordx2 %0, off, %1, %2 sc1\n\ts_waitcnt vmcnt(0)" : "=&v"(d) : "s"(r), "s"(soff) : "memory"); return d;
}
DI void spin_guard(unsigned& n, unsigned t0, rsrc_t ws) {
  if ((++n & 63u) != 0u) return;
  if (uld4(ws, C_ABORT) != 0u) __builtin_amdgcn_endpgm();
  if (ticks() - t0 > SPIN_TICKS) {
    unsigned one = 1u;
    asm volatile("buffer_store_dword %0, off, %1, %2 sc1" :: "v"(one), "s"(ws), "s"(C_ABORT) : "memory");
    __builtin_amdgcn_endpgm();
  }
}
DI unsigned wait_flags(rsrc_t ws, unsigned voff, bool on, unsigned need) {
  unsigned n = 0; const unsigned t0 = ticks();
  for (;;) {
    unsigned v = ld4_now(ws, voff);
    v = on ? v : 0xffffffffu;
    if (__all(v >= need)) {
#pragma unroll
      for (unsigned d = 16; d >= 1; d >>= 1) if (__all(v >= need + d)) need += d;
      return need;
    }
    spin_guard(n, t0, ws);
    __builtin_amdgcn_s_sleep(2);
  }
}
DI void wait_hint(rsrc_t ws, unsigned soff, unsigned tag) {
  unsigned n = 0; const unsigned t0 = ticks();
  for (;;) {
    const v2u hv = uld8(ws, soff);
    if (uni(hv.x) == tag) {
      const unsigned target = uni(hv.y);
      const int rem = (int)(target - ticks());
      if (rem > 2000) return;
      if (rem <= 12) { while ((int)(target - ticks()) > 0) {} return; }
    }
    spin_guard(n, t0, ws);
  }
}

enum { MAP_NAT = 0, MAP_SP = 1, MAP_PERM = 2 };
template <int MAP> DI unsigned rowmap(unsigned k) {
  if (MAP == MAP_SP) return (k & 48u) + 8u * ((k >> 1) & 1u) + 2u * ((k >> 2) & 3u) + (k & 1u);
  if (MAP == MAP_PERM) return 16u * ((k >> 2) & 1u) + 4u * (k >> 3) + (k & 3u);
  return k;
}
template <int ROWB> DI unsigned img_off(unsigned row, unsigned ch) {
  if (ROWB == 128) return 128u * row + 32u * ((ch >> 1) ^ (((row >> 1) & 1u) | (((row >> 3) & 1u) << 1))) + 16u * (ch & 1u);
  if (ROWB == 64) return 64u * row + 32u * ((ch >> 1) ^ ((row >> 3) & 1u)) + 16u * (ch & 1u);
  return 32u * (row ^ (((row >> 3) & 1u) << 2)) + 16u * ch;
}
template <int ROWB, int MAP> DI unsigned img_src(unsigned lane, unsigned p, unsigned half, unsigned stride, unsigned colbyte) {
  constexpr unsigned CPR = ROWB / 16;
  unsigned pos = p * 64u + lane, prow = pos / CPR, slot = pos % CPR, row, ch;
  if (ROWB == 128) { row = prow; ch = (((slot >> 1) ^ (((row >> 1) & 1u) | (((row >> 3) & 1u) << 1))) << 1) | (slot & 1u); }
  else if (ROWB == 64) { row = prow; ch = (((slot >> 1) ^ ((row >> 3) & 1u)) << 1) | (slot & 1u); }
  else { row = prow ^ (((prow >> 3) & 1u) << 2); ch = slot; }
  return rowmap<MAP>(row + 32u * half) * stride + colbyte + 16u * ch;
}
template <int ROWB> DI bf16x8 img_frag(unsigned base, unsigned lane, unsigned c) {
  const unsigned g = lane >> 4, q = (lane & 15) >> 2, p = lane & 3;
  const s16x4 a = trread(base + img_off<ROWB>(8 * g + q, 2 * c + (p >> 1)) + 8 * (p & 1));
  const s16x4 b = trread(base + img_off<ROWB>(8 * g + 4 + q, 2 * c + (p >> 1)) + 8 * (p & 1));
  return cat8(a, b);
}
template <int I> struct IC { static constexpr int v = I; };
template <int B, int E, class F> DI void sfor(F&& f) { if constexpr (B < E) { f(IC<B>{}); sfor<B + 1, E>(f); } }
template <int NST, int G, int DEPTH, class ISSUE, class READ> DI void stream_weights(ISSUE&& issue, READ&& read) {
  sfor<0, (DEPTH < NST ? DEPTH : NST)>([&](auto i) { issue(i); });
  sfor<0, NST>([&](auto i) {
    constexpr int left = (NST - 1 - i.v) < (DEPTH - 1) ? (NST - 1 - i.v) : (DEPTH - 1);
    vmwait<left * G>();
    read(i);
    if constexpr (i.v + DEPTH < NST) { lgkm0(); issue(IC<i.v + DEPTH>{}); }
  });
  lgkm0();
}

DI void role_head(const Args& a, rsrc_t ws, unsigned cfg, int h, int eta, unsigned lane, int w) {
  const unsigned g = lane >> 4, c16 = lane & 15;
  const int rot = (3 * w + 5 * eta + h) % 10;
  bf16x16 WQ[2][10], WK[2][10], WV[10];
  {
    const rsrc_t rq = mkrsrc(a.wq, DM * 4096 * 2), rk = mkrsrc(a.wk, DM * 4096 * 2), rv = mkrsrc(a.wv, DM * 4096 * 2);
    const unsigned base = w * 40960u;
    unsigned sqk[2][2], sv[2];
#pragma unroll
    for (int hf = 0; hf < 2; ++hf) {
      sqk[hf][0] = img_src<64, MAP_SP>(lane, 0, hf, 8192, (h * 128 + 32 * w) * 2);
      sqk[hf][1] = img_src<64, MAP_SP>(lane, 1, hf, 8192, (h * 128 + 32 * w) * 2);
      sv[hf] = img_src<32, MAP_SP>(lane, 0, hf, 8192, (h * 128 + 64 * eta + 16 * w) * 2);
    }
    bf16x8 tq[2], tk[2], tv;
    stream_weights<20, 5, 8>(
        [&](auto i) {
          int ks = (i.v >> 1) + rot; ks -= ks >= 10 ? 10 : 0;
          const unsigned so = ks * 64u * 8192u, d = base + (i.v % 8) * 5120u;
          dma(rq, sqk[i.v & 1][0], so, d); dma(rq, sqk[i.v & 1][1], so, d + 1024);
          dma(rk, sqk[i.v & 1][0], so, d + 2048); dma(rk, sqk[i.v & 1][1], so, d + 3072);
          dma(rv, sv[i.v & 1], so, d + 4096);
        },
        [&](auto i) {
          const unsigned d = base + (i.v % 8) * 5120u;
          const bf16x8 q0 = img_frag<64>(d, lane, 0), q1 = img_frag<64>(d, lane, 1);
          const bf16x8 k0 = img_frag<64>(d + 2048, lane, 0), k1 = img_frag<64>(d + 2048, lane, 1);
          const bf16x8 v0 = img_frag<32>(d + 4096, lane, 0);
          if constexpr ((i.v & 1) == 0) { tq[0] = q0; tq[1] = q1; tk[0] = k0; tk[1] = k1; tv = v0; }
          else {
            WQ[0][i.v >> 1] = cat16(tq[0], q0); WQ[1][i.v >> 1] = cat16(tq[1], q1);
            WK[0][i.v >> 1] = cat16(tk[0], k0); WK[1][i.v >> 1] = cat16(tk[1], k1); WV[i.v >> 1] = cat16(tv, v0);
          }
        });
  }
  sfor<0, 10>([&](auto j) { pin_a(WQ[0][j.v]); pin_a(WQ[1][j.v]); pin_a(WK[0][j.v]); if constexpr (j.v < 2) pin_a(WK[1][j.v]); else pin_v(WK[1][j.v]); pin_v(WV[j.v]); });
  bar();
  constexpr unsigned L_K = 0, L_Q = 8192, L_V = 16384, L_BUF = 20480, L_X = 40960, L_VM = 45056;
  int idx = (lane & 8) ? 0xEEEE : 0x4444;
  const unsigned r8 = lane & 7, ph = (lane >> 3) & 1;
  const unsigned lanemem = (2 * (g >> 1) + (r8 >> 2)) * 256 + (r8 & 3) * 64 + (2 * (g & 1) + ph) * 16;
  unsigned soff[10];
#pragma unroll
  for (int j = 0; j < 10; ++j) { int ks = j + rot; ks -= ks >= 10 ? 10 : 0; soff[j] = uni(exmap(EX_MEM + ks * 1024, cfg)); }
  const unsigned tau = 4 * eta + w;
  const unsigned opub = (c16 < 8 && !(g & 1)) ? exmap(EX_O + (c16 >> 2) * 32768 + h * 1024 + tau * 128 + (g >> 1) * 64 + (c16 & 3) * 16, cfg) : DROP;
  const unsigned oflag = lane == 0 ? C_OF + (2 * h + eta) * 128 + w * 4 : DROP;
  const bool rep = (h == 0 && eta == 0 && w == 0);
  const unsigned hintH = (rep && lane == 0) ? exmap(EX_HINT_H, cfg) : DROP, hintC = exmap(EX_HINT_C, cfg);
  const unsigned pfl = C_PF + ((lane >> 1 & 3) * 16 + (h >> 1)) * 128 + (2 * (h & 1) + (lane < 4 ? (lane & 1) : eta)) * 4;
  unsigned pseen = 0;
  const unsigned l16 = lane * 16;
  auto fetch_kqv = [&](int s) {
    if (pseen < (unsigned)s + 1) pseen = wait_flags(ws, pfl, lane < 5, s + 1);
    const unsigned o = (s * 32 + h) * 8192u, d = (s & 1) * L_BUF;
    dma_sc1(ws, l16, W_KX + o + w * 2048, d + L_K + w * 2048); dma_sc1(ws, l16, W_KX + o + w * 2048 + 1024, d + L_K + w * 2048 + 1024);
    dma_sc1(ws, l16, W_QX + o + w * 2048, d + L_Q + w * 2048); dma_sc1(ws, l16, W_QX + o + w * 2048 + 1024, d + L_Q + w * 2048 + 1024);
    dma_sc1(ws, l16, W_VX + o + eta * 4096 + w * 1024, d + L_V + w * 1024);
  };
  fetch_kqv(0);
  const float CEXP = 0.08838834764831845f * 1.4426950408889634f, NEG = -1e30f;
  int cest = 100;
  const unsigned xw = (32 * w + c16) * 32 + g * 8;
  const unsigned xr = (4 * g + (c16 >> 2)) * 32 + (c16 & 3) * 8;
  const float m2 = g < 2 ? NEG : 0.f;
  f32x4 M2 = {m2, m2, m2, m2};
  pin_v(M2); pin_v(idx);
  unsigned bcur = 0, bprev = 2 * EX_BUF;
  for (int s = 0; s < NSEG; ++s) {
    f32x4 aq0, aq1, ak0, ak1, av;
    v4u am[10];
    unsigned t0 = 0; bool first = true;
    auto pass = [&]() -> bool {
      aq0 = aq1 = ak0 = ak1 = av = f32x4{0, 0, 0, 0};
      unsigned mx = 0;
      pin_v(aq0); pin_v(aq1); pin_v(ak0); pin_v(ak1); pin_v(av); pin_v(mx);
#pragma unroll
      for (int j = 0; j < 10; ++j) ld16_sc1(am[j], ws, lanemem, soff[j] + bcur);
      if (rep) t0 = ticks();
      st8(v2u{(unsigned)s + 1, t0 + cest + DELTA}, ws, hintH, 0);
      sfor<0, 10>([&](auto j) { step4<10 - j.v, (j.v < 2)>(aq0, ak0, aq1, ak1, mx, am[j.v], WQ[0][j.v], WK[0][j.v], WQ[1][j.v], WK[1][j.v], idx); });
      NOPS(6);
      return __any(mx == 0xffffffffu);
    };
    if (s > 0) wait_hint(ws, hintC, s);
    if (__builtin_expect(pass(), 0)) {
      unsigned n = 0; const unsigned tw = ticks();
      first = false;
      do spin_guard(n, tw, ws); while (pass());
    }
    {
      float x0[4], x1[4];
#pragma unroll
      for (int r = 0; r < 4; ++r) {
        auto p0 = __builtin_amdgcn_permlane32_swap(__float_as_uint(aq0[r]), __float_as_uint(ak0[r]), false, false);
        x0[r] = __uint_as_float(p0[0]) + __uint_as_float(p0[1]);
      }
      const v2u w0 = {pk2(x0[0], x0[1]), pk2(x0[2], x0[3])};
      DSW64(xw, w0, L_X);
#pragma unroll
      for (int r = 0; r < 4; ++r) {
        auto p1 = __builtin_amdgcn_permlane32_swap(__float_as_uint(aq1[r]), __float_as_uint(ak1[r]), false, false);
        x1[r] = __uint_as_float(p1[0]) + __uint_as_float(p1[1]);
      }
      const v2u w1 = {pk2(x1[0], x1[1]), pk2(x1[2], x1[3])};
      DSW64(xw, w1, L_X + 512);
    }
    SMF_V(av, am[0], WV[0], idx); SMF_V(av, am[1], WV[1], idx); SMF_V(av, am[2], WV[2], idx); SMF_V(av, am[3], WV[3], idx);
    bar();
    const unsigned kb = l16 + (s & 1) * L_BUF;
    v2u xa[4], xb[4]; v4u k0[4], k1[4], vx;
    DSRTR(xa[0], xr, L_X); DSRTR(xb[0], xr, L_X + 512); DSR128(k0[0], kb, L_K); DSR128(k1[0], kb, L_K + 4096);
    DSRTR(xa[1], xr, L_X + 1024); DSRTR(xb[1], xr, L_X + 1536); DSR128(k0[1], kb, L_K + 1024); DSR128(k1[1], kb, L_K + 5120);
    DSRTR(xa[2], xr, L_X + 2048); DSRTR(xb[2], xr, L_X + 2560); DSR128(k0[2], kb, L_K + 2048); DSR128(k1[2], kb, L_K + 6144);
    DSRTR(xa[3], xr, L_X + 3072); DSRTR(xb[3], xr, L_X + 3584);
    SMF_V(av, am[4], WV[4], idx); SMF_V(av, am[5], WV[5], idx);
    st4(s, ws, oflag, 0);
    SMF_V(av, am[6], WV[6], idx); SMF_V(av, am[7], WV[7], idx);
    { unsigned ones = ~0u; pin_v(ones); st16(v4u{ones, ones, ones, ones}, ws, opub, bprev); }
    SMF_V(av, am[8], WV[8], idx); SMF_V(av, am[9], WV[9], idx);
    v4u X[4]; f32x4 s0, s1, s2;
    asm volatile("s_waitcnt lgkmcnt(10)" : "+v"(xa[0]), "+v"(xb[0]), "+v"(k0[0]), "+v"(k1[0]) :: "memory");
    X[0] = cat4(xa[0], xb[0]);
    unsigned vb = kb + w * 1024;
    DSR128(k0[3], kb, L_K + 3072); DSR128(k1[3], kb, L_K + 7168); DSR128(vx, vb, L_V);
    NOPS(1);
    MF32_0(s0, k0[0], X[0]); MF32_0(s1, k1[0], X[0]); MF32_C(s2, X[0], X[0], M2);
    asm volatile("s_waitcnt lgkmcnt(9)" : "+v"(xa[1]), "+v"(xb[1]), "+v"(k0[1]), "+v"(k1[1]) :: "memory");
    X[1] = cat4(xa[1], xb[1]); NOPS(2);
    MF32(s0, k0[1], X[1]); MF32(s1, k1[1], X[1]); MF32(s2, X[1], X[1]);
    asm volatile("s_waitcnt lgkmcnt(5)" : "+v"(xa[2]), "+v"(xb[2]), "+v"(k0[2]), "+v"(k1[2]) :: "memory");
    X[2] = cat4(xa[2], xb[2]); NOPS(2);
    MF32(s0, k0[2], X[2]); MF32(s1, k1[2], X[2]); MF32(s2, X[2], X[2]);
    asm volatile("s_waitcnt lgkmcnt(0)" : "+v"(xa[3]), "+v"(xb[3]), "+v"(k0[3]), "+v"(k1[3]), "+v"(vx) :: "memory");
    X[3] = cat4(xa[3], xb[3]); NOPS(2);
    MF32(s2, X[3], X[3]); MF32(s0, k0[3], X[3]); MF32(s1, k1[3], X[3]);
    fence_sched();
    v2u vm;
    {
      pin_v(av);
      vm = v2u{pk2(xsum32(av[0]), xsum32(av[1])), pk2(xsum32(av[2]), xsum32(av[3]))};
      pin_v(vm);
    }
    fence_sched();
    NOPS(2);
    pin_v(s0); pin_v(s1); pin_v(s2);
    {
      float m = max3f(max3f(s2[0], s2[1], s2[2]), max3f(s2[3], s0[0], s0[1]), max3f(s0[2], s0[3], s1[0]));
      m = max3f(m, max3f(s1[1], s1[2], s1[3]), m);
      m = xmax32(xmax16(m));
      const float mc = -m * CEXP;
      float p0[4], p1[4], p2[4];
#pragma unroll
      for (int r = 0; r < 4; ++r) p2[r] = __builtin_amdgcn_exp2f(fmaf(s2[r], CEXP, mc));
      v2u pm = {pk2(p2[0], p2[1]), pk2(p2[2], p2[3])};
      f32x4 o;
      pin_v(pm);
#pragma unroll
      for (int r = 0; r < 4; ++r) p0[r] = __builtin_amdgcn_exp2f(fmaf(s0[r], CEXP, mc));
      asm volatile("s_nop 1\n\tv_mfma_f32_16x16x16_bf16 %0, %1, %2, 0" : "=&v"(o) : "v"(vm), "v"(pm), "v"(p0[0]), "v"(p0[1]), "v"(p0[2]), "v"(p0[3]));
#pragma unroll
      for (int r = 0; r < 4; ++r) p1[r] = __builtin_amdgcn_exp2f(fmaf(s1[r], CEXP, mc));
      const v4u pb = {pk2(p0[0], p0[1]), pk2(p0[2], p0[3]), pk2(p1[0], p1[1]), pk2(p1[2], p1[3])};
      asm volatile("s_nop 1\n\tv_mfma_f32_16x16x32_bf16 %0, %1, %2, %0" : "+v"(o) : "v"(vx), "v"(pb));
      fence_sched();
      float l = ((p0[0] + p0[1]) + (p0[2] + p0[3])) + ((p1[0] + p1[1]) + (p1[2] + p1[3])) + ((p2[0] + p2[1]) + (p2[2] + p2[3]));
      l = xsum32(xsum16(l));
      float il = __builtin_amdgcn_rcpf(l);
      pin_v(il);
      fence_sched();
      pin_v(o);
      const unsigned d0 = noff(pk2(o[0] * il, o[1] * il)), d1 = noff(pk2(o[2] * il, o[3] * il));
      auto e0 = __builtin_amdgcn_permlane16_swap(d0, d0, false, false);
      auto e1 = __builtin_amdgcn_permlane16_swap(d1, d1, false, false);
      st16(v4u{e0[0], e1[0], e0[1], e1[1]}, ws, opub, bcur);
      fence_sched();
      if (rep && first) { const int c = (int)(ticks() - t0); cest = c > cest ? c : cest - ((s & 3) == 0); }
    }
    DSW64(lane * 8 + w * 512, vm, L_VM);
    if (s + 1 < NSEG) fetch_kqv(s + 1);
    bar();
    {
      const int t = w & 1, u = w >> 1;
      const unsigned qb = kb + t * 4096, ub = kb + u * 2048, mb = lane * 8 + u * 1024;
      v4u q[4], v0, v1; v2u vm0, vm1;
      DSR128(q[0], qb, L_Q); DSR128(q[1], qb, L_Q + 1024); DSR128(q[2], qb, L_Q + 2048); DSR128(q[3], qb, L_Q + 3072);
      DSR128(v0, ub, L_V); DSR128(v1, ub, L_V + 1024); DSR64(vm0, mb, L_VM); DSR64(vm1, mb, L_VM + 512);
      asm volatile("s_waitcnt lgkmcnt(0)" : "+v"(q[0]), "+v"(q[1]), "+v"(q[2]), "+v"(q[3]), "+v"(v0), "+v"(v1), "+v"(vm0), "+v"(vm1) :: "memory");
      MF32_0(s0, k0[0], q[0]); MF32_0(s1, k1[0], q[0]); MF32_0(s2, X[0], q[0]);
#pragma unroll
      for (int ks = 1; ks < 4; ++ks) { MF32(s0, k0[ks], q[ks]); MF32(s1, k1[ks], q[ks]); MF32(s2, X[ks], q[ks]); }
      NOPS(8);
      pin_v(s0); pin_v(s1); pin_v(s2);
      const float SCALE = 0.08838834764831845f, L2E = 1.4426950408889634f;
      float z[12];
      int lim = 16 * t + (int)c16 - 4 * (int)g;
      pin_v(lim);
#pragma unroll
      for (int r = 0; r < 4; ++r) {
        z[r] = r <= lim ? rbf(rbf(s0[r]) * SCALE) : NEG;
        z[4 + r] = r + 16 <= lim ? rbf(rbf(s1[r]) * SCALE) : NEG;
        z[8 + r] = rbf(rbf(s2[r]) * SCALE) + M2[0];
      }
      float m = max3f(max3f(z[0], z[1], z[2]), max3f(z[3], z[4], z[5]), max3f(z[6], z[7], z[8]));
      m = max3f(m, max3f(z[9], z[10], z[11]), m);
      m = xmax32(xmax16(m));
      float l = 0.f;
#pragma unroll
      for (int i = 0; i < 12; ++i) { z[i] = __builtin_amdgcn_exp2f((z[i] - m) * L2E); l += z[i]; }
      const float il = 1.0f / xsum32(xsum16(l));
      v4u pb = {pk2(z[0] * il, z[1] * il), pk2(z[2] * il, z[3] * il), pk2(z[4] * il, z[5] * il), pk2(z[6] * il, z[7] * il)};
      v2u pm = {pk2(z[8] * il, z[9] * il), pk2(z[10] * il, z[11] * il)};
      pin_v(pb); pin_v(pm);
      NOPS(2);
      f32x4 o0, o1;
      MF32_0(o0, v0, pb); MF32_0(o1, v1, pb); MF16(o0, vm0, pm); MF16(o1, vm1, pm);
      NOPS(8);
      pin_v(o0); pin_v(o1);
      st16(v4u{pk2(o0[0], o0[1]), pk2(o0[2], o0[3]), pk2(o1[0], o1[1]), pk2(o1[2], o1[3])}, ws, l16,
           W_OT + ((2 * s + t) * 128 + 4 * h + 2 * eta + u) * 1024u);
    }
    bprev = bcur; bcur = bcur == 2 * EX_BUF ? 0 : bcur + EX_BUF;
  }
  vmwait<0>();
  st4(NSEG, ws, oflag, 0);
}

DI void role_col(const Args& a, rsrc_t ws, unsigned cfg, int cg, int rho, unsigned lane, int w) {
  const unsigned g = lane >> 4, c16 = lane & 15;
  const unsigned mpub = exmap(EX_MEM + (2 * cg + rho) * 256 + (lane & 31) * 8, cfg);
  if (w == 0 && lane < 32) {
    const v2u v = *(const v2u*)(a.mem0 + (4 * rho + (lane >> 3)) * DM + 32 * cg + 4 * (lane & 7));
    st8(v2u{noff(v.x), noff(v.y)}, ws, mpub, 0);
    vmwait<0>();
  }
  bf16x16 W[8][2][2];
  {
    const rsrc_t ro = mkrsrc(a.wo, 4096 * DM * 2);
    const unsigned base = w * 40960u;
    unsigned src[2][2];
#pragma unroll
    for (int hf = 0; hf < 2; ++hf) { src[hf][0] = img_src<64, MAP_SP>(lane, 0, hf, 1280, cg * 64); src[hf][1] = img_src<64, MAP_SP>(lane, 1, hf, 1280, cg * 64); }
    bf16x8 t0, t1;
    stream_weights<32, 2, 16>(
        [&](auto i) {
          const unsigned so = (w * 1024u + (i.v >> 1) * 64u) * 1280u, d = base + (i.v % 16) * 2048u;
          dma(ro, src[i.v & 1][0], so, d); dma(ro, src[i.v & 1][1], so, d + 1024);
        },
        [&](auto i) {
          const unsigned d = base + (i.v % 16) * 2048u;
          const bf16x8 f0 = img_frag<64>(d, lane, 0), f1 = img_frag<64>(d, lane, 1);
          if constexpr ((i.v & 1) == 0) { t0 = f0; t1 = f1; }
          else { W[i.v >> 2][(i.v >> 1) & 1][0] = cat16(t0, f0); W[i.v >> 2][(i.v >> 1) & 1][1] = cat16(t1, f1); }
        });
  }
  sfor<0, 8>([&](auto i) { pin_a(W[i.v][0][0]); pin_a(W[i.v][0][1]); pin_a(W[i.v][1][0]); pin_a(W[i.v][1][1]); });
  bar();
  int idx = (lane & 4) ? 0xEEEE : 0x4444;
  pin_v(idx);
  const unsigned laneo = (4 * ((lane >> 3) & 1) + g) * 128 + ((lane >> 2) & 1) * 64 + (lane & 3) * 16;
  unsigned soff[8];
#pragma unroll
  for (int i = 0; i < 8; ++i) soff[i] = uni(exmap(EX_O + rho * 32768 + (8 * w + i) * 1024, cfg));
  const bool rep = (cg == 0 && rho == 0 && w == 0);
  const unsigned hintH = exmap(EX_HINT_H, cfg), hintC = (rep && lane == 0) ? exmap(EX_HINT_C, cfg) : DROP;
  const unsigned mpubm = lane < 32 ? mpub : DROP;
  const unsigned wa = ((w * 4 + (g & 1)) * 32 + 16 * (g >> 1) + c16) * 4, ra = (lane & 31) * 16;
  int cest = 80;
  unsigned bcur = 0, bnext = EX_BUF;
  for (int s = 0; s < NSEG - 1; ++s) {
    f32x4 c00, c01, c10, c11;
    unsigned t0 = 0; bool first = true;
    auto pass = [&]() -> bool {
      c00 = c01 = c10 = c11 = f32x4{0, 0, 0, 0};
      unsigned mx = 0;
      pin_v(c00); pin_v(c01); pin_v(c10); pin_v(c11); pin_v(mx);
      v4u ao[8];
#pragma unroll
      for (int i = 0; i < 8; ++i) ld16_sc1(ao[i], ws, laneo, soff[i] + bcur);
      if (rep) t0 = ticks();
      st8(v2u{(unsigned)s + 1, t0 + cest + DELTA}, ws, hintC, 0);
      sfor<0, 8>([&](auto i) { step4<8 - i.v, true>(c00, c10, c01, c11, mx, ao[i.v], W[i.v][0][0], W[i.v][1][0], W[i.v][0][1], W[i.v][1][1], idx); });
      NOPS(6);
      return __any(mx == 0xffffffffu);
    };
    wait_hint(ws, hintH, s + 1);
    if (__builtin_expect(pass(), 0)) {
      unsigned n = 0; const unsigned tw = ticks();
      first = false;
      do spin_guard(n, tw, ws); while (pass());
    }
    float y[4];
#pragma unroll
    for (int r = 0; r < 4; ++r) {
      auto p = __builtin_amdgcn_permlane32_swap(__float_as_uint(c10[r]), __float_as_uint(c01[r]), false, false);
      const float keep = lane < 32 ? c00[r] : c11[r], moved = lane < 32 ? __uint_as_float(p[1]) : __uint_as_float(p[0]);
      y[r] = keep + moved;
    }
    auto z0 = __builtin_amdgcn_permlane16_swap(__float_as_uint(y[0]), __float_as_uint(y[1]), false, false);
    auto z1 = __builtin_amdgcn_permlane16_swap(__float_as_uint(y[2]), __float_as_uint(y[3]), false, false);
    const unsigned red = (s & 1) * 2048u;
    const float r01 = __uint_as_float(z0[0]) + __uint_as_float(z0[1]), r23 = __uint_as_float(z1[0]) + __uint_as_float(z1[1]);
    DSW32(wa + red, r01, 0);
    DSW32(wa + red, r23, 256);
    bar();
    if (w == 0) {
      f32x4 u0, u1, u2, u3;
      const unsigned rr = ra + red;
      DSR128(u0, rr, 0); DSR128(u1, rr, 512); DSR128(u2, rr, 1024); DSR128(u3, rr, 1536);
      asm volatile("s_waitcnt lgkmcnt(0)" : "+v"(u0), "+v"(u1), "+v"(u2), "+v"(u3) :: "memory");
      const f32x4 v = (u0 + u1) + (u2 + u3);
      st8(v2u{noff(pk2(v[0], v[1])), noff(pk2(v[2], v[3]))}, ws, mpubm, bnext);
      fence_sched();
      st8(v2u{~0u, ~0u}, ws, mpubm, bcur);
      if (rep && first) { const int c = (int)(ticks() - t0); cest = c > cest ? c : cest - ((s & 3) == 0); }
    }
    bcur = bnext; bnext = bnext == 2 * EX_BUF ? 0 : bnext + EX_BUF;
  }
}

template <int MODE> DI void role_proj(const Args& a, rsrc_t ws, int p, unsigned lane, int w) {
  const int m = p >> 4, pair = p & 15, fc = p - 48, fks = 4 * fc + w;
  const bool live = MODE != 2 || fks < 54;
  bf16x8 W[20][4];
  {
    const unsigned base = w * 40960u;
    if constexpr (MODE == 2) {
      const rsrc_t rg = mkrsrc(a.wg, DM * FF * 2), ru = mkrsrc(a.wu, DM * FF * 2);
      const unsigned cb = (live ? fks : 0) * 64;
      const unsigned s0 = img_src<64, MAP_NAT>(lane, 0, 0, FF * 2, cb), s1 = img_src<64, MAP_NAT>(lane, 1, 0, FF * 2, cb);
      stream_weights<20, 4, 10>(
          [&](auto i) {
            const unsigned so = i.v * 32u * FF * 2u, d = base + (i.v % 10) * 4096u;
            dma(rg, s0, so, d); dma(rg, s1, so, d + 1024); dma(ru, s0, so, d + 2048); dma(ru, s1, so, d + 3072);
          },
          [&](auto i) {
            const unsigned d = base + (i.v % 10) * 4096u;
            W[i.v][0] = img_frag<64>(d, lane, 0); W[i.v][1] = img_frag<64>(d, lane, 1);
            W[i.v][2] = img_frag<64>(d + 2048, lane, 0); W[i.v][3] = img_frag<64>(d + 2048, lane, 1);
          });
    } else {
      const rsrc_t rw = mkrsrc(m == 0 ? a.wk : (m == 1 ? a.wq : a.wv), DM * 4096 * 2);
      unsigned src[4];
#pragma unroll
      for (int q = 0; q < 4; ++q) src[q] = img_src<128, MAP_NAT>(lane, q, 0, 8192, (pair * 256 + w * 64) * 2);
      stream_weights<20, 4, 10>(
          [&](auto i) {
            const unsigned so = i.v * 32u * 8192u, d = base + (i.v % 10) * 4096u;
#pragma unroll
            for (int q = 0; q < 4; ++q) dma(rw, src[q], so, d + q * 1024);
          },
          [&](auto i) {
            const unsigned d = base + (i.v % 10) * 4096u;
#pragma unroll
            for (int c = 0; c < 4; ++c) W[i.v][c] = img_frag<128>(d, lane, c);
          });
    }
  }
  sfor<0, 20>([&](auto k) { sfor<0, 4>([&](auto c) { if constexpr (k.v < 16) pin_a(W[k.v][c.v]); else pin_v(W[k.v][c.v]); }); });
  bar();
  constexpr int NSTORE = MODE == 2 ? 2 : 4;
  const unsigned l16 = lane * 16, flag = lane == 0 ? C_PF + p * 128 + w * 4 : DROP;
  auto fetch = [&](int s) {
    const unsigned so = W_XN + s * 40960u + w * 10240u, d = (s % 3) * 40960u + w * 10240u;
#pragma unroll
    for (int i = 0; i < 10; ++i) dma(ws, l16, so + i * 1024, d + i * 1024);
  };
  fetch(0); fetch(1);
  const int hh = 2 * pair + (w >> 1), eh = w & 1;
  for (int s = 0; s < NSEG; ++s) {
    if (s == 0) vmwait<10>(); else if (s + 1 < NSEG) vmwait<11 + NSTORE>(); else vmwait<1 + NSTORE>();
    bar();
    if (s + 2 < NSEG) fetch(s + 2);
    const unsigned buf = (s % 3) * 40960u + l16;
    f32x4 acc[2][4];
    v4u x0[3], x1[3];
    dsr128<0>(x0[0], buf); dsr128<20480>(x1[0], buf); dsr128<1024>(x0[1], buf); dsr128<21504>(x1[1], buf);
    sfor<0, 20>([&](auto k) {
      if constexpr (k.v + 2 < 20) { dsr128<(k.v + 2) * 1024>(x0[(k.v + 2) % 3], buf); dsr128<20480 + (k.v + 2) * 1024>(x1[(k.v + 2) % 3], buf); }
      lgkm2<(k.v + 2 < 20) ? 4 : (k.v + 1 < 20 ? 2 : 0)>(x0[k.v % 3], x1[k.v % 3]);
      sfor<0, 4>([&](auto c) {
        mfw<k.v == 0, MODE != 1, (k.v < 16)>(acc[0][c.v], W[k.v][c.v], x0[k.v % 3]);
        mfw<k.v == 0, MODE != 1, (k.v < 16)>(acc[1][c.v], W[k.v][c.v], x1[k.v % 3]);
      });
      if constexpr (k.v == 9) {
        if (s + 2 < NSEG) vmwait<10>(); else vmwait<0>();
        st4(s, ws, flag, 0);
      }
    });
    NOPS(8);
    sfor<0, 4>([&](auto c) { pin_v(acc[0][c.v]); pin_v(acc[1][c.v]); });
    if (MODE == 0) {
      const unsigned o = (m == 0 ? W_KX : W_QX) + (s * 32 + hh) * 8192u + eh * 2048u;
#pragma unroll
      for (int t = 0; t < 2; ++t)
#pragma unroll
        for (int b = 0; b < 2; ++b)
          st16(v4u{pk2(acc[t][2 * b][0], acc[t][2 * b][1]), pk2(acc[t][2 * b][2], acc[t][2 * b][3]),
                   pk2(acc[t][2 * b + 1][0], acc[t][2 * b + 1][1]), pk2(acc[t][2 * b + 1][2], acc[t][2 * b + 1][3])},
               ws, l16, o + t * 4096 + b * 1024);
    } else if (MODE == 1) {
      const unsigned o = W_VX + (s * 32 + hh) * 8192u + eh * 4096u;
#pragma unroll
      for (int c = 0; c < 4; ++c)
        st16(v4u{pk2(acc[0][c][0], acc[0][c][1]), pk2(acc[0][c][2], acc[0][c][3]), pk2(acc[1][c][0], acc[1][c][1]), pk2(acc[1][c][2], acc[1][c][3])},
             ws, l16, o + c * 1024);
    } else {
#pragma unroll
      for (int t = 0; t < 2; ++t) {
        float hv[8];
#pragma unroll
        for (int i = 0; i < 8; ++i) {
          const float gt = rbf(acc[t][i >> 2][i & 3]), up = rbf(acc[t][2 + (i >> 2)][i & 3]);
          hv[i] = (gt * (gt > 0.f ? 1.f : 0.f)) * up;
        }
        st16(v4u{pk2(hv[0], hv[1]), pk2(hv[2], hv[3]), pk2(hv[4], hv[5]), pk2(hv[6], hv[7])}, ws, live ? l16 : DROP,
             W_HD + ((2 * s + t) * 54 + fks) * 1024u);
      }
    }
  }
  vmwait<0>();
  st4(NSEG, ws, flag, 0);
}

DI void role_out(const Args& a, rsrc_t ws, int repl, int cg, unsigned lane, int w) {
  const unsigned g = lane >> 4, c16 = lane & 15;
  const bool last_on = w < 2;
  bf16x8 WO[32][2], WD[14][2];
  {
    const rsrc_t ro = mkrsrc(a.wo, 4096 * DM * 2), rd = mkrsrc(a.wd, FF * DM * 2);
    const unsigned base = w * 40960u;
    const unsigned s0 = img_src<64, MAP_PERM>(lane, 0, 0, 1280, cg * 64), s1 = img_src<64, MAP_PERM>(lane, 1, 0, 1280, cg * 64);
    stream_weights<46, 2, 16>(
        [&](auto i) {
          const unsigned d = base + (i.v % 16) * 2048u;
          if constexpr (i.v < 14) { const unsigned so = (4u * i.v + w) * 32u * 1280u; dma(rd, s0, so, d); dma(rd, s1, so, d + 1024); }
          else { const unsigned so = (w * 32u + (i.v - 14)) * 32u * 1280u; dma(ro, s0, so, d); dma(ro, s1, so, d + 1024); }
        },
        [&](auto i) {
          const unsigned d = base + (i.v % 16) * 2048u;
          if constexpr (i.v < 14) { WD[i.v][0] = img_frag<64>(d, lane, 0); WD[i.v][1] = img_frag<64>(d, lane, 1); }
          else { WO[i.v - 14][0] = img_frag<64>(d, lane, 0); WO[i.v - 14][1] = img_frag<64>(d, lane, 1); }
        });
  }
  sfor<0, 32>([&](auto i) { pin_a(WO[i.v][0]); pin_a(WO[i.v][1]); });
  sfor<0, 14>([&](auto i) { pin_v(WD[i.v][0]); pin_v(WD[i.v][1]); });
  bar();
  const unsigned l16 = lane * 16;
  const unsigned ffl = C_PF + (48 + min(lane, 13u)) * 128 + w * 4;
  const bool fon = lane < (last_on ? 14u : 13u);
  unsigned fseen = 0;
  const rsrc_t rx = mkrsrc(a.x, SEQ * DM * 2), rout = mkrsrc(a.out, SEQ * DM * 2);
  int par = 0;
  for (int u = repl; u < 2 * NSEG; u += 3, par ^= 1) {
    const unsigned s = u >> 1, t = u & 1;
    if (fseen < s + 1) fseen = wait_flags(ws, ffl, fon, s + 1);
    f32x4 cb[2], ca[2];
    {
      v4u f[14];
#pragma unroll
      for (int i = 0; i < 14; ++i) ld16_sc1(f[i], ws, l16, W_HD + (u * 54 + ((i == 13 && !last_on) ? w : 4 * i + w)) * 1024u);
      sfor<0, 14>([&](auto i) {
        landed<13 - i.v>(f[i.v]);
        mfw<i.v == 0, true, false>(cb[0], WD[i.v][0], f[i.v]); mfw<i.v == 0, true, false>(cb[1], WD[i.v][1], f[i.v]);
      });
    }
    wait_flags(ws, C_OF + (16 * w + (lane >> 1 & 15)) * 128 + (2 * (lane & 1) + t) * 4, lane < 32, s + 1);
    {
      const unsigned o = W_OT + (u * 128 + 32 * w) * 1024u;
      v4u f[16];
#pragma unroll
      for (int i = 0; i < 16; ++i) ld16_sc1(f[i], ws, l16, o + i * 1024);
      sfor<0, 32>([&](auto i) {
        constexpr int left = (31 - i.v) < 15 ? (31 - i.v) : 15;
        landed<left>(f[i.v & 15]);
        mfw<i.v == 0, true, true>(ca[0], WO[i.v][0], f[i.v & 15]); mfw<i.v == 0, true, true>(ca[1], WO[i.v][1], f[i.v & 15]);
        if constexpr (i.v + 16 < 32) ld16_sc1(f[i.v & 15], ws, l16, o + (i.v + 16) * 1024);
      });
    }
    NOPS(8);
    pin_v(ca[0]); pin_v(ca[1]); pin_v(cb[0]); pin_v(cb[1]);
    const unsigned red = par * 16384u;
    *(LAS f32x4*)(red + (w * 4 + 0) * 1024 + l16) = ca[0]; *(LAS f32x4*)(red + (w * 4 + 1) * 1024 + l16) = ca[1];
    *(LAS f32x4*)(red + (w * 4 + 2) * 1024 + l16) = cb[0]; *(LAS f32x4*)(red + (w * 4 + 3) * 1024 + l16) = cb[1];
    bar();
    if (w < 2) {
      const unsigned go = ((16 * u + c16) * DM + 32 * cg + 16 * w + 4 * g) * 2;
      const v2u xv = __builtin_amdgcn_raw_buffer_load_b64(rx, go, 0, 0);
      const unsigned ra = red + w * 1024 + l16;
      const f32x4 A = (*(LAS f32x4*)ra + *(LAS f32x4*)(ra + 4096)) + (*(LAS f32x4*)(ra + 8192) + *(LAS f32x4*)(ra + 12288));
      const f32x4 B = (*(LAS f32x4*)(ra + 2048) + *(LAS f32x4*)(ra + 6144)) + (*(LAS f32x4*)(ra + 10240) + *(LAS f32x4*)(ra + 14336));
      const float r0 = rbf(bflo(xv.x) + rbf(A[0])) + rbf(B[0]), r1 = rbf(bfhi(xv.x) + rbf(A[1])) + rbf(B[1]);
      const float r2 = rbf(bflo(xv.y) + rbf(A[2])) + rbf(B[2]), r3 = rbf(bfhi(xv.y) + rbf(A[3])) + rbf(B[3]);
      __builtin_amdgcn_raw_buffer_store_b64(v2u{pk2(r0, r1), pk2(r2, r3)}, rout, go, 0, 0);
    }
  }
}

__global__ void __launch_bounds__(256, 1) main_kernel(Args a) {
  extern __shared__ __attribute__((aligned(16))) unsigned char lds[];
  const unsigned lane = threadIdx.x & 63;
  const int w = (int)uni(threadIdx.x >> 6);
  const rsrc_t ws = mkrsrc(a.ws, W_END);
  if (threadIdx.x == 0) {
    unsigned* tick = (unsigned*)(a.ws + C_TICK);
    unsigned x = xcc_id(), t = atomicAdd(tick + x, 1u);
    atomicAdd((unsigned*)(a.ws + C_TOTAL), 1u);
    if (t >= 32) {
      unsigned rank = atomicAdd((unsigned*)(a.ws + C_OVF), 1u);
      const unsigned t0 = ticks();
      while (__hip_atomic_load((unsigned*)(a.ws + C_TOTAL), __ATOMIC_RELAXED, __HIP_MEMORY_SCOPE_AGENT) < 256u && ticks() - t0 < SPIN_TICKS) __builtin_amdgcn_s_sleep(4);
      x = 8;
      for (unsigned i = 0; i < 8; ++i) {
        const unsigned c = __hip_atomic_load(tick + i, __ATOMIC_RELAXED, __HIP_MEMORY_SCOPE_AGENT), fr = c < 32 ? 32 - c : 0;
        if (x == 8) { if (rank < fr) { x = i; t = c + rank; } else rank -= fr; }
      }
    }
    *(LAS unsigned*)0 = x * 32 + t;
    *(LAS unsigned*)4 = __hip_atomic_load((unsigned*)(a.ws + C_CFG), __ATOMIC_RELAXED, __HIP_MEMORY_SCOPE_AGENT);
  }
  bar();
  const unsigned role = uni(*(LAS unsigned*)0), cfg = uni(*(LAS unsigned*)4);
  bar();
  const int x = role >> 5, t = role & 31;
  if (x < 4) {
    if (t < 16) role_head(a, ws, cfg, 8 * x + (t >> 1), t & 1, lane, w);
    else if (t < 26) role_col(a, ws, cfg, (10 * x + t - 16) >> 1, t & 1, lane, w);
  } else if (x < 8) {
    const int y = x - 4;
    if (y < 3 && t < 20) role_out(a, ws, y, t, lane, w);
    else {
      const int p = y < 3 ? 12 * y + t - 20 : 36 + t;
      if (p < 32) role_proj<0>(a, ws, p, lane, w);
      else if (p < 48) role_proj<1>(a, ws, p, lane, w);
      else if (p < 62) role_proj<2>(a, ws, p, lane, w);
    }
  }
}

__global__ void __launch_bounds__(256) prep_kernel(Args a) {
  __shared__ float red[2][4][16];
  __shared__ unsigned lat[8];
  const unsigned lane = threadIdx.x & 63, w = threadIdx.x >> 6, b = blockIdx.x;
  const rsrc_t ws = mkrsrc(a.ws, W_END);
  if (b < 128) {
    const unsigned g = lane >> 4, c16 = lane & 15;
    const uint16_t* row = a.x + (size_t)(16 * b + c16) * DM + 8 * g;
    v4u v[5]; float f[40];
#pragma unroll
    for (int i = 0; i < 5; ++i) v[i] = *(const v4u*)(row + 32 * (5 * w + i));
    float sum = 0.f;
#pragma unroll
    for (int i = 0; i < 5; ++i)
#pragma unroll
      for (int j = 0; j < 4; ++j) { f[8 * i + 2 * j] = bflo(v[i][j]); f[8 * i + 2 * j + 1] = bfhi(v[i][j]); sum += f[8 * i + 2 * j] + f[8 * i + 2 * j + 1]; }
    sum += __shfl_xor(sum, 16); sum += __shfl_xor(sum, 32);
    if (lane < 16) red[0][w][lane] = sum;
    __syncthreads();
    const float mean = ((red[0][0][c16] + red[0][1][c16]) + (red[0][2][c16] + red[0][3][c16])) * (1.0f / DM);
    float sq = 0.f;
#pragma unroll
    for (int i = 0; i < 40; ++i) { const float d = f[i] - mean; sq += d * d; }
    sq += __shfl_xor(sq, 16); sq += __shfl_xor(sq, 32);
    if (lane < 16) red[1][w][lane] = sq;
    __syncthreads();
    const float var = ((red[1][0][c16] + red[1][1][c16]) + (red[1][2][c16] + red[1][3][c16])) * (1.0f / DM);
    const float mb = rbf(mean), den = rbf(sqrtf(rbf(rbf(var) + 1e-5f)));
#pragma unroll
    for (int i = 0; i < 5; ++i) {
      v4u o;
#pragma unroll
      for (int j = 0; j < 4; ++j) o[j] = pk2(rbf(f[8 * i + 2 * j] - mb) / den, rbf(f[8 * i + 2 * j + 1] - mb) / den);
      *(v4u*)(a.ws + W_XN + (size_t)(b * 20 + 5 * w + i) * 1024 + lane * 16) = o;
    }
  } else if (b < 248) {
    st16(v4u{~0u, ~0u, ~0u, ~0u}, ws, (b - 128) * 4096 + threadIdx.x * 16, 0);
  } else if (b < 255) {
    for (unsigned o = (b - 248) * 4096 + threadIdx.x * 16; o < CTL_BYTES; o += 7 * 4096)
      if (CTL + o != C_CFG) st16(v4u{0, 0, 0, 0}, ws, CTL + o, 0);
  } else {
    unsigned best[2] = {~0u, ~0u};
    for (int rep = 0; rep < 4; ++rep)
#pragma unroll
      for (int k = 0; k < 2; ++k) {
        const unsigned c0 = cycles();
        if (lane == 0) st4(0u, ws, PROBE + (2 * w + k) * 4096, 0);
        vmwait<0>();
        best[k] = min(best[k], cycles() - c0);
      }
    if (lane == 0) { lat[2 * w] = best[0]; lat[2 * w + 1] = best[1]; }
    __syncthreads();
    if (threadIdx.x == 0) {
      unsigned mask = 0;
      for (int i = 0; i < 8; ++i) { int less = 0; for (int j = 0; j < 8; ++j) less += (lat[j] < lat[i]) || (lat[j] == lat[i] && j < i); if (less < 4) mask |= 1u << i; }
      if (xcc_id() >= 4) mask = ~mask & 255u;
      unsigned cfg = 0, n = 0;
      for (unsigned i = 0; i < 8; ++i) if (mask >> i & 1) cfg |= i << (4 * n++);
      st16(v4u{cfg, 0, 0, 0}, ws, C_CFG, 0);
    }
  }
}

extern "C" void kernel_launch(void* const* d_in, const int* in_sizes, int n_in, void* d_out, int out_size, void* d_ws, size_t ws_size,
                              hipStream_t stream) {
  Args a;
  a.x = (const uint16_t*)d_in[0]; a.mem0 = (const uint16_t*)d_in[1]; a.wq = (const uint16_t*)d_in[2]; a.wk = (const uint16_t*)d_in[3];
  a.wv = (const uint16_t*)d_in[4]; a.wo = (const uint16_t*)d_in[5]; a.wg = (const uint16_t*)d_in[8]; a.wu = (const uint16_t*)d_in[9];
  a.wd = (const uint16_t*)d_in[10]; a.out = (uint16_t*)d_out;
  a.ws = (unsigned char*)(((uintptr_t)d_ws + (2u << 20) - 1) & ~(uintptr_t)((2u << 20) - 1));
  (void)hipFuncSetAttribute((const void*)main_kernel, hipFuncAttributeMaxDynamicSharedMemorySize, LDS_BYTES);
  prep_kernel<<<256, 256, 0, stream>>>(a);
  main_kernel<<<256, 256, LDS_BYTES, stream>>>(a);
}
```

```cpp
#define DLY_FFN 700
#define DLY_OUT 800
#define DH 12
#define DC 7
#define HA 38.f
#define CA 33.f
#define FIRST 5
#define LEAD 56
#include <hip/hip_runtime.h>
#include <stdint.h>

#define DI __device__ __forceinline__
typedef unsigned v2u __attribute__((ext_vector_type(2)));
typedef unsigned v4u __attribute__((ext_vector_type(4)));
typedef unsigned v16u __attribute__((ext_vector_type(16)));
typedef unsigned v32u __attribute__((ext_vector_type(32)));
typedef float f32x2 __attribute__((ext_vector_type(2)));
typedef __bf16 bf16x2 __attribute__((ext_vector_type(2)));
typedef __amdgpu_buffer_rsrc_t rsrc_t;

constexpr int SEQ = 2048, DM = 640, FF = 1728;

constexpr unsigned EX_BUF = 163840;
constexpr unsigned EX_O = 32768;
constexpr unsigned CTL = 1048576;
constexpr unsigned C_EPOCH = CTL, C_ABORT = CTL + 256, C_START = CTL + 32768;
constexpr unsigned C_LN = CTL + 2048, C_RDY = CTL + 12288, C_IN = CTL + 3072;
constexpr unsigned C_PF = CTL + 4096;
constexpr unsigned C_CD = CTL + 49152;
constexpr unsigned C_MH = C_CD + 4096, C_MC = C_MH + 2048;
constexpr unsigned C_OF = CTL + 16384;
constexpr unsigned C_M0 = CTL + 65536;
constexpr unsigned PROBE = CTL + 98304;
constexpr unsigned W_XN = 2u << 20;
constexpr unsigned W_HB = 6u << 20;
constexpr unsigned H_OT = 64 * 24576;
constexpr unsigned W_HD = W_HB + (64u << 20);
constexpr unsigned W_DBG = W_HD + (8u << 20);
constexpr unsigned W_END = W_DBG + (4u << 20);
constexpr unsigned DROP = 0xf0000000u;
constexpr int LDS_BYTES = 160 * 1024;
constexpr unsigned SPIN_TICKS = 3000000;
#define DG(...)
#define DLY_COL 100
#define CH 0
#define CC 0
#define CODEPF 1
#define PFWO 1
#define PFQ 0
#define BUMP 2
#define HB 1900.f
#define CB 1120.f
#define LEADM 64

struct Args {
  const uint16_t* x; const uint16_t* mem0; const uint16_t* wq; const uint16_t* wk; const uint16_t* wv; const uint16_t* wo;
  const uint16_t* wg; const uint16_t* wu; const uint16_t* wd; uint16_t* out; unsigned char* ws;
};

DI unsigned ticks() { unsigned long long t; asm volatile("s_memrealtime %0\n\ts_waitcnt lgkmcnt(0)" : "=s"(t) :: "memory"); return (unsigned)t; }
DI unsigned cycles() { unsigned long long t; asm volatile("s_memtime %0\n\ts_waitcnt lgkmcnt(0)" : "=s"(t) :: "memory"); return (unsigned)t; }
DI unsigned uni(unsigned v) { return __builtin_amdgcn_readfirstlane(v); }
DI void vm0() { asm volatile("s_waitcnt vmcnt(0)" ::: "memory"); }
DI unsigned pk2(float a, float b) { f32x2 v = {a, b}; return __builtin_bit_cast(unsigned, __builtin_convertvector(v, bf16x2)); }
DI float bflo(unsigned u) { return __uint_as_float(u << 16); }
DI float bfhi(unsigned u) { return __uint_as_float(u & 0xffff0000u); }
DI float rbf(float a) { return bflo(pk2(a, a)); }
DI unsigned ld4(rsrc_t r, unsigned voff) {
  unsigned d; asm volatile("buffer_load_dword %0, %1, %2, 0 offen sc1\n\ts_waitcnt vmcnt(0)" : "=&v"(d) : "v"(voff), "s"(r) : "memory"); return d;
}
DI v2u ld8(rsrc_t r, unsigned voff) {
  v2u d; asm volatile("buffer_load_dwordx2 %0, %1, %2, 0 offen sc1\n\ts_waitcnt vmcnt(0)" : "=&v"(d) : "v"(voff), "s"(r) : "memory"); return d;
}
DI v4u ld16(rsrc_t r, unsigned voff) {
  v4u d; asm volatile("buffer_load_dwordx4 %0, %1, %2, 0 offen sc1\n\ts_waitcnt vmcnt(0)" : "=&v"(d) : "v"(voff), "s"(r) : "memory"); return d;
}
DI void st4(unsigned v, rsrc_t r, unsigned voff) { asm volatile("buffer_store_dword %0, %1, %2, 0 offen sc1" :: "v"(v), "v"(voff), "s"(r) : "memory"); }
DI void st8(v2u v, rsrc_t r, unsigned voff) { asm volatile("buffer_store_dwordx2 %0, %1, %2, 0 offen sc1" :: "v"(v), "v"(voff), "s"(r) : "memory"); }
DI void st16(v4u v, rsrc_t r, unsigned voff) { asm volatile("buffer_store_dwordx4 %0, %1, %2, 0 offen sc1\n\ts_nop 1" :: "v"(v), "v"(voff), "s"(r) : "memory"); }
DI void until(unsigned t) { while ((int)(t - ticks()) > 0) __builtin_amdgcn_s_sleep(4); }
DI void spin_guard(unsigned& n, unsigned t0, rsrc_t ws, unsigned e) {
  if ((++n & 63u) != 0u) return;
  if (uni(ld4(ws, C_ABORT)) == e) asm volatile("s_endpgm");
  if (ticks() - t0 > SPIN_TICKS) { st4(e, ws, C_ABORT); asm volatile("s_endpgm"); }
}

enum { MAP_NAT = 0, MAP_SP = 1, MAP_PERM = 2 };
template <int MAP> DI unsigned rowmap(unsigned k) {
  if (MAP == MAP_SP) return (k & 48u) + 8u * ((k >> 1) & 1u) + 2u * ((k >> 2) & 3u) + (k & 1u);
  if (MAP == MAP_PERM) return 16u * ((k >> 2) & 1u) + 4u * (k >> 3) + (k & 3u);
  return k;
}
template <int ROWB> DI unsigned img_off(unsigned row, unsigned ch) {
  if (ROWB == 128) return 128u * row + 32u * ((ch >> 1) ^ (((row >> 1) & 1u) | (((row >> 3) & 1u) << 1))) + 16u * (ch & 1u);
  if (ROWB == 64) return 64u * row + 32u * ((ch >> 1) ^ ((row >> 3) & 1u)) + 16u * (ch & 1u);
  return 32u * (row ^ (((row >> 3) & 1u) << 2)) + 16u * ch;
}
template <int ROWB, int MAP> DI unsigned img_src(unsigned lane, unsigned p, unsigned half, unsigned stride, unsigned colbyte) {
  constexpr unsigned CPR = ROWB / 16;
  unsigned pos = p * 64u + lane, prow = pos / CPR, slot = pos % CPR, row, ch;
  if (ROWB == 128) { row = prow; ch = (((slot >> 1) ^ (((row >> 1) & 1u) | (((row >> 3) & 1u) << 1))) << 1) | (slot & 1u); }
  else if (ROWB == 64) { row = prow; ch = (((slot >> 1) ^ ((row >> 3) & 1u)) << 1) | (slot & 1u); }
  else { row = prow ^ (((prow >> 3) & 1u) << 2); ch = slot; }
  return rowmap<MAP>(row + 32u * half) * stride + colbyte + 16u * ch;
}
template <int ROWB> DI unsigned tr_addr(unsigned lane, unsigned c, unsigned r) {
  const unsigned g = lane >> 4, q = (lane & 15) >> 2, p = lane & 3;
  return img_off<ROWB>(8 * g + 4 * r + q, 2 * c + (p >> 1)) + 8 * (p & 1);
}

#define HEAD_ASM \
    "s_nop 4\n" \
    "s_memtime s[92:93]\n" \
    "s_memrealtime s[94:95]\n" \
    "v_mov_b32 v48, -1\n" \
    "v_mov_b32 v49, -1\n" \
    "v_xor_b32 v50, 16384, v18\n" \
    "s_mov_b32 s68, 0\n" \
    "buffer_store_dwordx2 v[48:49], v18, s[36:39], s68 offen sc1\n" \
    "s_mov_b32 s68, 8192\n" \
    "buffer_store_dwordx2 v[48:49], v18, s[36:39], s68 offen sc1\n" \
    "s_mov_b32 s68, 0\n" \
    "buffer_store_dwordx2 v[48:49], v50, s[36:39], s68 offen sc1\n" \
    "s_mov_b32 s68, 8192\n" \
    "buffer_store_dwordx2 v[48:49], v50, s[36:39], s68 offen sc1\n" \
    "s_mov_b32 s68, 163840\n" \
    "buffer_store_dwordx2 v[48:49], v18, s[36:39], s68 offen sc1\n" \
    "s_mov_b32 s68, 172032\n" \
    "buffer_store_dwordx2 v[48:49], v18, s[36:39], s68 offen sc1\n" \
    "s_mov_b32 s68, 163840\n" \
    "buffer_store_dwordx2 v[48:49], v50, s[36:39], s68 offen sc1\n" \
    "s_mov_b32 s68, 172032\n" \
    "buffer_store_dwordx2 v[48:49], v50, s[36:39], s68 offen sc1\n" \
    "s_mov_b32 s68, 327680\n" \
    "buffer_store_dwordx2 v[48:49], v18, s[36:39], s68 offen sc1\n" \
    "s_mov_b32 s68, 335872\n" \
    "buffer_store_dwordx2 v[48:49], v18, s[36:39], s68 offen sc1\n" \
    "s_mov_b32 s68, 327680\n" \
    "buffer_store_dwordx2 v[48:49], v50, s[36:39], s68 offen sc1\n" \
    "s_mov_b32 s68, 335872\n" \
    "buffer_store_dwordx2 v[48:49], v50, s[36:39], s68 offen sc1\n" \
    "s_xor_b32 s68, s61, 0\n" \
    "s_add_u32 m0, s52, 0\n" \
    "s_nop 0\n" \
    "buffer_load_dwordx4 v0, s[40:43], s68 offen lds\n" \
    "s_add_u32 m0, s52, 1024\n" \
    "s_nop 0\n" \
    "buffer_load_dwordx4 v1, s[40:43], s68 offen lds\n" \
    "s_add_u32 m0, s52, 2048\n" \
    "s_nop 0\n" \
    "buffer_load_dwordx4 v2, s[40:43], s68 offen lds\n" \
    "s_add_u32 m0, s52, 3072\n" \
    "s_nop 0\n" \
    "buffer_load_dwordx4 v3, s[40:43], s68 offen lds\n" \
    "s_add_u32 m0, s52, 4096\n" \
    "s_nop 0\n" \
    "buffer_load_dwordx4 v4, s[48:51], s68 offen lds\n" \
    "s_xor_b32 s68, s61, 262144\n" \
    "s_add_u32 m0, s52, 5120\n" \
    "s_nop 0\n" \
    "buffer_load_dwordx4 v0, s[40:43], s68 offen lds\n" \
    "s_add_u32 m0, s52, 6144\n" \
    "s_nop 0\n" \
    "buffer_load_dwordx4 v1, s[40:43], s68 offen lds\n" \
    "s_add_u32 m0, s52, 7168\n" \
    "s_nop 0\n" \
    "buffer_load_dwordx4 v2, s[40:43], s68 offen lds\n" \
    "s_add_u32 m0, s52, 8192\n" \
    "s_nop 0\n" \
    "buffer_load_dwordx4 v3, s[40:43], s68 offen lds\n" \
    "s_add_u32 m0, s52, 9216\n" \
    "s_nop 0\n" \
    "buffer_load_dwordx4 v4, s[48:51], s68 offen lds\n" \
    "s_xor_b32 s68, s61, 524288\n" \
    "s_add_u32 m0, s52, 10240\n" \
    "s_nop 0\n" \
    "buffer_load_dwordx4 v0, s[40:43], s68 offen lds\n" \
    "s_add_u32 m0, s52, 11264\n" \
    "s_nop 0\n" \
    "buffer_load_dwordx4 v1, s[40:43], s68 offen lds\n" \
    "s_add_u32 m0, s52, 12288\n" \
    "s_nop 0\n" \
    "buffer_load_dwordx4 v2, s[40:43], s68 offen lds\n" \
    "s_add_u32 m0, s52, 13312\n" \
    "s_nop 0\n" \
    "buffer_load_dwordx4 v3, s[40:43], s68 offen lds\n" \
    "s_add_u32 m0, s52, 14336\n" \
    "s_nop 0\n" \
    "buffer_load_dwordx4 v4, s[48:51], s68 offen lds\n" \
    "s_xor_b32 s68, s61, 786432\n" \
    "s_add_u32 m0, s52, 15360\n" \
    "s_nop 0\n" \
    "buffer_load_dwordx4 v0, s[40:43], s68 offen lds\n" \
    "s_add_u32 m0, s52, 16384\n" \
    "s_nop 0\n" \
    "buffer_load_dwordx4 v1, s[40:43], s68 offen lds\n" \
    "s_add_u32 m0, s52, 17408\n" \
    "s_nop 0\n" \
    "buffer_load_dwordx4 v2, s[40:43], s68 offen lds\n" \
    "s_add_u32 m0, s52, 18432\n" \
    "s_nop 0\n" \
    "buffer_load_dwordx4 v3, s[40:43], s68 offen lds\n" \
    "s_add_u32 m0, s52, 19456\n" \
    "s_nop 0\n" \
    "buffer_load_dwordx4 v4, s[48:51], s68 offen lds\n" \
    "s_xor_b32 s68, s61, 1048576\n" \
    "s_add_u32 m0, s52, 20480\n" \
    "s_nop 0\n" \
    "buffer_load_dwordx4 v0, s[40:43], s68 offen lds\n" \
    "s_add_u32 m0, s52, 21504\n" \
    "s_nop 0\n" \
    "buffer_load_dwordx4 v1, s[40:43], s68 offen lds\n" \
    "s_add_u32 m0, s52, 22528\n" \
    "s_nop 0\n" \
    "buffer_load_dwordx4 v2, s[40:43], s68 offen lds\n" \
    "s_add_u32 m0, s52, 23552\n" \
    "s_nop 0\n" \
    "buffer_load_dwordx4 v3, s[40:43], s68 offen lds\n" \
    "s_add_u32 m0, s52, 24576\n" \
    "s_nop 0\n" \
    "buffer_load_dwordx4 v4, s[48:51], s68 offen lds\n" \
    "s_xor_b32 s68, s61, 1310720\n" \
    "s_add_u32 m0, s52, 25600\n" \
    "s_nop 0\n" \
    "buffer_load_dwordx4 v0, s[40:43], s68 offen lds\n" \
    "s_add_u32 m0, s52, 26624\n" \
    "s_nop 0\n" \
    "buffer_load_dwordx4 v1, s[40:43], s68 offen lds\n" \
    "s_add_u32 m0, s52, 27648\n" \
    "s_nop 0\n" \
    "buffer_load_dwordx4 v2, s[40:43], s68 offen lds\n" \
    "s_add_u32 m0, s52, 28672\n" \
    "s_nop 0\n" \
    "buffer_load_dwordx4 v3, s[40:43], s68 offen lds\n" \
    "s_add_u32 m0, s52, 29696\n" \
    "s_nop 0\n" \
    "buffer_load_dwordx4 v4, s[48:51], s68 offen lds\n" \
    "s_xor_b32 s68, s61, 1572864\n" \
    "s_add_u32 m0, s52, 30720\n" \
    "s_nop 0\n" \
    "buffer_load_dwordx4 v0, s[40:43], s68 offen lds\n" \
    "s_add_u32 m0, s52, 31744\n" \
    "s_nop 0\n" \
    "buffer_load_dwordx4 v1, s[40:43], s68 offen lds\n" \
    "s_add_u32 m0, s52, 32768\n" \
    "s_nop 0\n" \
    "buffer_load_dwordx4 v2, s[40:43], s68 offen lds\n" \
    "s_add_u32 m0, s52, 33792\n" \
    "s_nop 0\n" \
    "buffer_load_dwordx4 v3, s[40:43], s68 offen lds\n" \
    "s_add_u32 m0, s52, 34816\n" \
    "s_nop 0\n" \
    "buffer_load_dwordx4 v4, s[48:51], s68 offen lds\n" \
    "s_xor_b32 s68, s61, 1835008\n" \
    "s_add_u32 m0, s52, 35840\n" \
    "s_nop 0\n" \
    "buffer_load_dwordx4 v0, s[40:43], s68 offen lds\n" \
    "s_add_u32 m0, s52, 36864\n" \
    "s_nop 0\n" \
    "buffer_load_dwordx4 v1, s[40:43], s68 offen lds\n" \
    "s_add_u32 m0, s52, 37888\n" \
    "s_nop 0\n" \
    "buffer_load_dwordx4 v2, s[40:43], s68 offen lds\n" \
    "s_add_u32 m0, s52, 38912\n" \
    "s_nop 0\n" \
    "buffer_load_dwordx4 v3, s[40:43], s68 offen lds\n" \
    "s_add_u32 m0, s52, 39936\n" \
    "s_nop 0\n" \
    "buffer_load_dwordx4 v4, s[48:51], s68 offen lds\n" \
    "s_waitcnt vmcnt(35)\n" \
    "ds_read_b64_tr_b16 a[0:1], v5 offset:0\n" \
    "ds_read_b64_tr_b16 a[2:3], v6 offset:0\n" \
    "ds_read_b64_tr_b16 a[8:9], v7 offset:0\n" \
    "ds_read_b64_tr_b16 a[10:11], v8 offset:0\n" \
    "ds_read_b64_tr_b16 a[16:17], v9 offset:0\n" \
    "ds_read_b64_tr_b16 a[18:19], v10 offset:0\n" \
    "ds_read_b64_tr_b16 a[240:241], v11 offset:0\n" \
    "ds_read_b64_tr_b16 a[242:243], v12 offset:0\n" \
    "ds_read_b64_tr_b16 v[108:109], v13 offset:4096\n" \
    "ds_read_b64_tr_b16 v[110:111], v14 offset:4096\n" \
    "s_waitcnt lgkmcnt(0)\n" \
    "s_xor_b32 s68, s61, 2097152\n" \
    "s_add_u32 m0, s52, 0\n" \
    "s_nop 0\n" \
    "buffer_load_dwordx4 v0, s[40:43], s68 offen lds\n" \
    "s_add_u32 m0, s52, 1024\n" \
    "s_nop 0\n" \
    "buffer_load_dwordx4 v1, s[40:43], s68 offen lds\n" \
    "s_add_u32 m0, s52, 2048\n" \
    "s_nop 0\n" \
    "buffer_load_dwordx4 v2, s[40:43], s68 offen lds\n" \
    "s_add_u32 m0, s52, 3072\n" \
    "s_nop 0\n" \
    "buffer_load_dwordx4 v3, s[40:43], s68 offen lds\n" \
    "s_add_u32 m0, s52, 4096\n" \
    "s_nop 0\n" \
    "buffer_load_dwordx4 v4, s[48:51], s68 offen lds\n" \
    "s_waitcnt vmcnt(35)\n" \
    "ds_read_b64_tr_b16 a[4:5], v5 offset:5120\n" \
    "ds_read_b64_tr_b16 a[6:7], v6 offset:5120\n" \
    "ds_read_b64_tr_b16 a[12:13], v7 offset:5120\n" \
    "ds_read_b64_tr_b16 a[14:15], v8 offset:5120\n" \
    "ds_read_b64_tr_b16 a[20:21], v9 offset:5120\n" \
    "ds_read_b64_tr_b16 a[22:23], v10 offset:5120\n" \
    "ds_read_b64_tr_b16 a[244:245], v11 offset:5120\n" \
    "ds_read_b64_tr_b16 a[246:247], v12 offset:5120\n" \
    "ds_read_b64_tr_b16 v[112:113], v13 offset:9216\n" \
    "ds_read_b64_tr_b16 v[114:115], v14 offset:9216\n" \
    "s_waitcnt lgkmcnt(0)\n" \
    "s_xor_b32 s68, s61, 2359296\n" \
    "s_add_u32 m0, s52, 5120\n" \
    "s_nop 0\n" \
    "buffer_load_dwordx4 v0, s[40:43], s68 offen lds\n" \
    "s_add_u32 m0, s52, 6144\n" \
    "s_nop 0\n" \
    "buffer_load_dwordx4 v1, s[40:43], s68 offen lds\n" \
    "s_add_u32 m0, s52, 7168\n" \
    "s_nop 0\n" \
    "buffer_load_dwordx4 v2, s[40:43], s68 offen lds\n" \
    "s_add_u32 m0, s52, 8192\n" \
    "s_nop 0\n" \
    "buffer_load_dwordx4 v3, s[40:43], s68 offen lds\n" \
    "s_add_u32 m0, s52, 9216\n" \
    "s_nop 0\n" \
    "buffer_load_dwordx4 v4, s[48:51], s68 offen lds\n" \
    "s_waitcnt vmcnt(35)\n" \
    "ds_read_b64_tr_b16 a[24:25], v5 offset:10240\n" \
    "ds_read_b64_tr_b16 a[26:27], v6 offset:10240\n" \
    "ds_read_b64_tr_b16 a[32:33], v7 offset:10240\n" \
    "ds_read_b64_tr_b16 a[34:35], v8 offset:10240\n" \
    "ds_read_b64_tr_b16 a[40:41], v9 offset:10240\n" \
    "ds_read_b64_tr_b16 a[42:43], v10 offset:10240\n" \
    "ds_read_b64_tr_b16 a[248:249], v11 offset:10240\n" \
    "ds_read_b64_tr_b16 a[250:251], v12 offset:10240\n" \
    "ds_read_b64_tr_b16 v[116:117], v13 offset:14336\n" \
    "ds_read_b64_tr_b16 v[118:119], v14 offset:14336\n" \
    "s_waitcnt lgkmcnt(0)\n" \
    "s_xor_b32 s68, s61, 2621440\n" \
    "s_add_u32 m0, s52, 10240\n" \
    "s_nop 0\n" \
    "buffer_load_dwordx4 v0, s[40:43], s68 offen lds\n" \
    "s_add_u32 m0, s52, 11264\n" \
    "s_nop 0\n" \
    "buffer_load_dwordx4 v1, s[40:43], s68 offen lds\n" \
    "s_add_u32 m0, s52, 12288\n" \
    "s_nop 0\n" \
    "buffer_load_dwordx4 v2, s[40:43], s68 offen lds\n" \
    "s_add_u32 m0, s52, 13312\n" \
    "s_nop 0\n" \
    "buffer_load_dwordx4 v3, s[40:43], s68 offen lds\n" \
    "s_add_u32 m0, s52, 14336\n" \
    "s_nop 0\n" \
    "buffer_load_dwordx4 v4, s[48:51], s68 offen lds\n" \
    "s_waitcnt vmcnt(35)\n" \
    "ds_read_b64_tr_b16 a[28:29], v5 offset:15360\n" \
    "ds_read_b64_tr_b16 a[30:31], v6 offset:15360\n" \
    "ds_read_b64_tr_b16 a[36:37], v7 offset:15360\n" \
    "ds_read_b64_tr_b16 a[38:39], v8 offset:15360\n" \
    "ds_read_b64_tr_b16 a[44:45], v9 offset:15360\n" \
    "ds_read_b64_tr_b16 a[46:47], v10 offset:15360\n" \
    "ds_read_b64_tr_b16 a[252:253], v11 offset:15360\n" \
    "ds_read_b64_tr_b16 a[254:255], v12 offset:15360\n" \
    "ds_read_b64_tr_b16 v[120:121], v13 offset:19456\n" \
    "ds_read_b64_tr_b16 v[122:123], v14 offset:19456\n" \
    "s_waitcnt lgkmcnt(0)\n" \
    "s_xor_b32 s68, s61, 2883584\n" \
    "s_add_u32 m0, s52, 15360\n" \
    "s_nop 0\n" \
    "buffer_load_dwordx4 v0, s[40:43], s68 offen lds\n" \
    "s_add_u32 m0, s52, 16384\n" \
    "s_nop 0\n" \
    "buffer_load_dwordx4 v1, s[40:43], s68 offen lds\n" \
    "s_add_u32 m0, s52, 17408\n" \
    "s_nop 0\n" \
    "buffer_load_dwordx4 v2, s[40:43], s68 offen lds\n" \
    "s_add_u32 m0, s52, 18432\n" \
    "s_nop 0\n" \
    "buffer_load_dwordx4 v3, s[40:43], s68 offen lds\n" \
    "s_add_u32 m0, s52, 19456\n" \
    "s_nop 0\n" \
    "buffer_load_dwordx4 v4, s[48:51], s68 offen lds\n" \
    "s_waitcnt vmcnt(35)\n" \
    "ds_read_b64_tr_b16 a[48:49], v5 offset:20480\n" \
    "ds_read_b64_tr_b16 a[50:51], v6 offset:20480\n" \
    "ds_read_b64_tr_b16 a[56:57], v7 offset:20480\n" \
    "ds_read_b64_tr_b16 a[58:59], v8 offset:20480\n" \
    "ds_read_b64_tr_b16 a[64:65], v9 offset:20480\n" \
    "ds_read_b64_tr_b16 a[66:67], v10 offset:20480\n" \
    "ds_read_b64_tr_b16 v[144:145], v11 offset:20480\n" \
    "ds_read_b64_tr_b16 v[146:147], v12 offset:20480\n" \
    "ds_read_b64_tr_b16 v[124:125], v13 offset:24576\n" \
    "ds_read_b64_tr_b16 v[126:127], v14 offset:24576\n" \
    "s_waitcnt lgkmcnt(0)\n" \
    "s_xor_b32 s68, s61, 3145728\n" \
    "s_add_u32 m0, s52, 20480\n" \
    "s_nop 0\n" \
    "buffer_load_dwordx4 v0, s[40:43], s68 offen lds\n" \
    "s_add_u32 m0, s52, 21504\n" \
    "s_nop 0\n" \
    "buffer_load_dwordx4 v1, s[40:43], s68 offen lds\n" \
    "s_add_u32 m0, s52, 22528\n" \
    "s_nop 0\n" \
    "buffer_load_dwordx4 v2, s[40:43], s68 offen lds\n" \
    "s_add_u32 m0, s52, 23552\n" \
    "s_nop 0\n" \
    "buffer_load_dwordx4 v3, s[40:43], s68 offen lds\n" \
    "s_add_u32 m0, s52, 24576\n" \
    "s_nop 0\n" \
    "buffer_load_dwordx4 v4, s[48:51], s68 offen lds\n" \
    "s_waitcnt vmcnt(35)\n" \
    "ds_read_b64_tr_b16 a[52:53], v5 offset:25600\n" \
    "ds_read_b64_tr_b16 a[54:55], v6 offset:25600\n" \
    "ds_read_b64_tr_b16 a[60:61], v7 offset:25600\n" \
    "ds_read_b64_tr_b16 a[62:63], v8 offset:25600\n" \
    "ds_read_b64_tr_b16 a[68:69], v9 offset:25600\n" \
    "ds_read_b64_tr_b16 a[70:71], v10 offset:25600\n" \
    "ds_read_b64_tr_b16 v[148:149], v11 offset:25600\n" \
    "ds_read_b64_tr_b16 v[150:151], v12 offset:25600\n" \
    "ds_read_b64_tr_b16 v[128:129], v13 offset:29696\n" \
    "ds_read_b64_tr_b16 v[130:131], v14 offset:29696\n" \
    "s_waitcnt lgkmcnt(0)\n" \
    "s_xor_b32 s68, s61, 3407872\n" \
    "s_add_u32 m0, s52, 25600\n" \
    "s_nop 0\n" \
    "buffer_load_dwordx4 v0, s[40:43], s68 offen lds\n" \
    "s_add_u32 m0, s52, 26624\n" \
    "s_nop 0\n" \
    "buffer_load_dwordx4 v1, s[40:43], s68 offen lds\n" \
    "s_add_u32 m0, s52, 27648\n" \
    "s_nop 0\n" \
    "buffer_load_dwordx4 v2, s[40:43], s68 offen lds\n" \
    "s_add_u32 m0, s52, 28672\n" \
    "s_nop 0\n" \
    "buffer_load_dwordx4 v3, s[40:43], s68 offen lds\n" \
    "s_add_u32 m0, s52, 29696\n" \
    "s_nop 0\n" \
    "buffer_load_dwordx4 v4, s[48:51], s68 offen lds\n" \
    "s_waitcnt vmcnt(35)\n" \
    "ds_read_b64_tr_b16 a[72:73], v5 offset:30720\n" \
    "ds_read_b64_tr_b16 a[74:75], v6 offset:30720\n" \
    "ds_read_b64_tr_b16 a[80:81], v7 offset:30720\n" \
    "ds_read_b64_tr_b16 a[82:83], v8 offset:30720\n" \
    "ds_read_b64_tr_b16 a[88:89], v9 offset:30720\n" \
    "ds_read_b64_tr_b16 a[90:91], v10 offset:30720\n" \
    "ds_read_b64_tr_b16 v[152:153], v11 offset:30720\n" \
    "ds_read_b64_tr_b16 v[154:155], v12 offset:30720\n" \
    "ds_read_b64_tr_b16 v[132:133], v13 offset:34816\n" \
    "ds_read_b64_tr_b16 v[134:135], v14 offset:34816\n" \
    "s_waitcnt lgkmcnt(0)\n" \
    "s_xor_b32 s68, s61, 3670016\n" \
    "s_add_u32 m0, s52, 30720\n" \
    "s_nop 0\n" \
    "buffer_load_dwordx4 v0, s[40:43], s68 offen lds\n" \
    "s_add_u32 m0, s52, 31744\n" \
    "s_nop 0\n" \
    "buffer_load_dwordx4 v1, s[40:43], s68 offen lds\n" \
    "s_add_u32 m0, s52, 32768\n" \
    "s_nop 0\n" \
    "buffer_load_dwordx4 v2, s[40:43], s68 offen lds\n" \
    "s_add_u32 m0, s52, 33792\n" \
    "s_nop 0\n" \
    "buffer_load_dwordx4 v3, s[40:43], s68 offen lds\n" \
    "s_add_u32 m0, s52, 34816\n" \
    "s_nop 0\n" \
    "buffer_load_dwordx4 v4, s[48:51], s68 offen lds\n" \
    "s_waitcnt vmcnt(35)\n" \
    "ds_read_b64_tr_b16 a[76:77], v5 offset:35840\n" \
    "ds_read_b64_tr_b16 a[78:79], v6 offset:35840\n" \
    "ds_read_b64_tr_b16 a[84:85], v7 offset:35840\n" \
    "ds_read_b64_tr_b16 a[86:87], v8 offset:35840\n" \
    "ds_read_b64_tr_b16 a[92:93], v9 offset:35840\n" \
    "ds_read_b64_tr_b16 a[94:95], v10 offset:35840\n" \
    "ds_read_b64_tr_b16 v[156:157], v11 offset:35840\n" \
    "ds_read_b64_tr_b16 v[158:159], v12 offset:35840\n" \
    "ds_read_b64_tr_b16 v[136:137], v13 offset:39936\n" \
    "ds_read_b64_tr_b16 v[138:139], v14 offset:39936\n" \
    "s_waitcnt lgkmcnt(0)\n" \
    "s_xor_b32 s68, s61, 3932160\n" \
    "s_add_u32 m0, s52, 35840\n" \
    "s_nop 0\n" \
    "buffer_load_dwordx4 v0, s[40:43], s68 offen lds\n" \
    "s_add_u32 m0, s52, 36864\n" \
    "s_nop 0\n" \
    "buffer_load_dwordx4 v1, s[40:43], s68 offen lds\n" \
    "s_add_u32 m0, s52, 37888\n" \
    "s_nop 0\n" \
    "buffer_load_dwordx4 v2, s[40:43], s68 offen lds\n" \
    "s_add_u32 m0, s52, 38912\n" \
    "s_nop 0\n" \
    "buffer_load_dwordx4 v3, s[40:43], s68 offen lds\n" \
    "s_add_u32 m0, s52, 39936\n" \
    "s_nop 0\n" \
    "buffer_load_dwordx4 v4, s[48:51], s68 offen lds\n" \
    "s_waitcnt vmcnt(35)\n" \
    "ds_read_b64_tr_b16 a[96:97], v5 offset:0\n" \
    "ds_read_b64_tr_b16 a[98:99], v6 offset:0\n" \
    "ds_read_b64_tr_b16 a[104:105], v7 offset:0\n" \
    "ds_read_b64_tr_b16 a[106:107], v8 offset:0\n" \
    "ds_read_b64_tr_b16 a[112:113], v9 offset:0\n" \
    "ds_read_b64_tr_b16 a[114:115], v10 offset:0\n" \
    "ds_read_b64_tr_b16 v[160:161], v11 offset:0\n" \
    "ds_read_b64_tr_b16 v[162:163], v12 offset:0\n" \
    "ds_read_b64_tr_b16 v[208:209], v13 offset:4096\n" \
    "ds_read_b64_tr_b16 v[210:211], v14 offset:4096\n" \
    "s_waitcnt lgkmcnt(0)\n" \
    "s_mov_b32 s68, 4194304\n" \
    "s_add_u32 m0, s52, 0\n" \
    "s_nop 0\n" \
    "buffer_load_dwordx4 v0, s[40:43], s68 offen lds\n" \
    "s_add_u32 m0, s52, 1024\n" \
    "s_nop 0\n" \
    "buffer_load_dwordx4 v1, s[40:43], s68 offen lds\n" \
    "s_add_u32 m0, s52, 2048\n" \
    "s_nop 0\n" \
    "buffer_load_dwordx4 v2, s[40:43], s68 offen lds\n" \
    "s_add_u32 m0, s52, 3072\n" \
    "s_nop 0\n" \
    "buffer_load_dwordx4 v3, s[40:43], s68 offen lds\n" \
    "s_add_u32 m0, s52, 4096\n" \
    "s_nop 0\n" \
    "buffer_load_dwordx4 v4, s[48:51], s68 offen lds\n" \
    "s_waitcnt vmcnt(35)\n" \
    "ds_read_b64_tr_b16 a[100:101], v5 offset:5120\n" \
    "ds_read_b64_tr_b16 a[102:103], v6 offset:5120\n" \
    "ds_read_b64_tr_b16 a[108:109], v7 offset:5120\n" \
    "ds_read_b64_tr_b16 a[110:111], v8 offset:5120\n" \
    "ds_read_b64_tr_b16 a[116:117], v9 offset:5120\n" \
    "ds_read_b64_tr_b16 a[118:119], v10 offset:5120\n" \
    "ds_read_b64_tr_b16 v[164:165], v11 offset:5120\n" \
    "ds_read_b64_tr_b16 v[166:167], v12 offset:5120\n" \
    "ds_read_b64_tr_b16 v[212:213], v13 offset:9216\n" \
    "ds_read_b64_tr_b16 v[214:215], v14 offset:9216\n" \
    "s_waitcnt lgkmcnt(0)\n" \
    "s_mov_b32 s68, 4456448\n" \
    "s_add_u32 m0, s52, 5120\n" \
    "s_nop 0\n" \
    "buffer_load_dwordx4 v0, s[40:43], s68 offen lds\n" \
    "s_add_u32 m0, s52, 6144\n" \
    "s_nop 0\n" \
    "buffer_load_dwordx4 v1, s[40:43], s68 offen lds\n" \
    "s_add_u32 m0, s52, 7168\n" \
    "s_nop 0\n" \
    "buffer_load_dwordx4 v2, s[40:43], s68 offen lds\n" \
    "s_add_u32 m0, s52, 8192\n" \
    "s_nop 0\n" \
    "buffer_load_dwordx4 v3, s[40:43], s68 offen lds\n" \
    "s_add_u32 m0, s52, 9216\n" \
    "s_nop 0\n" \
    "buffer_load_dwordx4 v4, s[48:51], s68 offen lds\n" \
    "s_waitcnt vmcnt(35)\n" \
    "ds_read_b64_tr_b16 a[120:121], v5 offset:10240\n" \
    "ds_read_b64_tr_b16 a[122:123], v6 offset:10240\n" \
    "ds_read_b64_tr_b16 a[128:129], v7 offset:10240\n" \
    "ds_read_b64_tr_b16 a[130:131], v8 offset:10240\n" \
    "ds_read_b64_tr_b16 a[136:137], v9 offset:10240\n" \
    "ds_read_b64_tr_b16 a[138:139], v10 offset:10240\n" \
    "ds_read_b64_tr_b16 v[168:169], v11 offset:10240\n" \
    "ds_read_b64_tr_b16 v[170:171], v12 offset:10240\n" \
    "ds_read_b64_tr_b16 v[216:217], v13 offset:14336\n" \
    "ds_read_b64_tr_b16 v[218:219], v14 offset:14336\n" \
    "s_waitcnt lgkmcnt(0)\n" \
    "s_mov_b32 s68, 4718592\n" \
    "s_add_u32 m0, s52, 10240\n" \
    "s_nop 0\n" \
    "buffer_load_dwordx4 v0, s[40:43], s68 offen lds\n" \
    "s_add_u32 m0, s52, 11264\n" \
    "s_nop 0\n" \
    "buffer_load_dwordx4 v1, s[40:43], s68 offen lds\n" \
    "s_add_u32 m0, s52, 12288\n" \
    "s_nop 0\n" \
    "buffer_load_dwordx4 v2, s[40:43], s68 offen lds\n" \
    "s_add_u32 m0, s52, 13312\n" \
    "s_nop 0\n" \
    "buffer_load_dwordx4 v3, s[40:43], s68 offen lds\n" \
    "s_add_u32 m0, s52, 14336\n" \
    "s_nop 0\n" \
    "buffer_load_dwordx4 v4, s[48:51], s68 offen lds\n" \
    "s_waitcnt vmcnt(35)\n" \
    "ds_read_b64_tr_b16 a[124:125], v5 offset:15360\n" \
    "ds_read_b64_tr_b16 a[126:127], v6 offset:15360\n" \
    "ds_read_b64_tr_b16 a[132:133], v7 offset:15360\n" \
    "ds_read_b64_tr_b16 a[134:135], v8 offset:15360\n" \
    "ds_read_b64_tr_b16 a[140:141], v9 offset:15360\n" \
    "ds_read_b64_tr_b16 a[142:143], v10 offset:15360\n" \
    "ds_read_b64_tr_b16 v[172:173], v11 offset:15360\n" \
    "ds_read_b64_tr_b16 v[174:175], v12 offset:15360\n" \
    "ds_read_b64_tr_b16 v[220:221], v13 offset:19456\n" \
    "ds_read_b64_tr_b16 v[222:223], v14 offset:19456\n" \
    "s_waitcnt lgkmcnt(0)\n" \
    "s_mov_b32 s68, 4980736\n" \
    "s_add_u32 m0, s52, 15360\n" \
    "s_nop 0\n" \
    "buffer_load_dwordx4 v0, s[40:43], s68 offen lds\n" \
    "s_add_u32 m0, s52, 16384\n" \
    "s_nop 0\n" \
    "buffer_load_dwordx4 v1, s[40:43], s68 offen lds\n" \
    "s_add_u32 m0, s52, 17408\n" \
    "s_nop 0\n" \
    "buffer_load_dwordx4 v2, s[40:43], s68 offen lds\n" \
    "s_add_u32 m0, s52, 18432\n" \
    "s_nop 0\n" \
    "buffer_load_dwordx4 v3, s[40:43], s68 offen lds\n" \
    "s_add_u32 m0, s52, 19456\n" \
    "s_nop 0\n" \
    "buffer_load_dwordx4 v4, s[48:51], s68 offen lds\n" \
    "s_waitcnt vmcnt(35)\n" \
    "ds_read_b64_tr_b16 a[144:145], v5 offset:20480\n" \
    "ds_read_b64_tr_b16 a[146:147], v6 offset:20480\n" \
    "ds_read_b64_tr_b16 a[152:153], v7 offset:20480\n" \
    "ds_read_b64_tr_b16 a[154:155], v8 offset:20480\n" \
    "ds_read_b64_tr_b16 a[160:161], v9 offset:20480\n" \
    "ds_read_b64_tr_b16 a[162:163], v10 offset:20480\n" \
    "ds_read_b64_tr_b16 v[176:177], v11 offset:20480\n" \
    "ds_read_b64_tr_b16 v[178:179], v12 offset:20480\n" \
    "ds_read_b64_tr_b16 v[224:225], v13 offset:24576\n" \
    "ds_read_b64_tr_b16 v[226:227], v14 offset:24576\n" \
    "s_waitcnt lgkmcnt(0)\n" \
    "s_waitcnt vmcnt(30)\n" \
    "ds_read_b64_tr_b16 a[148:149], v5 offset:25600\n" \
    "ds_read_b64_tr_b16 a[150:151], v6 offset:25600\n" \
    "ds_read_b64_tr_b16 a[156:157], v7 offset:25600\n" \
    "ds_read_b64_tr_b16 a[158:159], v8 offset:25600\n" \
    "ds_read_b64_tr_b16 a[164:165], v9 offset:25600\n" \
    "ds_read_b64_tr_b16 a[166:167], v10 offset:25600\n" \
    "ds_read_b64_tr_b16 v[180:181], v11 offset:25600\n" \
    "ds_read_b64_tr_b16 v[182:183], v12 offset:25600\n" \
    "ds_read_b64_tr_b16 v[228:229], v13 offset:29696\n" \
    "ds_read_b64_tr_b16 v[230:231], v14 offset:29696\n" \
    "s_waitcnt lgkmcnt(0)\n" \
    "s_waitcnt vmcnt(25)\n" \
    "ds_read_b64_tr_b16 a[168:169], v5 offset:30720\n" \
    "ds_read_b64_tr_b16 a[170:171], v6 offset:30720\n" \
    "ds_read_b64_tr_b16 a[176:177], v7 offset:30720\n" \
    "ds_read_b64_tr_b16 a[178:179], v8 offset:30720\n" \
    "ds_read_b64_tr_b16 a[184:185], v9 offset:30720\n" \
    "ds_read_b64_tr_b16 a[186:187], v10 offset:30720\n" \
    "ds_read_b64_tr_b16 v[184:185], v11 offset:30720\n" \
    "ds_read_b64_tr_b16 v[186:187], v12 offset:30720\n" \
    "ds_read_b64_tr_b16 v[232:233], v13 offset:34816\n" \
    "ds_read_b64_tr_b16 v[234:235], v14 offset:34816\n" \
    "s_waitcnt lgkmcnt(0)\n" \
    "s_waitcnt vmcnt(20)\n" \
    "ds_read_b64_tr_b16 a[172:173], v5 offset:35840\n" \
    "ds_read_b64_tr_b16 a[174:175], v6 offset:35840\n" \
    "ds_read_b64_tr_b16 a[180:181], v7 offset:35840\n" \
    "ds_read_b64_tr_b16 a[182:183], v8 offset:35840\n" \
    "ds_read_b64_tr_b16 a[188:189], v9 offset:35840\n" \
    "ds_read_b64_tr_b16 a[190:191], v10 offset:35840\n" \
    "ds_read_b64_tr_b16 v[188:189], v11 offset:35840\n" \
    "ds_read_b64_tr_b16 v[190:191], v12 offset:35840\n" \
    "ds_read_b64_tr_b16 v[236:237], v13 offset:39936\n" \
    "ds_read_b64_tr_b16 v[238:239], v14 offset:39936\n" \
    "s_waitcnt lgkmcnt(0)\n" \
    "s_waitcnt vmcnt(15)\n" \
    "ds_read_b64_tr_b16 a[192:193], v5 offset:0\n" \
    "ds_read_b64_tr_b16 a[194:195], v6 offset:0\n" \
    "ds_read_b64_tr_b16 a[200:201], v7 offset:0\n" \
    "ds_read_b64_tr_b16 a[202:203], v8 offset:0\n" \
    "ds_read_b64_tr_b16 a[208:209], v9 offset:0\n" \
    "ds_read_b64_tr_b16 a[210:211], v10 offset:0\n" \
    "ds_read_b64_tr_b16 v[192:193], v11 offset:0\n" \
    "ds_read_b64_tr_b16 v[194:195], v12 offset:0\n" \
    "ds_read_b64_tr_b16 v[240:241], v13 offset:4096\n" \
    "ds_read_b64_tr_b16 v[242:243], v14 offset:4096\n" \
    "s_waitcnt lgkmcnt(0)\n" \
    "s_waitcnt vmcnt(10)\n" \
    "ds_read_b64_tr_b16 a[196:197], v5 offset:5120\n" \
    "ds_read_b64_tr_b16 a[198:199], v6 offset:5120\n" \
    "ds_read_b64_tr_b16 a[204:205], v7 offset:5120\n" \
    "ds_read_b64_tr_b16 a[206:207], v8 offset:5120\n" \
    "ds_read_b64_tr_b16 a[212:213], v9 offset:5120\n" \
    "ds_read_b64_tr_b16 a[214:215], v10 offset:5120\n" \
    "ds_read_b64_tr_b16 v[196:197], v11 offset:5120\n" \
    "ds_read_b64_tr_b16 v[198:199], v12 offset:5120\n" \
    "ds_read_b64_tr_b16 v[244:245], v13 offset:9216\n" \
    "ds_read_b64_tr_b16 v[246:247], v14 offset:9216\n" \
    "s_waitcnt lgkmcnt(0)\n" \
    "s_waitcnt vmcnt(5)\n" \
    "ds_read_b64_tr_b16 a[216:217], v5 offset:10240\n" \
    "ds_read_b64_tr_b16 a[218:219], v6 offset:10240\n" \
    "ds_read_b64_tr_b16 a[224:225], v7 offset:10240\n" \
    "ds_read_b64_tr_b16 a[226:227], v8 offset:10240\n" \
    "ds_read_b64_tr_b16 a[232:233], v9 offset:10240\n" \
    "ds_read_b64_tr_b16 a[234:235], v10 offset:10240\n" \
    "ds_read_b64_tr_b16 v[200:201], v11 offset:10240\n" \
    "ds_read_b64_tr_b16 v[202:203], v12 offset:10240\n" \
    "ds_read_b64_tr_b16 v[248:249], v13 offset:14336\n" \
    "ds_read_b64_tr_b16 v[250:251], v14 offset:14336\n" \
    "s_waitcnt lgkmcnt(0)\n" \
    "s_waitcnt vmcnt(0)\n" \
    "ds_read_b64_tr_b16 a[220:221], v5 offset:15360\n" \
    "ds_read_b64_tr_b16 a[222:223], v6 offset:15360\n" \
    "ds_read_b64_tr_b16 a[228:229], v7 offset:15360\n" \
    "ds_read_b64_tr_b16 a[230:231], v8 offset:15360\n" \
    "ds_read_b64_tr_b16 a[236:237], v9 offset:15360\n" \
    "ds_read_b64_tr_b16 a[238:239], v10 offset:15360\n" \
    "ds_read_b64_tr_b16 v[204:205], v11 offset:15360\n" \
    "ds_read_b64_tr_b16 v[206:207], v12 offset:15360\n" \
    "ds_read_b64_tr_b16 v[252:253], v13 offset:19456\n" \
    "ds_read_b64_tr_b16 v[254:255], v14 offset:19456\n" \
    "s_waitcnt lgkmcnt(0)\n" \
    "s_memtime s[96:97]\n" \
    "s_memrealtime s[80:81]\n" \
    "s_waitcnt lgkmcnt(0)\n" \
    "s_sub_u32 s68, s96, s92\n" \
    "s_sub_u32 s69, s80, s94\n" \
    "s_min_u32 s69, s69, 4095\n" \
    "s_and_b32 s68, s68, 0xfffff\n" \
    "s_lshl_b32 s69, s69, 20\n" \
    "s_or_b32 s68, s68, s69\n" \
    "v_mov_b32 v48, s63\n" \
    "v_mov_b32 v49, s68\n" \
    "v_mov_b32 v141, v44\n" \
    "v_readfirstlane_b32 s50, v46\n" \
    "v_mov_b32 v142, 0\n" \
    "v_mov_b32 v143, 0\n" \
    "s_barrier\n" \
    "buffer_store_dwordx2 v[48:49], v45, s[36:39], 0 offen sc1\n" \
    "ds_write_b128 v141, v[108:111] offset:0\n" \
    "ds_write_b128 v141, v[112:115] offset:1024\n" \
    "ds_write_b128 v141, v[116:119] offset:2048\n" \
    "ds_write_b128 v141, v[120:123] offset:3072\n" \
    "ds_write_b128 v141, v[124:127] offset:4096\n" \
    "ds_write_b128 v141, v[128:131] offset:5120\n" \
    "ds_write_b128 v141, v[132:135] offset:6144\n" \
    "ds_write_b128 v141, v[136:139] offset:7168\n" \
    "s_mov_b32 s86, 0x3e0293ee\n" \
    "s_mov_b32 s88, 0x3db504f3\n" \
    "s_mov_b32 s90, 0x3fb8aa3b\n" \
    "s_mov_b32 s72, 327680\n" \
    "s_mov_b32 s67, 0\n" \
    "s_mov_b32 s77, 0\n" \
    "s_mov_b32 s78, 0\n" \
    "v_mov_b32 v26, 0\n" \
    "v_mov_b32 v27, 0\n" \
    "s_mov_b32 s70, -1\n" \
    "v_mov_b32 v25, v16\n" \
    "H_next_%=:\n" \
    "v_readfirstlane_b32 s48, v142\n" \
    "v_readfirstlane_b32 s49, v143\n" \
    "s_add_u32 s68, s70, 1\n" \
    "v_mov_b32 v142, s63\n" \
    "v_mov_b32 v143, s68\n" \
    "v_cmp_eq_u32 vcc, s63, v26\n" \
    "s_nop 1\n" \
    "v_cndmask_b32 v27, 0, v27, vcc\n" \
    "s_nop 0\n" \
    "v_readlane_b32 s40, v27, 0\n" \
    "v_readlane_b32 s41, v27, 1\n" \
    "v_readlane_b32 s42, v27, 2\n" \
    "v_readlane_b32 s43, v27, 3\n" \
    "v_readlane_b32 s44, v27, 4\n" \
    "s_min_u32 s40, s40, s41\n" \
    "s_min_u32 s42, s42, s43\n" \
    "s_min_u32 s40, s40, s44\n" \
    "s_min_u32 s40, s40, s42\n" \
    "s_max_u32 s77, s77, s40\n" \
    "s_cmp_lt_i32 s70, 2\n" \
    "s_cbranch_scc1 H_near_%=\n" \
    "H_fetched_%=:\n" \
    "s_mov_b32 s73, s72\n" \
    "s_add_u32 s72, s72, 163840\n" \
    "s_cmp_eq_u32 s72, 491520\n" \
    "s_cselect_b32 s72, 0, s72\n" \
    "s_add_u32 s70, s70, 1\n" \
    "s_mov_b32 s82, 0\n" \
    "s_mov_b32 s45, 0\n" \
    "s_cmp_eq_u32 s70, 0\n" \
    "s_cbranch_scc1 H_start_%=\n" \
    "s_add_u32 s74, s72, s46\n" \
    "s_add_u32 s75, s72, s47\n" \
    "s_add_u32 s76, s72, s61\n" \
    "H_based_%=:\n" \
    "s_mov_b32 s83, s71\n" \
    "s_cmp_lg_u32 s78, 0\n" \
    "s_cbranch_scc1 H_recover_%=\n" \
    "H_clock_%=:\n" \
    "s_memrealtime s[80:81]\n" \
    "s_waitcnt lgkmcnt(0)\n" \
    "s_sub_i32 s84, s71, s80\n" \
    "s_cmp_lt_i32 s84, -3\n" \
    "s_cbranch_scc1 H_late_%=\n" \
    "s_cmp_lt_i32 s84, 4\n" \
    "s_cbranch_scc1 H_wc_fine_%=\n" \
    "H_wc_spin_%=:\n" \
    "s_memrealtime s[80:81]\n" \
    "s_waitcnt lgkmcnt(0)\n" \
    "s_sub_i32 s84, s71, s80\n" \
    "s_cmp_gt_i32 s84, 3\n" \
    "s_cbranch_scc1 H_wc_spin_%=\n" \
    "H_wc_fine_%=:\n" \
    "s_cmp_lt_i32 s84, 1\n" \
    "s_cbranch_scc1 H_wc_go_%=\n" \
    "s_nop 3\n" \
    "s_cmp_lt_i32 s84, 2\n" \
    "s_cbranch_scc1 H_wc_go_%=\n" \
    "s_nop 3\n" \
    "s_cmp_lt_i32 s84, 3\n" \
    "s_cbranch_scc1 H_wc_go_%=\n" \
    "s_nop 6\n" \
    "H_wc_go_%=:\n" \
    "H_pass_%=:\n" \
    "buffer_load_dwordx4 v[48:51], v16, s[36:39], s74 offen offset:0 sc1\n" \
    "s_nop 5\n" \
    "buffer_load_dwordx4 v[52:55], v16, s[36:39], s74 offen offset:1024 sc1\n" \
    "s_nop 5\n" \
    "buffer_load_dwordx4 v[56:59], v16, s[36:39], s74 offen offset:2048 sc1\n" \
    "s_nop 5\n" \
    "buffer_load_dwordx4 v[60:63], v16, s[36:39], s74 offen offset:3072 sc1\n" \
    "s_nop 5\n" \
    "buffer_load_dwordx4 v[64:67], v16, s[36:39], s75 offen offset:0 sc1\n" \
    "s_nop 5\n" \
    "buffer_load_dwordx4 v[68:71], v16, s[36:39], s75 offen offset:1024 sc1\n" \
    "s_nop 5\n" \
    "buffer_load_dwordx4 v[72:75], v16, s[36:39], s75 offen offset:2048 sc1\n" \
    "s_nop 5\n" \
    "buffer_load_dwordx4 v[76:79], v16, s[36:39], s75 offen offset:3072 sc1\n" \
    "s_nop 5\n" \
    "buffer_load_dwordx4 v[80:83], v16, s[36:39], s76 offen offset:0 sc1\n" \
    "s_nop 5\n" \
    "buffer_load_dwordx4 v[84:87], v16, s[36:39], s76 offen offset:1024 sc1\n" \
    "s_waitcnt vmcnt(9)\n" \
    "v_smfmac_f32_16x16x64_bf16 v[88:91], v[48:51], a[0:7], v17\n" \
    "v_max3_u32 v140, v140, v48, v50\n" \
    "v_smfmac_f32_16x16x64_bf16 v[92:95], v[48:51], a[8:15], v17\n" \
    "v_smfmac_f32_16x16x64_bf16 v[96:99], v[48:51], a[16:23], v17\n" \
    "v_smfmac_f32_16x16x64_bf16 v[100:103], v[48:51], a[240:247], v17\n" \
    "s_waitcnt vmcnt(8)\n" \
    "v_smfmac_f32_16x16x64_bf16 v[88:91], v[52:55], a[24:31], v17\n" \
    "v_max3_u32 v140, v140, v52, v54\n" \
    "v_smfmac_f32_16x16x64_bf16 v[92:95], v[52:55], a[32:39], v17\n" \
    "v_smfmac_f32_16x16x64_bf16 v[96:99], v[52:55], a[40:47], v17\n" \
    "v_smfmac_f32_16x16x64_bf16 v[100:103], v[52:55], a[248:255], v17\n" \
    "s_waitcnt vmcnt(7)\n" \
    "v_smfmac_f32_16x16x64_bf16 v[88:91], v[56:59], a[48:55], v17\n" \
    "v_max3_u32 v140, v140, v56, v58\n" \
    "v_smfmac_f32_16x16x64_bf16 v[92:95], v[56:59], a[56:63], v17\n" \
    "v_smfmac_f32_16x16x64_bf16 v[96:99], v[56:59], a[64:71], v17\n" \
    "v_smfmac_f32_16x16x64_bf16 v[100:103], v[56:59], v[144:151], v17\n" \
    "s_waitcnt vmcnt(6)\n" \
    "v_smfmac_f32_16x16x64_bf16 v[88:91], v[60:63], a[72:79], v17\n" \
    "v_max3_u32 v140, v140, v60, v62\n" \
    "v_smfmac_f32_16x16x64_bf16 v[92:95], v[60:63], a[80:87], v17\n" \
    "v_smfmac_f32_16x16x64_bf16 v[96:99], v[60:63], a[88:95], v17\n" \
    "v_smfmac_f32_16x16x64_bf16 v[100:103], v[60:63], v[152:159], v17\n" \
    "s_waitcnt vmcnt(5)\n" \
    "v_smfmac_f32_16x16x64_bf16 v[88:91], v[64:67], a[96:103], v17\n" \
    "v_max3_u32 v140, v140, v64, v66\n" \
    "v_smfmac_f32_16x16x64_bf16 v[92:95], v[64:67], a[104:111], v17\n" \
    "v_smfmac_f32_16x16x64_bf16 v[96:99], v[64:67], a[112:119], v17\n" \
    "v_smfmac_f32_16x16x64_bf16 v[100:103], v[64:67], v[160:167], v17\n" \
    "s_waitcnt vmcnt(4)\n" \
    "v_smfmac_f32_16x16x64_bf16 v[88:91], v[68:71], a[120:127], v17\n" \
    "v_max3_u32 v140, v140, v68, v70\n" \
    "v_smfmac_f32_16x16x64_bf16 v[92:95], v[68:71], a[128:135], v17\n" \
    "v_smfmac_f32_16x16x64_bf16 v[96:99], v[68:71], a[136:143], v17\n" \
    "v_smfmac_f32_16x16x64_bf16 v[100:103], v[68:71], v[168:175], v17\n" \
    "s_waitcnt vmcnt(3)\n" \
    "v_smfmac_f32_16x16x64_bf16 v[88:91], v[72:75], a[144:151], v17\n" \
    "v_max3_u32 v140, v140, v72, v74\n" \
    "v_smfmac_f32_16x16x64_bf16 v[92:95], v[72:75], a[152:159], v17\n" \
    "v_smfmac_f32_16x16x64_bf16 v[96:99], v[72:75], a[160:167], v17\n" \
    "buffer_store_dwordx2 v[142:143], v19, s[36:39], 0 offen offset:64 sc1\n" \
    "v_smfmac_f32_16x16x64_bf16 v[100:103], v[72:75], v[176:183], v17\n" \
    "s_waitcnt vmcnt(3)\n" \
    "v_smfmac_f32_16x16x64_bf16 v[88:91], v[76:79], a[168:175], v17\n" \
    "v_max3_u32 v140, v140, v76, v78\n" \
    "v_smfmac_f32_16x16x64_bf16 v[92:95], v[76:79], a[176:183], v17\n" \
    "v_smfmac_f32_16x16x64_bf16 v[96:99], v[76:79], a[184:191], v17\n" \
    "v_smfmac_f32_16x16x64_bf16 v[100:103], v[76:79], v[184:191], v17\n" \
    "s_waitcnt vmcnt(2)\n" \
    "v_smfmac_f32_16x16x64_bf16 v[88:91], v[80:83], a[192:199], v17\n" \
    "v_max3_u32 v140, v140, v80, v82\n" \
    "v_smfmac_f32_16x16x64_bf16 v[92:95], v[80:83], a[200:207], v17\n" \
    "s_waitcnt vmcnt(1)\n" \
    "v_smfmac_f32_16x16x64_bf16 v[88:91], v[84:87], a[216:223], v17\n" \
    "v_max3_u32 v140, v140, v84, v86\n" \
    "v_smfmac_f32_16x16x64_bf16 v[92:95], v[84:87], a[224:231], v17\n" \
    "v_smfmac_f32_16x16x64_bf16 v[96:99], v[80:83], a[208:215], v17\n" \
    "v_cmp_eq_u32 vcc, -1, v140\n" \
    "s_cbranch_vccnz H_retry_%=\n" \
    "v_smfmac_f32_16x16x64_bf16 v[100:103], v[80:83], v[192:199], v17\n" \
    "s_nop 1\n" \
    "v_smfmac_f32_16x16x64_bf16 v[96:99], v[84:87], a[232:239], v17\n" \
    "s_nop 0\n" \
    "v_smfmac_f32_16x16x64_bf16 v[100:103], v[84:87], v[200:207], v17\n" \
    "v_permlane16_swap_b32 v88, v92\n" \
    "v_permlane16_swap_b32 v89, v93\n" \
    "v_permlane16_swap_b32 v90, v94\n" \
    "v_permlane16_swap_b32 v91, v95\n" \
    "v_pk_add_f32 v[88:89], v[88:89], v[92:93]\n" \
    "v_pk_add_f32 v[90:91], v[90:91], v[94:95]\n" \
    "v_cvt_pk_bf16_f32 v88, v88, v89\n" \
    "v_cvt_pk_bf16_f32 v89, v90, v91\n" \
    "ds_write_b64 v21, v[88:89] offset:0\n" \
    "v_smfmac_f32_16x16x64_bf16 v[104:107], v[48:51], v[108:115], v17\n" \
    "v_permlane16_swap_b32 v96, v100\n" \
    "v_permlane16_swap_b32 v97, v101\n" \
    "v_permlane16_swap_b32 v98, v102\n" \
    "v_permlane16_swap_b32 v99, v103\n" \
    "v_pk_add_f32 v[96:97], v[96:97], v[100:101]\n" \
    "v_pk_add_f32 v[98:99], v[98:99], v[102:103]\n" \
    "v_cvt_pk_bf16_f32 v96, v96, v97\n" \
    "v_cvt_pk_bf16_f32 v97, v98, v99\n" \
    "ds_write_b64 v21, v[96:97] offset:1024\n" \
    "v_smfmac_f32_16x16x64_bf16 v[104:107], v[52:55], v[116:123], v17\n" \
    "v_smfmac_f32_16x16x64_bf16 v[104:107], v[56:59], v[124:131], v17\n" \
    "v_smfmac_f32_16x16x64_bf16 v[104:107], v[60:63], v[132:139], v17\n" \
    "s_waitcnt lgkmcnt(0)\n" \
    "s_barrier\n" \
    "ds_read_b64_tr_b16 v[88:89], v22 offset:0\n" \
    "ds_read_b64_tr_b16 v[90:91], v22 offset:512\n" \
    "v_smfmac_f32_16x16x64_bf16 v[104:107], v[64:67], v[208:215], v17\n" \
    "ds_read_b64_tr_b16 v[92:93], v22 offset:1024\n" \
    "ds_read_b64_tr_b16 v[94:95], v22 offset:1536\n" \
    "v_smfmac_f32_16x16x64_bf16 v[104:107], v[68:71], v[216:223], v17\n" \
    "ds_read_b128 v[108:111], v25 offset:2048\n" \
    "ds_read_b128 v[112:115], v25 offset:6144\n" \
    "v_smfmac_f32_16x16x64_bf16 v[104:107], v[72:75], v[224:231], v17\n" \
    "ds_read_b64_tr_b16 v[96:97], v22 offset:2048\n" \
    "ds_read_b64_tr_b16 v[98:99], v22 offset:2560\n" \
    "v_smfmac_f32_16x16x64_bf16 v[104:107], v[76:79], v[232:239], v17\n" \
    "ds_read_b128 v[116:119], v25 offset:3072\n" \
    "ds_read_b128 v[120:123], v25 offset:7168\n" \
    "v_smfmac_f32_16x16x64_bf16 v[104:107], v[80:83], v[240:247], v17\n" \
    "ds_read_b64_tr_b16 v[100:101], v22 offset:3072\n" \
    "ds_read_b64_tr_b16 v[102:103], v22 offset:3584\n" \
    "v_smfmac_f32_16x16x64_bf16 v[104:107], v[84:87], v[248:255], v17\n" \
    "s_waitcnt lgkmcnt(10)\n" \
    "v_mfma_f32_16x16x32_bf16 v[48:51], v[0:3], v[88:91], 0\n" \
    "v_mfma_f32_16x16x32_bf16 v[52:55], v[4:7], v[88:91], 0\n" \
    "v_mfma_f32_16x16x32_bf16 v[56:59], v[88:91], v[88:91], v[28:31]\n" \
    "s_waitcnt lgkmcnt(8)\n" \
    "v_mfma_f32_16x16x32_bf16 v[48:51], v[8:11], v[92:95], v[48:51]\n" \
    "s_nop 1\n" \
    "v_mfma_f32_16x16x32_bf16 v[52:55], v[12:15], v[92:95], v[52:55]\n" \
    "v_mov_b32 v60, v104\n" \
    "v_mov_b32 v61, v105\n" \
    "v_mfma_f32_16x16x32_bf16 v[56:59], v[92:95], v[92:95], v[56:59]\n" \
    "v_mov_b32 v62, v106\n" \
    "v_mov_b32 v63, v107\n" \
    "s_waitcnt lgkmcnt(4)\n" \
    "v_mfma_f32_16x16x32_bf16 v[48:51], v[108:111], v[96:99], v[48:51]\n" \
    "v_permlane16_swap_b32 v104, v60\n" \
    "v_permlane16_swap_b32 v105, v61\n" \
    "v_mfma_f32_16x16x32_bf16 v[52:55], v[112:115], v[96:99], v[52:55]\n" \
    "v_permlane16_swap_b32 v106, v62\n" \
    "v_permlane16_swap_b32 v107, v63\n" \
    "v_mfma_f32_16x16x32_bf16 v[56:59], v[96:99], v[96:99], v[56:59]\n" \
    "v_add_f32 v60, v104, v60\n" \
    "v_add_f32 v61, v105, v61\n" \
    "s_waitcnt lgkmcnt(0)\n" \
    "v_mfma_f32_16x16x32_bf16 v[56:59], v[100:103], v[100:103], v[56:59]\n" \
    "v_add_f32 v62, v106, v62\n" \
    "v_add_f32 v63, v107, v63\n" \
    "v_mfma_f32_16x16x32_bf16 v[48:51], v[116:119], v[100:103], v[48:51]\n" \
    "v_cvt_pk_bf16_f32 v104, v60, v61\n" \
    "v_cvt_pk_bf16_f32 v105, v62, v63\n" \
    "v_mfma_f32_16x16x32_bf16 v[52:55], v[120:123], v[100:103], v[52:55]\n" \
    "s_nop 1\n" \
    "v_maximum3_f32 v64, v56, v57, v58\n" \
    "s_nop 1\n" \
    "v_maximum3_f32 v65, v59, v48, v49\n" \
    "v_maximum3_f32 v64, v64, v50, v51\n" \
    "s_nop 0\n" \
    "v_maximum3_f32 v65, v65, v52, v53\n" \
    "v_maximum3_f32 v64, v64, v54, v55\n" \
    "v_maximum3_f32 v64, v64, v65, v65\n" \
    "v_mov_b32 v65, v64\n" \
    "s_nop 1\n" \
    "v_permlane16_swap_b32 v64, v65\n" \
    "v_maximum3_f32 v64, v64, v65, v65\n" \
    "v_mov_b32 v65, v64\n" \
    "s_nop 1\n" \
    "v_permlane32_swap_b32 v64, v65\n" \
    "v_maximum3_f32 v64, v64, v65, v65\n" \
    "v_mul_f32 v66, 0xbe0293ee, v64\n" \
    "v_pk_fma_f32 v[68:69], v[56:57], s[86:87], v[66:67] op_sel_hi:[1,0,0]\n" \
    "v_pk_fma_f32 v[70:71], v[58:59], s[86:87], v[66:67] op_sel_hi:[1,0,0]\n" \
    "v_pk_fma_f32 v[72:73], v[48:49], s[86:87], v[66:67] op_sel_hi:[1,0,0]\n" \
    "v_pk_fma_f32 v[74:75], v[50:51], s[86:87], v[66:67] op_sel_hi:[1,0,0]\n" \
    "v_pk_fma_f32 v[76:77], v[52:53], s[86:87], v[66:67] op_sel_hi:[1,0,0]\n" \
    "v_pk_fma_f32 v[78:79], v[54:55], s[86:87], v[66:67] op_sel_hi:[1,0,0]\n" \
    "v_exp_f32 v68, v68\n" \
    "v_exp_f32 v69, v69\n" \
    "v_exp_f32 v70, v70\n" \
    "v_exp_f32 v71, v71\n" \
    "v_exp_f32 v72, v72\n" \
    "v_cvt_pk_bf16_f32 v80, v68, v69\n" \
    "v_cvt_pk_bf16_f32 v81, v70, v71\n" \
    "v_exp_f32 v73, v73\n" \
    "v_exp_f32 v74, v74\n" \
    "v_mfma_f32_16x16x16_bf16 v[124:127], v[104:105], v[80:81], 0\n" \
    "v_exp_f32 v75, v75\n" \
    "v_exp_f32 v76, v76\n" \
    "v_mfma_f32_16x16x16_bf16 v[128:131], v[40:41], v[80:81], 0\n" \
    "v_cvt_pk_bf16_f32 v84, v72, v73\n" \
    "v_exp_f32 v77, v77\n" \
    "v_cvt_pk_bf16_f32 v85, v74, v75\n" \
    "v_exp_f32 v78, v78\n" \
    "v_exp_f32 v79, v79\n" \
    "v_cvt_pk_bf16_f32 v86, v76, v77\n" \
    "v_cvt_pk_bf16_f32 v87, v78, v79\n" \
    "s_nop 1\n" \
    "v_mfma_f32_16x16x32_bf16 v[128:131], v[40:43], v[84:87], v[128:131]\n" \
    "v_mfma_f32_16x16x32_bf16 v[124:127], v[44:47], v[84:87], v[124:127]\n" \
    "s_nop 6\n" \
    "v_rcp_f32 v66, v128\n" \
    "s_nop 0\n" \
    "v_pk_mul_f32 v[124:125], v[124:125], v[66:67] op_sel_hi:[1,0]\n" \
    "v_pk_mul_f32 v[126:127], v[126:127], v[66:67] op_sel_hi:[1,0]\n" \
    "v_cvt_pk_bf16_f32 v64, v124, v125\n" \
    "v_cvt_pk_bf16_f32 v65, v126, v127\n" \
    "v_min_u32 v64, -2, v64\n" \
    "v_min_u32 v65, -2, v65\n" \
    "buffer_store_dwordx2 v[64:65], v18, s[36:39], s72 offen sc1\n" \
    "s_memrealtime s[80:81]\n" \
    "v_add_u32 v124, s62, v25\n" \
    "v_add_u32 v125, s66, v25\n" \
    "v_mov_b32 v128, s63\n" \
    "v_mov_b32 v129, s70\n" \
    "s_mov_b32 s68, s48\n" \
    "s_mov_b32 s69, s49\n" \
    "s_waitcnt lgkmcnt(0)\n" \
    "ds_write_b64 v23, v[104:105]\n" \
    "s_sub_u32 s85, s80, s71\n" \
    "s_lshl_b32 s85, s85, 8\n" \
    "s_or_b32 s85, s85, s45\n" \
    "v_mov_b32 v130, s85\n" \
    "buffer_store_dwordx3 v[128:130], v19, s[36:39], 0 offen sc1\n" \
    "ds_read_b128 v[60:63], v124 offset:8192\n" \
    "ds_read_b128 v[64:67], v124 offset:9216\n" \
    "ds_read_b128 v[68:71], v124 offset:10240\n" \
    "ds_read_b128 v[72:75], v124 offset:11264\n" \
    "ds_read_b128 v[76:79], v125 offset:16384\n" \
    "ds_read_b128 v[80:83], v125 offset:17408\n" \
    "s_add_u32 s71, s71, s64\n" \
    "s_add_u32 s71, s71, s65\n" \
    "s_and_b32 s85, s69, 255\n" \
    "s_sub_u32 s85, s85, s70\n" \
    "s_xor_b32 s68, s68, s63\n" \
    "s_xor_b32 s85, s85, 1\n" \
    "s_or_b32 s68, s68, s85\n" \
    "s_bfe_u32 s85, s69, 786440\n" \
    "s_lshr_b32 s69, s69, 20\n" \
    "s_cmp_eq_u32 s68, 0\n" \
    "s_cselect_b32 s64, s85, s64\n" \
    "s_cselect_b32 s65, s69, s65\n" \
    "s_lshl_b32 s68, s70, 3\n" \
    "s_lshr_b32 s68, 791592, s68\n" \
    "s_and_b32 s68, s68, 255\n" \
    "s_cmp_lt_u32 s70, 4\n" \
    "s_cselect_b32 s68, s68, 0\n" \
    "s_lshl_b32 s68, s68, 1\n" \
    "s_add_u32 s71, s71, s68\n" \
    "v_mov_b32 v126, -1\n" \
    "v_mov_b32 v127, -1\n" \
    "s_waitcnt lgkmcnt(5)\n" \
    "v_mfma_f32_16x16x32_bf16 v[48:51], v[0:3], v[60:63], v[32:35]\n" \
    "v_mfma_f32_16x16x32_bf16 v[52:55], v[4:7], v[60:63], v[36:39]\n" \
    "v_mfma_f32_16x16x32_bf16 v[56:59], v[88:91], v[60:63], v[28:31]\n" \
    "buffer_store_dwordx2 v[126:127], v18, s[36:39], s73 offen sc1\n" \
    "s_waitcnt lgkmcnt(4)\n" \
    "v_mfma_f32_16x16x32_bf16 v[48:51], v[8:11], v[64:67], v[48:51]\n" \
    "v_mfma_f32_16x16x32_bf16 v[52:55], v[12:15], v[64:67], v[52:55]\n" \
    "v_mfma_f32_16x16x32_bf16 v[56:59], v[92:95], v[64:67], v[56:59]\n" \
    "s_waitcnt lgkmcnt(3)\n" \
    "v_mfma_f32_16x16x32_bf16 v[48:51], v[108:111], v[68:71], v[48:51]\n" \
    "v_mfma_f32_16x16x32_bf16 v[52:55], v[112:115], v[68:71], v[52:55]\n" \
    "v_mfma_f32_16x16x32_bf16 v[56:59], v[96:99], v[68:71], v[56:59]\n" \
    "buffer_load_dwordx2 v[26:27], v20, s[36:39], 0 offen sc1\n" \
    "s_waitcnt lgkmcnt(2)\n" \
    "v_mfma_f32_16x16x32_bf16 v[48:51], v[116:119], v[72:75], v[48:51]\n" \
    "v_mfma_f32_16x16x32_bf16 v[52:55], v[120:123], v[72:75], v[52:55]\n" \
    "v_mfma_f32_16x16x32_bf16 v[56:59], v[100:103], v[72:75], v[56:59]\n" \
    "s_nop 7\n" \
    "v_maximum3_f32 v0, v48, v49, v50\n" \
    "v_maximum3_f32 v1, v51, v52, v53\n" \
    "v_maximum3_f32 v2, v54, v55, v56\n" \
    "v_maximum3_f32 v3, v57, v58, v59\n" \
    "v_maximum3_f32 v0, v0, v1, v2\n" \
    "v_maximum3_f32 v0, v0, v3, v3\n" \
    "v_mov_b32 v1, v0\n" \
    "s_nop 1\n" \
    "v_permlane16_swap_b32 v0, v1\n" \
    "v_maximum3_f32 v0, v0, v1, v1\n" \
    "v_mov_b32 v1, v0\n" \
    "s_nop 1\n" \
    "v_permlane32_swap_b32 v0, v1\n" \
    "v_maximum3_f32 v0, v0, v1, v1\n" \
    "v_mul_f32 v2, 0xbe0293ee, v0\n" \
    "v_pk_fma_f32 v[60:61], v[48:49], s[86:87], v[2:3] op_sel_hi:[1,0,0]\n" \
    "v_pk_fma_f32 v[62:63], v[50:51], s[86:87], v[2:3] op_sel_hi:[1,0,0]\n" \
    "v_pk_fma_f32 v[64:65], v[52:53], s[86:87], v[2:3] op_sel_hi:[1,0,0]\n" \
    "v_pk_fma_f32 v[66:67], v[54:55], s[86:87], v[2:3] op_sel_hi:[1,0,0]\n" \
    "v_pk_fma_f32 v[68:69], v[56:57], s[86:87], v[2:3] op_sel_hi:[1,0,0]\n" \
    "v_pk_fma_f32 v[70:71], v[58:59], s[86:87], v[2:3] op_sel_hi:[1,0,0]\n" \
    "s_waitcnt lgkmcnt(0)\n" \
    "s_barrier\n" \
    "ds_read_b64 v[84:85], v24\n" \
    "ds_read_b64 v[86:87], v24 offset:512\n" \
    "ds_read_b128 v[108:111], v141 offset:0\n" \
    "ds_read_b128 v[112:115], v141 offset:1024\n" \
    "ds_read_b128 v[116:119], v141 offset:2048\n" \
    "ds_read_b128 v[120:123], v141 offset:3072\n" \
    "ds_read_b128 v[124:127], v141 offset:4096\n" \
    "ds_read_b128 v[128:131], v141 offset:5120\n" \
    "ds_read_b128 v[132:135], v141 offset:6144\n" \
    "ds_read_b128 v[136:139], v141 offset:7168\n" \
    "s_add_u32 s67, s67, 20480\n" \
    "s_cmp_eq_u32 s67, 61440\n" \
    "s_cselect_b32 s67, 0, s67\n" \
    "s_add_u32 s79, s67, 20480\n" \
    "s_cmp_eq_u32 s79, 61440\n" \
    "s_cselect_b32 s79, 0, s79\n" \
    "v_add_u32 v25, s67, v16\n" \
    "v_add_u32 v48, s57, v25\n" \
    "ds_read_b128 v[0:3], v25 offset:0\n" \
    "ds_read_b128 v[4:7], v25 offset:4096\n" \
    "ds_read_b128 v[8:11], v25 offset:1024\n" \
    "ds_read_b128 v[12:15], v25 offset:5120\n" \
    "ds_read_b128 v[44:47], v48\n" \
    "s_add_u32 s69, s70, 3\n" \
    "s_min_u32 s69, s69, 64\n" \
    "s_cmp_eq_u32 s70, 0\n" \
    "s_cselect_b32 s69, 1, s69\n" \
    "s_cmp_lt_u32 s77, s69\n" \
    "s_cbranch_scc1 H_fl2_%=\n" \
    "H_fl2_done_%=:\n" \
    "s_sub_u32 s69, s69, 1\n" \
    "s_mul_i32 s68, s69, 24576\n" \
    "s_add_u32 s40, s54, s68\n" \
    "s_add_u32 s41, s40, 8192\n" \
    "s_add_u32 s42, s55, s68\n" \
    "s_add_u32 s43, s56, s79\n" \
    "s_add_u32 s44, s57, s79\n" \
    "buffer_load_dwordx2 v[142:143], off, s[36:39], s51 sc1\n" \
    "s_mov_b32 m0, s43\n" \
    "s_nop 0\n" \
    "buffer_load_dwordx4 v16, s[36:39], s40 offen offset:0 sc1 lds\n" \
    "v_exp_f32 v60, v60\n" \
    "v_exp_f32 v61, v61\n" \
    "v_exp_f32 v62, v62\n" \
    "v_exp_f32 v63, v63\n" \
    "v_exp_f32 v64, v64\n" \
    "v_exp_f32 v65, v65\n" \
    "buffer_load_dwordx4 v16, s[36:39], s40 offen offset:1024 sc1 lds\n" \
    "v_exp_f32 v66, v66\n" \
    "v_exp_f32 v67, v67\n" \
    "v_exp_f32 v68, v68\n" \
    "v_exp_f32 v69, v69\n" \
    "v_exp_f32 v70, v70\n" \
    "v_exp_f32 v71, v71\n" \
    "v_cvt_pk_bf16_f32 v104, v68, v69\n" \
    "v_cvt_pk_bf16_f32 v105, v70, v71\n" \
    "v_cvt_pk_bf16_f32 v72, v60, v61\n" \
    "v_cvt_pk_bf16_f32 v73, v62, v63\n" \
    "v_cvt_pk_bf16_f32 v74, v64, v65\n" \
    "v_cvt_pk_bf16_f32 v75, v66, v67\n" \
    "s_add_u32 m0, s43, 8192\n" \
    "s_nop 0\n" \
    "buffer_load_dwordx4 v16, s[36:39], s41 offen offset:0 sc1 lds\n" \
    "s_waitcnt lgkmcnt(13)\n" \
    "v_mfma_f32_16x16x32_bf16 v[56:59], v[40:43], v[72:75], 0\n" \
    "v_mfma_f32_16x16x32_bf16 v[48:51], v[76:79], v[72:75], 0\n" \
    "v_mfma_f32_16x16x32_bf16 v[52:55], v[80:83], v[72:75], 0\n" \
    "v_mfma_f32_16x16x16_bf16 v[56:59], v[40:41], v[104:105], v[56:59]\n" \
    "v_mfma_f32_16x16x16_bf16 v[48:51], v[84:85], v[104:105], v[48:51]\n" \
    "v_mfma_f32_16x16x16_bf16 v[52:55], v[86:87], v[104:105], v[52:55]\n" \
    "buffer_load_dwordx4 v16, s[36:39], s41 offen offset:1024 sc1 lds\n" \
    "v_mov_b64 v[88:89], 0\n" \
    "v_mov_b64 v[90:91], 0\n" \
    "v_mov_b64 v[92:93], 0\n" \
    "v_mov_b64 v[94:95], 0\n" \
    "v_mov_b64 v[96:97], 0\n" \
    "v_mov_b64 v[98:99], 0\n" \
    "v_mov_b64 v[100:101], 0\n" \
    "v_mov_b64 v[102:103], 0\n" \
    "v_mov_b64 v[104:105], 0\n" \
    "v_mov_b64 v[106:107], 0\n" \
    "v_mov_b32 v140, 0\n" \
    "s_mov_b32 m0, s44\n" \
    "s_nop 0\n" \
    "buffer_load_dwordx4 v16, s[36:39], s42 offen offset:0 sc1 lds\n" \
    "s_lshl_b32 s68, s70, 13\n" \
    "s_add_u32 s68, s68, s58\n" \
    "v_rcp_f32 v64, v56\n" \
    "s_nop 0\n" \
    "v_pk_mul_f32 v[48:49], v[48:49], v[64:65] op_sel_hi:[1,0]\n" \
    "v_pk_mul_f32 v[50:51], v[50:51], v[64:65] op_sel_hi:[1,0]\n" \
    "v_pk_mul_f32 v[52:53], v[52:53], v[64:65] op_sel_hi:[1,0]\n" \
    "v_pk_mul_f32 v[54:55], v[54:55], v[64:65] op_sel_hi:[1,0]\n" \
    "v_cvt_pk_bf16_f32 v60, v48, v49\n" \
    "v_cvt_pk_bf16_f32 v61, v50, v51\n" \
    "v_cvt_pk_bf16_f32 v62, v52, v53\n" \
    "v_cvt_pk_bf16_f32 v63, v54, v55\n" \
    "buffer_store_dwordx4 v[60:63], v16, s[36:39], s68 offen sc1\n" \
    "s_cmp_eq_u32 s70, 63\n" \
    "s_cbranch_scc1 H_last_%=\n" \
    "s_waitcnt vmcnt(6) lgkmcnt(0)\n" \
    "s_branch H_next_%=\n" \
    "H_near_%=:\n" \
    "s_add_u32 s69, s70, 2\n" \
    "s_cmp_lt_u32 s77, s69\n" \
    "s_cbranch_scc1 H_fl1_%=\n" \
    "H_fl1_done_%=:\n" \
    "s_sub_u32 s69, s69, 1\n" \
    "s_mul_i32 s68, s69, 24576\n" \
    "s_add_u32 s40, s54, s68\n" \
    "s_add_u32 s41, s40, 8192\n" \
    "s_add_u32 s42, s55, s68\n" \
    "s_add_u32 s43, s56, s67\n" \
    "s_add_u32 s44, s57, s67\n" \
    "s_mov_b32 m0, s43\n" \
    "s_nop 0\n" \
    "buffer_load_dwordx4 v16, s[36:39], s40 offen offset:0 sc1 lds\n" \
    "buffer_load_dwordx4 v16, s[36:39], s40 offen offset:1024 sc1 lds\n" \
    "s_add_u32 m0, s43, 8192\n" \
    "s_nop 0\n" \
    "buffer_load_dwordx4 v16, s[36:39], s41 offen offset:0 sc1 lds\n" \
    "buffer_load_dwordx4 v16, s[36:39], s41 offen offset:1024 sc1 lds\n" \
    "s_mov_b32 m0, s44\n" \
    "s_nop 0\n" \
    "buffer_load_dwordx4 v16, s[36:39], s42 offen offset:0 sc1 lds\n" \
    "s_waitcnt vmcnt(0) lgkmcnt(0)\n" \
    "s_barrier\n" \
    "v_add_u32 v48, s57, v25\n" \
    "ds_read_b128 v[0:3], v25 offset:0\n" \
    "ds_read_b128 v[4:7], v25 offset:4096\n" \
    "ds_read_b128 v[8:11], v25 offset:1024\n" \
    "ds_read_b128 v[12:15], v25 offset:5120\n" \
    "ds_read_b128 v[44:47], v48\n" \
    "s_branch H_fetched_%=\n" \
    "H_late_%=:\n" \
    "s_branch H_pass_%=\n" \
    "H_retry_%=:\n" \
    "s_nop 7\n" \
    "s_nop 7\n" \
    "s_or_b32 s45, s45, 1\n" \
    "v_mov_b64 v[88:89], 0\n" \
    "v_mov_b64 v[90:91], 0\n" \
    "v_mov_b64 v[92:93], 0\n" \
    "v_mov_b64 v[94:95], 0\n" \
    "v_mov_b64 v[96:97], 0\n" \
    "v_mov_b64 v[98:99], 0\n" \
    "v_mov_b64 v[100:101], 0\n" \
    "v_mov_b64 v[102:103], 0\n" \
    "v_mov_b64 v[104:105], 0\n" \
    "v_mov_b64 v[106:107], 0\n" \
    "v_mov_b32 v140, 0\n" \
    "s_add_u32 s82, s82, 8\n" \
    "s_and_b32 s85, s82, 63\n" \
    "s_cmp_lg_u32 s85, 0\n" \
    "s_cbranch_scc1 H_g0_ok_%=\n" \
    "s_mov_b32 s85, 1048832\n" \
    "buffer_load_dword v48, off, s[36:39], s85 sc1\n" \
    "s_waitcnt vmcnt(0)\n" \
    "v_readfirstlane_b32 s85, v48\n" \
    "s_cmp_eq_u32 s85, s63\n" \
    "s_cbranch_scc1 H_end_%=\n" \
    "s_memrealtime s[80:81]\n" \
    "s_waitcnt lgkmcnt(0)\n" \
    "s_sub_u32 s85, s80, s83\n" \
    "s_cmp_gt_u32 s85, 3000000\n" \
    "s_cbranch_scc1 H_giveup_%=\n" \
    "H_g0_ok_%=:\n" \
    "H_recover_%=:\n" \
    "H_msg_%=:\n" \
    "buffer_load_dwordx4 v[48:51], off, s[36:39], s59 sc1\n" \
    "s_waitcnt vmcnt(0)\n" \
    "v_readfirstlane_b32 s68, v48\n" \
    "v_readfirstlane_b32 s69, v49\n" \
    "v_readfirstlane_b32 s71, v50\n" \
    "v_readfirstlane_b32 s79, v51\n" \
    "s_cmp_eq_u32 s68, s63\n" \
    "s_cselect_b32 s69, s69, -1\n" \
    "s_cmp_ge_i32 s69, s70\n" \
    "s_cbranch_scc1 H_msg_d_%=\n" \
    "s_add_u32 s82, s82, 2\n" \
    "s_and_b32 s85, s82, 63\n" \
    "s_cmp_lg_u32 s85, 0\n" \
    "s_cbranch_scc1 H_msg_g_ok_%=\n" \
    "s_mov_b32 s85, 1048832\n" \
    "buffer_load_dword v48, off, s[36:39], s85 sc1\n" \
    "s_waitcnt vmcnt(0)\n" \
    "v_readfirstlane_b32 s85, v48\n" \
    "s_cmp_eq_u32 s85, s63\n" \
    "s_cbranch_scc1 H_end_%=\n" \
    "s_memrealtime s[80:81]\n" \
    "s_waitcnt lgkmcnt(0)\n" \
    "s_sub_u32 s85, s80, s83\n" \
    "s_cmp_gt_u32 s85, 3000000\n" \
    "s_cbranch_scc1 H_giveup_%=\n" \
    "H_msg_g_ok_%=:\n" \
    "s_sleep 1\n" \
    "s_branch H_msg_%=\n" \
    "H_msg_d_%=:\n" \
    "s_bfe_u32 s64, s79, 786440\n" \
    "s_lshr_b32 s65, s79, 20\n" \
    "s_and_b32 s79, s79, 1\n" \
    "s_xor_b32 s78, s79, 1\n" \
    "s_branch H_clock_%=\n" \
    "H_fl1_%=:\n" \
    "s_waitcnt vmcnt(0)\n" \
    "s_memrealtime s[80:81]\n" \
    "s_waitcnt lgkmcnt(0)\n" \
    "s_mov_b32 s83, s80\n" \
    "s_mov_b32 s82, 0\n" \
    "H_fl1_l_%=:\n" \
    "v_cmp_eq_u32 vcc, s63, v26\n" \
    "s_nop 1\n" \
    "v_cndmask_b32 v27, 0, v27, vcc\n" \
    "v_cmp_le_u32 vcc, s69, v27\n" \
    "s_cmp_eq_u64 vcc, exec\n" \
    "s_cbranch_scc1 H_fl1_up_%=\n" \
    "s_add_u32 s82, s82, 1\n" \
    "s_and_b32 s85, s82, 63\n" \
    "s_cmp_lg_u32 s85, 0\n" \
    "s_cbranch_scc1 H_fl1_g_ok_%=\n" \
    "s_mov_b32 s85, 1048832\n" \
    "buffer_load_dword v64, off, s[36:39], s85 sc1\n" \
    "s_waitcnt vmcnt(0)\n" \
    "v_readfirstlane_b32 s85, v64\n" \
    "s_cmp_eq_u32 s85, s63\n" \
    "s_cbranch_scc1 H_end_%=\n" \
    "s_memrealtime s[80:81]\n" \
    "s_waitcnt lgkmcnt(0)\n" \
    "s_sub_u32 s85, s80, s83\n" \
    "s_cmp_gt_u32 s85, 3000000\n" \
    "s_cbranch_scc1 H_giveup_%=\n" \
    "H_fl1_g_ok_%=:\n" \
    "s_sleep 1\n" \
    "buffer_load_dwordx2 v[26:27], v20, s[36:39], 0 offen sc1\n" \
    "s_waitcnt vmcnt(0)\n" \
    "s_branch H_fl1_l_%=\n" \
    "H_fl1_up_%=:\n" \
    "s_mov_b32 s77, s69\n" \
    "s_add_u32 s85, s77, 16\n" \
    "v_cmp_le_u32 vcc, s85, v27\n" \
    "s_cmp_eq_u64 vcc, exec\n" \
    "s_cselect_b32 s77, s85, s77\n" \
    "s_add_u32 s85, s77, 8\n" \
    "v_cmp_le_u32 vcc, s85, v27\n" \
    "s_cmp_eq_u64 vcc, exec\n" \
    "s_cselect_b32 s77, s85, s77\n" \
    "s_add_u32 s85, s77, 4\n" \
    "v_cmp_le_u32 vcc, s85, v27\n" \
    "s_cmp_eq_u64 vcc, exec\n" \
    "s_cselect_b32 s77, s85, s77\n" \
    "s_add_u32 s85, s77, 2\n" \
    "v_cmp_le_u32 vcc, s85, v27\n" \
    "s_cmp_eq_u64 vcc, exec\n" \
    "s_cselect_b32 s77, s85, s77\n" \
    "s_add_u32 s85, s77, 1\n" \
    "v_cmp_le_u32 vcc, s85, v27\n" \
    "s_cmp_eq_u64 vcc, exec\n" \
    "s_cselect_b32 s77, s85, s77\n" \
    "s_branch H_fl1_done_%=\n" \
    "H_fl2_%=:\n" \
    "s_waitcnt vmcnt(0)\n" \
    "s_memrealtime s[80:81]\n" \
    "s_waitcnt lgkmcnt(0)\n" \
    "s_mov_b32 s83, s80\n" \
    "s_mov_b32 s82, 0\n" \
    "H_fl2_l_%=:\n" \
    "v_cmp_eq_u32 vcc, s63, v26\n" \
    "s_nop 1\n" \
    "v_cndmask_b32 v27, 0, v27, vcc\n" \
    "v_cmp_le_u32 vcc, s69, v27\n" \
    "s_cmp_eq_u64 vcc, exec\n" \
    "s_cbranch_scc1 H_fl2_up_%=\n" \
    "s_add_u32 s82, s82, 1\n" \
    "s_and_b32 s85, s82, 63\n" \
    "s_cmp_lg_u32 s85, 0\n" \
    "s_cbranch_scc1 H_fl2_g_ok_%=\n" \
    "s_mov_b32 s85, 1048832\n" \
    "buffer_load_dword v49, off, s[36:39], s85 sc1\n" \
    "s_waitcnt vmcnt(0)\n" \
    "v_readfirstlane_b32 s85, v49\n" \
    "s_cmp_eq_u32 s85, s63\n" \
    "s_cbranch_scc1 H_end_%=\n" \
    "s_memrealtime s[80:81]\n" \
    "s_waitcnt lgkmcnt(0)\n" \
    "s_sub_u32 s85, s80, s83\n" \
    "s_cmp_gt_u32 s85, 3000000\n" \
    "s_cbranch_scc1 H_giveup_%=\n" \
    "H_fl2_g_ok_%=:\n" \
    "s_sleep 1\n" \
    "buffer_load_dwordx2 v[26:27], v20, s[36:39], 0 offen sc1\n" \
    "s_waitcnt vmcnt(0)\n" \
    "s_branch H_fl2_l_%=\n" \
    "H_fl2_up_%=:\n" \
    "s_mov_b32 s77, s69\n" \
    "s_add_u32 s85, s77, 16\n" \
    "v_cmp_le_u32 vcc, s85, v27\n" \
    "s_cmp_eq_u64 vcc, exec\n" \
    "s_cselect_b32 s77, s85, s77\n" \
    "s_add_u32 s85, s77, 8\n" \
    "v_cmp_le_u32 vcc, s85, v27\n" \
    "s_cmp_eq_u64 vcc, exec\n" \
    "s_cselect_b32 s77, s85, s77\n" \
    "s_add_u32 s85, s77, 4\n" \
    "v_cmp_le_u32 vcc, s85, v27\n" \
    "s_cmp_eq_u64 vcc, exec\n" \
    "s_cselect_b32 s77, s85, s77\n" \
    "s_add_u32 s85, s77, 2\n" \
    "v_cmp_le_u32 vcc, s85, v27\n" \
    "s_cmp_eq_u64 vcc, exec\n" \
    "s_cselect_b32 s77, s85, s77\n" \
    "s_add_u32 s85, s77, 1\n" \
    "v_cmp_le_u32 vcc, s85, v27\n" \
    "s_cmp_eq_u64 vcc, exec\n" \
    "s_cselect_b32 s77, s85, s77\n" \
    "s_branch H_fl2_done_%=\n" \
    "H_start_%=:\n" \
    "v_mov_b64 v[88:89], 0\n" \
    "v_mov_b64 v[90:91], 0\n" \
    "v_mov_b64 v[92:93], 0\n" \
    "v_mov_b64 v[94:95], 0\n" \
    "v_mov_b64 v[96:97], 0\n" \
    "v_mov_b64 v[98:99], 0\n" \
    "v_mov_b64 v[100:101], 0\n" \
    "v_mov_b64 v[102:103], 0\n" \
    "v_mov_b64 v[104:105], 0\n" \
    "v_mov_b64 v[106:107], 0\n" \
    "v_mov_b32 v140, 0\n" \
    "s_memrealtime s[80:81]\n" \
    "s_waitcnt lgkmcnt(0)\n" \
    "s_mov_b32 s83, s80\n" \
    "s_mov_b32 s82, 0\n" \
    "H_start_l_%=:\n" \
    "buffer_load_dwordx4 v[64:67], off, s[36:39], s53 sc1\n" \
    "s_waitcnt vmcnt(0)\n" \
    "v_readfirstlane_b32 s85, v64\n" \
    "v_readfirstlane_b32 s71, v65\n" \
    "v_readfirstlane_b32 s64, v66\n" \
    "v_readfirstlane_b32 s60, v67\n" \
    "s_cmp_eq_u32 s85, s63\n" \
    "s_cbranch_scc1 H_started_%=\n" \
    "s_add_u32 s82, s82, 1\n" \
    "s_and_b32 s85, s82, 63\n" \
    "s_cmp_lg_u32 s85, 0\n" \
    "s_cbranch_scc1 H_g3_ok_%=\n" \
    "s_mov_b32 s85, 1048832\n" \
    "buffer_load_dword v64, off, s[36:39], s85 sc1\n" \
    "s_waitcnt vmcnt(0)\n" \
    "v_readfirstlane_b32 s85, v64\n" \
    "s_cmp_eq_u32 s85, s63\n" \
    "s_cbranch_scc1 H_end_%=\n" \
    "s_memrealtime s[80:81]\n" \
    "s_waitcnt lgkmcnt(0)\n" \
    "s_sub_u32 s85, s80, s83\n" \
    "s_cmp_gt_u32 s85, 3000000\n" \
    "s_cbranch_scc1 H_giveup_%=\n" \
    "H_g3_ok_%=:\n" \
    "s_sleep 1\n" \
    "s_branch H_start_l_%=\n" \
    "H_started_%=:\n" \
    "s_lshr_b32 s65, s64, 16\n" \
    "s_and_b32 s64, s64, 0xffff\n" \
    "s_and_b32 s43, s60, 16384\n" \
    "s_and_b32 s60, s60, 8192\n" \
    "s_add_u32 s41, s60, s43\n" \
    "s_xor_b32 s40, s41, 16384\n" \
    "s_add_u32 s42, s60, 4096\n" \
    "s_xor_b32 s43, s43, 16384\n" \
    "s_cmp_eq_u32 s61, 0\n" \
    "s_cselect_b32 s46, s40, s41\n" \
    "s_cselect_b32 s47, s41, s40\n" \
    "s_cselect_b32 s74, 0, 4096\n" \
    "s_mov_b32 s61, s42\n" \
    "v_xor_b32 v18, s43, v18\n" \
    "v_add_u32 v18, s60, v18\n" \
    "s_add_u32 s59, s59, s60\n" \
    "s_sub_u32 s51, s59, 4096\n" \
    "s_xor_b32 s75, s74, 4096\n" \
    "s_add_u32 s74, s74, 1114112\n" \
    "s_add_u32 s75, s75, 1114112\n" \
    "s_mov_b32 s76, 1122304\n" \
    "s_branch H_based_%=\n" \
    "H_last_%=:\n" \
    "v_mov_b32 v50, s63\n" \
    "v_mov_b32 v51, 64\n" \
    "s_waitcnt vmcnt(0)\n" \
    "buffer_store_dwordx2 v[50:51], v19, s[36:39], 0 offen offset:64 sc1\n" \
    "s_endpgm\n" \
    "H_giveup_%=:\n" \
    "s_mov_b32 s85, 1048832\n" \
    "v_mov_b32 v64, s63\n" \
    "buffer_store_dword v64, off, s[36:39], s85 sc1\n" \
    "H_end_%=:\n" \
    "s_endpgm\n" \
    "rmt_code_end:\n"

#define COL_ASM \
    "s_nop 4\n" \
    "s_add_u32 s68, s56, 0\n" \
    "s_add_u32 m0, s52, 0\n" \
    "s_nop 0\n" \
    "buffer_load_dwordx4 v0, s[40:43], s68 offen lds\n" \
    "s_add_u32 m0, s52, 1024\n" \
    "s_nop 0\n" \
    "buffer_load_dwordx4 v1, s[40:43], s68 offen lds\n" \
    "s_add_u32 s68, s56, 0\n" \
    "s_add_u32 m0, s52, 2048\n" \
    "s_nop 0\n" \
    "buffer_load_dwordx4 v2, s[40:43], s68 offen lds\n" \
    "s_add_u32 m0, s52, 3072\n" \
    "s_nop 0\n" \
    "buffer_load_dwordx4 v3, s[40:43], s68 offen lds\n" \
    "s_add_u32 s68, s56, 81920\n" \
    "s_add_u32 m0, s52, 4096\n" \
    "s_nop 0\n" \
    "buffer_load_dwordx4 v0, s[40:43], s68 offen lds\n" \
    "s_add_u32 m0, s52, 5120\n" \
    "s_nop 0\n" \
    "buffer_load_dwordx4 v1, s[40:43], s68 offen lds\n" \
    "s_add_u32 s68, s56, 81920\n" \
    "s_add_u32 m0, s52, 6144\n" \
    "s_nop 0\n" \
    "buffer_load_dwordx4 v2, s[40:43], s68 offen lds\n" \
    "s_add_u32 m0, s52, 7168\n" \
    "s_nop 0\n" \
    "buffer_load_dwordx4 v3, s[40:43], s68 offen lds\n" \
    "s_add_u32 s68, s56, 163840\n" \
    "s_add_u32 m0, s52, 8192\n" \
    "s_nop 0\n" \
    "buffer_load_dwordx4 v0, s[40:43], s68 offen lds\n" \
    "s_add_u32 m0, s52, 9216\n" \
    "s_nop 0\n" \
    "buffer_load_dwordx4 v1, s[40:43], s68 offen lds\n" \
    "s_add_u32 s68, s56, 163840\n" \
    "s_add_u32 m0, s52, 10240\n" \
    "s_nop 0\n" \
    "buffer_load_dwordx4 v2, s[40:43], s68 offen lds\n" \
    "s_add_u32 m0, s52, 11264\n" \
    "s_nop 0\n" \
    "buffer_load_dwordx4 v3, s[40:43], s68 offen lds\n" \
    "s_add_u32 s68, s56, 245760\n" \
    "s_add_u32 m0, s52, 12288\n" \
    "s_nop 0\n" \
    "buffer_load_dwordx4 v0, s[40:43], s68 offen lds\n" \
    "s_add_u32 m0, s52, 13312\n" \
    "s_nop 0\n" \
    "buffer_load_dwordx4 v1, s[40:43], s68 offen lds\n" \
    "s_add_u32 s68, s56, 245760\n" \
    "s_add_u32 m0, s52, 14336\n" \
    "s_nop 0\n" \
    "buffer_load_dwordx4 v2, s[40:43], s68 offen lds\n" \
    "s_add_u32 m0, s52, 15360\n" \
    "s_nop 0\n" \
    "buffer_load_dwordx4 v3, s[40:43], s68 offen lds\n" \
    "s_add_u32 s68, s56, 327680\n" \
    "s_add_u32 m0, s52, 16384\n" \
    "s_nop 0\n" \
    "buffer_load_dwordx4 v0, s[40:43], s68 offen lds\n" \
    "s_add_u32 m0, s52, 17408\n" \
    "s_nop 0\n" \
    "buffer_load_dwordx4 v1, s[40:43], s68 offen lds\n" \
    "s_add_u32 s68, s56, 327680\n" \
    "s_add_u32 m0, s52, 18432\n" \
    "s_nop 0\n" \
    "buffer_load_dwordx4 v2, s[40:43], s68 offen lds\n" \
    "s_add_u32 m0, s52, 19456\n" \
    "s_nop 0\n" \
    "buffer_load_dwordx4 v3, s[40:43], s68 offen lds\n" \
    "s_add_u32 s68, s56, 409600\n" \
    "s_add_u32 m0, s52, 20480\n" \
    "s_nop 0\n" \
    "buffer_load_dwordx4 v0, s[40:43], s68 offen lds\n" \
    "s_add_u32 m0, s52, 21504\n" \
    "s_nop 0\n" \
    "buffer_load_dwordx4 v1, s[40:43], s68 offen lds\n" \
    "s_add_u32 s68, s56, 409600\n" \
    "s_add_u32 m0, s52, 22528\n" \
    "s_nop 0\n" \
    "buffer_load_dwordx4 v2, s[40:43], s68 offen lds\n" \
    "s_add_u32 m0, s52, 23552\n" \
    "s_nop 0\n" \
    "buffer_load_dwordx4 v3, s[40:43], s68 offen lds\n" \
    "s_add_u32 s68, s56, 491520\n" \
    "s_add_u32 m0, s52, 24576\n" \
    "s_nop 0\n" \
    "buffer_load_dwordx4 v0, s[40:43], s68 offen lds\n" \
    "s_add_u32 m0, s52, 25600\n" \
    "s_nop 0\n" \
    "buffer_load_dwordx4 v1, s[40:43], s68 offen lds\n" \
    "s_add_u32 s68, s56, 491520\n" \
    "s_add_u32 m0, s52, 26624\n" \
    "s_nop 0\n" \
    "buffer_load_dwordx4 v2, s[40:43], s68 offen lds\n" \
    "s_add_u32 m0, s52, 27648\n" \
    "s_nop 0\n" \
    "buffer_load_dwordx4 v3, s[40:43], s68 offen lds\n" \
    "s_add_u32 s68, s56, 573440\n" \
    "s_add_u32 m0, s52, 28672\n" \
    "s_nop 0\n" \
    "buffer_load_dwordx4 v0, s[40:43], s68 offen lds\n" \
    "s_add_u32 m0, s52, 29696\n" \
    "s_nop 0\n" \
    "buffer_load_dwordx4 v1, s[40:43], s68 offen lds\n" \
    "s_add_u32 s68, s56, 573440\n" \
    "s_add_u32 m0, s52, 30720\n" \
    "s_nop 0\n" \
    "buffer_load_dwordx4 v2, s[40:43], s68 offen lds\n" \
    "s_add_u32 m0, s52, 31744\n" \
    "s_nop 0\n" \
    "buffer_load_dwordx4 v3, s[40:43], s68 offen lds\n" \
    "s_waitcnt vmcnt(30)\n" \
    "ds_read_b64_tr_b16 a[0:1], v6 offset:0\n" \
    "ds_read_b64_tr_b16 a[2:3], v7 offset:0\n" \
    "ds_read_b64_tr_b16 a[8:9], v8 offset:0\n" \
    "ds_read_b64_tr_b16 a[10:11], v9 offset:0\n" \
    "s_waitcnt lgkmcnt(0)\n" \
    "s_add_u32 s68, s56, 655360\n" \
    "s_add_u32 m0, s52, 0\n" \
    "s_nop 0\n" \
    "buffer_load_dwordx4 v0, s[40:43], s68 offen lds\n" \
    "s_add_u32 m0, s52, 1024\n" \
    "s_nop 0\n" \
    "buffer_load_dwordx4 v1, s[40:43], s68 offen lds\n" \
    "s_waitcnt vmcnt(30)\n" \
    "ds_read_b64_tr_b16 a[4:5], v6 offset:2048\n" \
    "ds_read_b64_tr_b16 a[6:7], v7 offset:2048\n" \
    "ds_read_b64_tr_b16 a[12:13], v8 offset:2048\n" \
    "ds_read_b64_tr_b16 a[14:15], v9 offset:2048\n" \
    "s_waitcnt lgkmcnt(0)\n" \
    "s_add_u32 s68, s56, 655360\n" \
    "s_add_u32 m0, s52, 2048\n" \
    "s_nop 0\n" \
    "buffer_load_dwordx4 v2, s[40:43], s68 offen lds\n" \
    "s_add_u32 m0, s52, 3072\n" \
    "s_nop 0\n" \
    "buffer_load_dwordx4 v3, s[40:43], s68 offen lds\n" \
    "s_waitcnt vmcnt(30)\n" \
    "ds_read_b64_tr_b16 a[16:17], v6 offset:4096\n" \
    "ds_read_b64_tr_b16 a[18:19], v7 offset:4096\n" \
    "ds_read_b64_tr_b16 a[24:25], v8 offset:4096\n" \
    "ds_read_b64_tr_b16 a[26:27], v9 offset:4096\n" \
    "s_waitcnt lgkmcnt(0)\n" \
    "s_add_u32 s68, s56, 737280\n" \
    "s_add_u32 m0, s52, 4096\n" \
    "s_nop 0\n" \
    "buffer_load_dwordx4 v0, s[40:43], s68 offen lds\n" \
    "s_add_u32 m0, s52, 5120\n" \
    "s_nop 0\n" \
    "buffer_load_dwordx4 v1, s[40:43], s68 offen lds\n" \
    "s_waitcnt vmcnt(30)\n" \
    "ds_read_b64_tr_b16 a[20:21], v6 offset:6144\n" \
    "ds_read_b64_tr_b16 a[22:23], v7 offset:6144\n" \
    "ds_read_b64_tr_b16 a[28:29], v8 offset:6144\n" \
    "ds_read_b64_tr_b16 a[30:31], v9 offset:6144\n" \
    "s_waitcnt lgkmcnt(0)\n" \
    "s_add_u32 s68, s56, 737280\n" \
    "s_add_u32 m0, s52, 6144\n" \
    "s_nop 0\n" \
    "buffer_load_dwordx4 v2, s[40:43], s68 offen lds\n" \
    "s_add_u32 m0, s52, 7168\n" \
    "s_nop 0\n" \
    "buffer_load_dwordx4 v3, s[40:43], s68 offen lds\n" \
    "s_waitcnt vmcnt(30)\n" \
    "ds_read_b64_tr_b16 a[32:33], v6 offset:8192\n" \
    "ds_read_b64_tr_b16 a[34:35], v7 offset:8192\n" \
    "ds_read_b64_tr_b16 a[40:41], v8 offset:8192\n" \
    "ds_read_b64_tr_b16 a[42:43], v9 offset:8192\n" \
    "s_waitcnt lgkmcnt(0)\n" \
    "s_add_u32 s68, s56, 819200\n" \
    "s_add_u32 m0, s52, 8192\n" \
    "s_nop 0\n" \
    "buffer_load_dwordx4 v0, s[40:43], s68 offen lds\n" \
    "s_add_u32 m0, s52, 9216\n" \
    "s_nop 0\n" \
    "buffer_load_dwordx4 v1, s[40:43], s68 offen lds\n" \
    "s_waitcnt vmcnt(30)\n" \
    "ds_read_b64_tr_b16 a[36:37], v6 offset:10240\n" \
    "ds_read_b64_tr_b16 a[38:39], v7 offset:10240\n" \
    "ds_read_b64_tr_b16 a[44:45], v8 offset:10240\n" \
    "ds_read_b64_tr_b16 a[46:47], v9 offset:10240\n" \
    "s_waitcnt lgkmcnt(0)\n" \
    "s_add_u32 s68, s56, 819200\n" \
    "s_add_u32 m0, s52, 10240\n" \
    "s_nop 0\n" \
    "buffer_load_dwordx4 v2, s[40:43], s68 offen lds\n" \
    "s_add_u32 m0, s52, 11264\n" \
    "s_nop 0\n" \
    "buffer_load_dwordx4 v3, s[40:43], s68 offen lds\n" \
    "s_waitcnt vmcnt(30)\n" \
    "ds_read_b64_tr_b16 a[48:49], v6 offset:12288\n" \
    "ds_read_b64_tr_b16 a[50:51], v7 offset:12288\n" \
    "ds_read_b64_tr_b16 a[56:57], v8 offset:12288\n" \
    "ds_read_b64_tr_b16 a[58:59], v9 offset:12288\n" \
    "s_waitcnt lgkmcnt(0)\n" \
    "s_add_u32 s68, s56, 901120\n" \
    "s_add_u32 m0, s52, 12288\n" \
    "s_nop 0\n" \
    "buffer_load_dwordx4 v0, s[40:43], s68 offen lds\n" \
    "s_add_u32 m0, s52, 13312\n" \
    "s_nop 0\n" \
    "buffer_load_dwordx4 v1, s[40:43], s68 offen lds\n" \
    "s_waitcnt vmcnt(30)\n" \
    "ds_read_b64_tr_b16 a[52:53], v6 offset:14336\n" \
    "ds_read_b64_tr_b16 a[54:55], v7 offset:14336\n" \
    "ds_read_b64_tr_b16 a[60:61], v8 offset:14336\n" \
    "ds_read_b64_tr_b16 a[62:63], v9 offset:14336\n" \
    "s_waitcnt lgkmcnt(0)\n" \
    "s_add_u32 s68, s56, 901120\n" \
    "s_add_u32 m0, s52, 14336\n" \
    "s_nop 0\n" \
    "buffer_load_dwordx4 v2, s[40:43], s68 offen lds\n" \
    "s_add_u32 m0, s52, 15360\n" \
    "s_nop 0\n" \
    "buffer_load_dwordx4 v3, s[40:43], s68 offen lds\n" \
    "s_waitcnt vmcnt(30)\n" \
    "ds_read_b64_tr_b16 a[64:65], v6 offset:16384\n" \
    "ds_read_b64_tr_b16 a[66:67], v7 offset:16384\n" \
    "ds_read_b64_tr_b16 a[72:73], v8 offset:16384\n" \
    "ds_read_b64_tr_b16 a[74:75], v9 offset:16384\n" \
    "s_waitcnt lgkmcnt(0)\n" \
    "s_add_u32 s68, s56, 983040\n" \
    "s_add_u32 m0, s52, 16384\n" \
    "s_nop 0\n" \
    "buffer_load_dwordx4 v0, s[40:43], s68 offen lds\n" \
    "s_add_u32 m0, s52, 17408\n" \
    "s_nop 0\n" \
    "buffer_load_dwordx4 v1, s[40:43], s68 offen lds\n" \
    "s_waitcnt vmcnt(30)\n" \
    "ds_read_b64_tr_b16 a[68:69], v6 offset:18432\n" \
    "ds_read_b64_tr_b16 a[70:71], v7 offset:18432\n" \
    "ds_read_b64_tr_b16 a[76:77], v8 offset:18432\n" \
    "ds_read_b64_tr_b16 a[78:79], v9 offset:18432\n" \
    "s_waitcnt lgkmcnt(0)\n" \
    "s_add_u32 s68, s56, 983040\n" \
    "s_add_u32 m0, s52, 18432\n" \
    "s_nop 0\n" \
    "buffer_load_dwordx4 v2, s[40:43], s68 offen lds\n" \
    "s_add_u32 m0, s52, 19456\n" \
    "s_nop 0\n" \
    "buffer_load_dwordx4 v3, s[40:43], s68 offen lds\n" \
    "s_waitcnt vmcnt(30)\n" \
    "ds_read_b64_tr_b16 a[80:81], v6 offset:20480\n" \
    "ds_read_b64_tr_b16 a[82:83], v7 offset:20480\n" \
    "ds_read_b64_tr_b16 a[88:89], v8 offset:20480\n" \
    "ds_read_b64_tr_b16 a[90:91], v9 offset:20480\n" \
    "s_waitcnt lgkmcnt(0)\n" \
    "s_add_u32 s68, s56, 1064960\n" \
    "s_add_u32 m0, s52, 20480\n" \
    "s_nop 0\n" \
    "buffer_load_dwordx4 v0, s[40:43], s68 offen lds\n" \
    "s_add_u32 m0, s52, 21504\n" \
    "s_nop 0\n" \
    "buffer_load_dwordx4 v1, s[40:43], s68 offen lds\n" \
    "s_waitcnt vmcnt(30)\n" \
    "ds_read_b64_tr_b16 a[84:85], v6 offset:22528\n" \
    "ds_read_b64_tr_b16 a[86:87], v7 offset:22528\n" \
    "ds_read_b64_tr_b16 a[92:93], v8 offset:22528\n" \
    "ds_read_b64_tr_b16 a[94:95], v9 offset:22528\n" \
    "s_waitcnt lgkmcnt(0)\n" \
    "s_add_u32 s68, s56, 1064960\n" \
    "s_add_u32 m0, s52, 22528\n" \
    "s_nop 0\n" \
    "buffer_load_dwordx4 v2, s[40:43], s68 offen lds\n" \
    "s_add_u32 m0, s52, 23552\n" \
    "s_nop 0\n" \
    "buffer_load_dwordx4 v3, s[40:43], s68 offen lds\n" \
    "s_waitcnt vmcnt(30)\n" \
    "ds_read_b64_tr_b16 a[96:97], v6 offset:24576\n" \
    "ds_read_b64_tr_b16 a[98:99], v7 offset:24576\n" \
    "ds_read_b64_tr_b16 a[104:105], v8 offset:24576\n" \
    "ds_read_b64_tr_b16 a[106:107], v9 offset:24576\n" \
    "s_waitcnt lgkmcnt(0)\n" \
    "s_add_u32 s68, s56, 1146880\n" \
    "s_add_u32 m0, s52, 24576\n" \
    "s_nop 0\n" \
    "buffer_load_dwordx4 v0, s[40:43], s68 offen lds\n" \
    "s_add_u32 m0, s52, 25600\n" \
    "s_nop 0\n" \
    "buffer_load_dwordx4 v1, s[40:43], s68 offen lds\n" \
    "s_waitcnt vmcnt(30)\n" \
    "ds_read_b64_tr_b16 a[100:101], v6 offset:26624\n" \
    "ds_read_b64_tr_b16 a[102:103], v7 offset:26624\n" \
    "ds_read_b64_tr_b16 a[108:109], v8 offset:26624\n" \
    "ds_read_b64_tr_b16 a[110:111], v9 offset:26624\n" \
    "s_waitcnt lgkmcnt(0)\n" \
    "s_add_u32 s68, s56, 1146880\n" \
    "s_add_u32 m0, s52, 26624\n" \
    "s_nop 0\n" \
    "buffer_load_dwordx4 v2, s[40:43], s68 offen lds\n" \
    "s_add_u32 m0, s52, 27648\n" \
    "s_nop 0\n" \
    "buffer_load_dwordx4 v3, s[40:43], s68 offen lds\n" \
    "s_waitcnt vmcnt(30)\n" \
    "ds_read_b64_tr_b16 a[112:113], v6 offset:28672\n" \
    "ds_read_b64_tr_b16 a[114:115], v7 offset:28672\n" \
    "ds_read_b64_tr_b16 a[120:121], v8 offset:28672\n" \
    "ds_read_b64_tr_b16 a[122:123], v9 offset:28672\n" \
    "s_waitcnt lgkmcnt(0)\n" \
    "s_add_u32 s68, s56, 1228800\n" \
    "s_add_u32 m0, s52, 28672\n" \
    "s_nop 0\n" \
    "buffer_load_dwordx4 v0, s[40:43], s68 offen lds\n" \
    "s_add_u32 m0, s52, 29696\n" \
    "s_nop 0\n" \
    "buffer_load_dwordx4 v1, s[40:43], s68 offen lds\n" \
    "s_waitcnt vmcnt(30)\n" \
    "ds_read_b64_tr_b16 a[116:117], v6 offset:30720\n" \
    "ds_read_b64_tr_b16 a[118:119], v7 offset:30720\n" \
    "ds_read_b64_tr_b16 a[124:125], v8 offset:30720\n" \
    "ds_read_b64_tr_b16 a[126:127], v9 offset:30720\n" \
    "s_waitcnt lgkmcnt(0)\n" \
    "s_add_u32 s68, s56, 1228800\n" \
    "s_add_u32 m0, s52, 30720\n" \
    "s_nop 0\n" \
    "buffer_load_dwordx4 v2, s[40:43], s68 offen lds\n" \
    "s_add_u32 m0, s52, 31744\n" \
    "s_nop 0\n" \
    "buffer_load_dwordx4 v3, s[40:43], s68 offen lds\n" \
    "s_waitcnt vmcnt(30)\n" \
    "ds_read_b64_tr_b16 a[128:129], v6 offset:0\n" \
    "ds_read_b64_tr_b16 a[130:131], v7 offset:0\n" \
    "ds_read_b64_tr_b16 a[136:137], v8 offset:0\n" \
    "ds_read_b64_tr_b16 a[138:139], v9 offset:0\n" \
    "s_waitcnt lgkmcnt(0)\n" \
    "s_waitcnt vmcnt(28)\n" \
    "ds_read_b64_tr_b16 a[132:133], v6 offset:2048\n" \
    "ds_read_b64_tr_b16 a[134:135], v7 offset:2048\n" \
    "ds_read_b64_tr_b16 a[140:141], v8 offset:2048\n" \
    "ds_read_b64_tr_b16 a[142:143], v9 offset:2048\n" \
    "s_waitcnt lgkmcnt(0)\n" \
    "s_waitcnt vmcnt(26)\n" \
    "ds_read_b64_tr_b16 a[144:145], v6 offset:4096\n" \
    "ds_read_b64_tr_b16 a[146:147], v7 offset:4096\n" \
    "ds_read_b64_tr_b16 a[152:153], v8 offset:4096\n" \
    "ds_read_b64_tr_b16 a[154:155], v9 offset:4096\n" \
    "s_waitcnt lgkmcnt(0)\n" \
    "s_waitcnt vmcnt(24)\n" \
    "ds_read_b64_tr_b16 a[148:149], v6 offset:6144\n" \
    "ds_read_b64_tr_b16 a[150:151], v7 offset:6144\n" \
    "ds_read_b64_tr_b16 a[156:157], v8 offset:6144\n" \
    "ds_read_b64_tr_b16 a[158:159], v9 offset:6144\n" \
    "s_waitcnt lgkmcnt(0)\n" \
    "s_waitcnt vmcnt(22)\n" \
    "ds_read_b64_tr_b16 a[160:161], v6 offset:8192\n" \
    "ds_read_b64_tr_b16 a[162:163], v7 offset:8192\n" \
    "ds_read_b64_tr_b16 a[168:169], v8 offset:8192\n" \
    "ds_read_b64_tr_b16 a[170:171], v9 offset:8192\n" \
    "s_waitcnt lgkmcnt(0)\n" \
    "s_waitcnt vmcnt(20)\n" \
    "ds_read_b64_tr_b16 a[164:165], v6 offset:10240\n" \
    "ds_read_b64_tr_b16 a[166:167], v7 offset:10240\n" \
    "ds_read_b64_tr_b16 a[172:173], v8 offset:10240\n" \
    "ds_read_b64_tr_b16 a[174:175], v9 offset:10240\n" \
    "s_waitcnt lgkmcnt(0)\n" \
    "s_waitcnt vmcnt(18)\n" \
    "ds_read_b64_tr_b16 a[176:177], v6 offset:12288\n" \
    "ds_read_b64_tr_b16 a[178:179], v7 offset:12288\n" \
    "ds_read_b64_tr_b16 a[184:185], v8 offset:12288\n" \
    "ds_read_b64_tr_b16 a[186:187], v9 offset:12288\n" \
    "s_waitcnt lgkmcnt(0)\n" \
    "s_waitcnt vmcnt(16)\n" \
    "ds_read_b64_tr_b16 a[180:181], v6 offset:14336\n" \
    "ds_read_b64_tr_b16 a[182:183], v7 offset:14336\n" \
    "ds_read_b64_tr_b16 a[188:189], v8 offset:14336\n" \
    "ds_read_b64_tr_b16 a[190:191], v9 offset:14336\n" \
    "s_waitcnt lgkmcnt(0)\n" \
    "s_waitcnt vmcnt(14)\n" \
    "ds_read_b64_tr_b16 a[192:193], v6 offset:16384\n" \
    "ds_read_b64_tr_b16 a[194:195], v7 offset:16384\n" \
    "ds_read_b64_tr_b16 a[200:201], v8 offset:16384\n" \
    "ds_read_b64_tr_b16 a[202:203], v9 offset:16384\n" \
    "s_waitcnt lgkmcnt(0)\n" \
    "s_waitcnt vmcnt(12)\n" \
    "ds_read_b64_tr_b16 a[196:197], v6 offset:18432\n" \
    "ds_read_b64_tr_b16 a[198:199], v7 offset:18432\n" \
    "ds_read_b64_tr_b16 a[204:205], v8 offset:18432\n" \
    "ds_read_b64_tr_b16 a[206:207], v9 offset:18432\n" \
    "s_waitcnt lgkmcnt(0)\n" \
    "s_waitcnt vmcnt(10)\n" \
    "ds_read_b64_tr_b16 a[208:209], v6 offset:20480\n" \
    "ds_read_b64_tr_b16 a[210:211], v7 offset:20480\n" \
    "ds_read_b64_tr_b16 a[216:217], v8 offset:20480\n" \
    "ds_read_b64_tr_b16 a[218:219], v9 offset:20480\n" \
    "s_waitcnt lgkmcnt(0)\n" \
    "v_mov_b32 v12, s63\n" \
    "buffer_store_dword v12, v13, s[36:39], 0 offen sc1\n" \
    "s_waitcnt vmcnt(9)\n" \
    "ds_read_b64_tr_b16 a[212:213], v6 offset:22528\n" \
    "ds_read_b64_tr_b16 a[214:215], v7 offset:22528\n" \
    "ds_read_b64_tr_b16 a[220:221], v8 offset:22528\n" \
    "ds_read_b64_tr_b16 a[222:223], v9 offset:22528\n" \
    "s_waitcnt lgkmcnt(0)\n" \
    "s_waitcnt vmcnt(7)\n" \
    "ds_read_b64_tr_b16 a[224:225], v6 offset:24576\n" \
    "ds_read_b64_tr_b16 a[226:227], v7 offset:24576\n" \
    "ds_read_b64_tr_b16 a[232:233], v8 offset:24576\n" \
    "ds_read_b64_tr_b16 a[234:235], v9 offset:24576\n" \
    "s_waitcnt lgkmcnt(0)\n" \
    "s_waitcnt vmcnt(5)\n" \
    "ds_read_b64_tr_b16 a[228:229], v6 offset:26624\n" \
    "ds_read_b64_tr_b16 a[230:231], v7 offset:26624\n" \
    "ds_read_b64_tr_b16 a[236:237], v8 offset:26624\n" \
    "ds_read_b64_tr_b16 a[238:239], v9 offset:26624\n" \
    "s_waitcnt lgkmcnt(0)\n" \
    "s_waitcnt vmcnt(3)\n" \
    "ds_read_b64_tr_b16 a[240:241], v6 offset:28672\n" \
    "ds_read_b64_tr_b16 a[242:243], v7 offset:28672\n" \
    "ds_read_b64_tr_b16 a[248:249], v8 offset:28672\n" \
    "ds_read_b64_tr_b16 a[250:251], v9 offset:28672\n" \
    "s_waitcnt lgkmcnt(0)\n" \
    "s_waitcnt vmcnt(1)\n" \
    "ds_read_b64_tr_b16 a[244:245], v6 offset:30720\n" \
    "ds_read_b64_tr_b16 a[246:247], v7 offset:30720\n" \
    "ds_read_b64_tr_b16 a[252:253], v8 offset:30720\n" \
    "ds_read_b64_tr_b16 a[254:255], v9 offset:30720\n" \
    "s_waitcnt lgkmcnt(0)\n" \
    "s_barrier\n" \
    "s_mov_b32 s72, 0\n" \
    "s_mov_b32 s73, 163840\n" \
    "s_mov_b32 s70, 0\n" \
    "s_mov_b32 s76, 0\n" \
    "s_mov_b32 s78, 0\n" \
    "s_mov_b32 s44, -1\n" \
    "s_mov_b32 s45, 0\n" \
    "s_memrealtime s[80:81]\n" \
    "s_waitcnt lgkmcnt(0)\n" \
    "s_mov_b32 s83, s80\n" \
    "s_mov_b32 s82, 0\n" \
    "C_start_l_%=:\n" \
    "buffer_load_dwordx4 v[0:3], off, s[36:39], s53 sc1\n" \
    "s_waitcnt vmcnt(0)\n" \
    "v_readfirstlane_b32 s85, v0\n" \
    "v_readfirstlane_b32 s71, v1\n" \
    "v_readfirstlane_b32 s64, v2\n" \
    "v_readfirstlane_b32 s60, v3\n" \
    "s_cmp_eq_u32 s85, s63\n" \
    "s_cbranch_scc1 C_started_%=\n" \
    "s_add_u32 s82, s82, 1\n" \
    "s_and_b32 s85, s82, 63\n" \
    "s_cmp_lg_u32 s85, 0\n" \
    "s_cbranch_scc1 C_g3_ok_%=\n" \
    "s_mov_b32 s85, 1048832\n" \
    "buffer_load_dword v0, off, s[36:39], s85 sc1\n" \
    "s_waitcnt vmcnt(0)\n" \
    "v_readfirstlane_b32 s85, v0\n" \
    "s_cmp_eq_u32 s85, s63\n" \
    "s_cbranch_scc1 C_end_%=\n" \
    "s_memrealtime s[80:81]\n" \
    "s_waitcnt lgkmcnt(0)\n" \
    "s_sub_u32 s85, s80, s83\n" \
    "s_cmp_gt_u32 s85, 3000000\n" \
    "s_cbranch_scc1 C_giveup_%=\n" \
    "C_g3_ok_%=:\n" \
    "s_sleep 1\n" \
    "s_branch C_start_l_%=\n" \
    "C_started_%=:\n" \
    "s_lshr_b32 s65, s64, 16\n" \
    "s_and_b32 s64, s64, 0xffff\n" \
    "s_and_b32 s43, s60, 16384\n" \
    "s_and_b32 s60, s60, 8192\n" \
    "s_add_u32 s41, s60, s43\n" \
    "s_xor_b32 s40, s41, 16384\n" \
    "s_add_u32 s42, s60, 4096\n" \
    "s_xor_b32 s43, s43, 16384\n" \
    "s_cmp_eq_u32 s58, 1\n" \
    "s_cselect_b32 s40, s41, s40\n" \
    "s_cmp_eq_u32 s58, 2\n" \
    "s_cselect_b32 s40, s42, s40\n" \
    "v_add_u32 v18, s40, v18\n" \
    "s_xor_b32 s68, s60, 8192\n" \
    "v_add_u32 v22, s68, v22\n" \
    "s_add_u32 s57, s57, s60\n" \
    "s_add_u32 s59, s59, s60\n" \
    "s_xor_b32 s54, s54, s43\n" \
    "s_add_u32 s54, s54, s60\n" \
    "s_add_u32 s55, s54, 4096\n" \
    "s_add_u32 s71, s71, 40\n" \
    "s_add_u32 s71, s71, s64\n" \
    "C_next_%=:\n" \
    "v_mov_b64 v[88:89], 0\n" \
    "v_mov_b64 v[90:91], 0\n" \
    "v_mov_b64 v[92:93], 0\n" \
    "v_mov_b64 v[94:95], 0\n" \
    "v_mov_b64 v[96:97], 0\n" \
    "v_mov_b64 v[98:99], 0\n" \
    "v_mov_b64 v[100:101], 0\n" \
    "v_mov_b64 v[102:103], 0\n" \
    "v_mov_b32 v104, 0\n" \
    "v_add_u32 v30, s76, v19\n" \
    "v_add_u32 v31, s76, v20\n" \
    "s_mov_b32 s82, 0\n" \
    "s_mov_b32 s83, s71\n" \
    "s_add_u32 s74, s72, s54\n" \
    "s_add_u32 s75, s72, s55\n" \
    "C_clock_%=:\n" \
    "s_lshl_b32 s68, s62, 3\n" \
    "s_add_u32 s68, s68, s76\n" \
    "v_mov_b32 v2, s68\n" \
    "v_mov_b32 v0, s71\n" \
    "v_mov_b32 v1, s78\n" \
    "ds_write_b64 v2, v[0:1] offset:4096\n" \
    "s_memrealtime s[80:81]\n" \
    "s_waitcnt lgkmcnt(0)\n" \
    "s_sub_i32 s84, s71, s80\n" \
    "s_cmp_lt_i32 s84, -3\n" \
    "s_cbranch_scc1 C_late_%=\n" \
    "s_cmp_lt_i32 s84, 4\n" \
    "s_cbranch_scc1 C_wc_fine_%=\n" \
    "C_wc_spin_%=:\n" \
    "s_memrealtime s[80:81]\n" \
    "s_waitcnt lgkmcnt(0)\n" \
    "s_sub_i32 s84, s71, s80\n" \
    "s_cmp_gt_i32 s84, 3\n" \
    "s_cbranch_scc1 C_wc_spin_%=\n" \
    "C_wc_fine_%=:\n" \
    "s_cmp_lt_i32 s84, 1\n" \
    "s_cbranch_scc1 C_wc_go_%=\n" \
    "s_nop 3\n" \
    "s_cmp_lt_i32 s84, 2\n" \
    "s_cbranch_scc1 C_wc_go_%=\n" \
    "s_nop 3\n" \
    "s_cmp_lt_i32 s84, 3\n" \
    "s_cbranch_scc1 C_wc_go_%=\n" \
    "s_nop 6\n" \
    "C_wc_go_%=:\n" \
    "C_pass_%=:\n" \
    "buffer_load_dwordx4 v[48:51], v16, s[36:39], s74 offen offset:0 sc1\n" \
    "s_nop 5\n" \
    "buffer_load_dwordx4 v[52:55], v16, s[36:39], s74 offen offset:1024 sc1\n" \
    "s_nop 5\n" \
    "buffer_load_dwordx4 v[56:59], v16, s[36:39], s74 offen offset:2048 sc1\n" \
    "s_nop 5\n" \
    "buffer_load_dwordx4 v[60:63], v16, s[36:39], s74 offen offset:3072 sc1\n" \
    "s_nop 5\n" \
    "buffer_load_dwordx4 v[64:67], v16, s[36:39], s75 offen offset:0 sc1\n" \
    "s_nop 5\n" \
    "buffer_load_dwordx4 v[68:71], v16, s[36:39], s75 offen offset:1024 sc1\n" \
    "s_nop 5\n" \
    "buffer_load_dwordx4 v[72:75], v16, s[36:39], s75 offen offset:2048 sc1\n" \
    "s_nop 5\n" \
    "buffer_load_dwordx4 v[76:79], v16, s[36:39], s75 offen offset:3072 sc1\n" \
    "s_waitcnt vmcnt(7)\n" \
    "v_smfmac_f32_16x16x64_bf16 v[92:95], v[48:51], a[16:23], v17\n" \
    "v_max3_u32 v104, v104, v48, v50\n" \
    "v_smfmac_f32_16x16x64_bf16 v[96:99], v[48:51], a[8:15], v17\n" \
    "v_smfmac_f32_16x16x64_bf16 v[88:91], v[48:51], a[0:7], v17\n" \
    "v_smfmac_f32_16x16x64_bf16 v[100:103], v[48:51], a[24:31], v17\n" \
    "s_waitcnt vmcnt(6)\n" \
    "v_smfmac_f32_16x16x64_bf16 v[92:95], v[52:55], a[48:55], v17\n" \
    "v_max3_u32 v104, v104, v52, v54\n" \
    "v_smfmac_f32_16x16x64_bf16 v[96:99], v[52:55], a[40:47], v17\n" \
    "v_smfmac_f32_16x16x64_bf16 v[88:91], v[52:55], a[32:39], v17\n" \
    "v_smfmac_f32_16x16x64_bf16 v[100:103], v[52:55], a[56:63], v17\n" \
    "s_waitcnt vmcnt(5)\n" \
    "v_smfmac_f32_16x16x64_bf16 v[92:95], v[56:59], a[80:87], v17\n" \
    "v_max3_u32 v104, v104, v56, v58\n" \
    "v_smfmac_f32_16x16x64_bf16 v[96:99], v[56:59], a[72:79], v17\n" \
    "v_smfmac_f32_16x16x64_bf16 v[88:91], v[56:59], a[64:71], v17\n" \
    "v_smfmac_f32_16x16x64_bf16 v[100:103], v[56:59], a[88:95], v17\n" \
    "s_waitcnt vmcnt(4)\n" \
    "v_smfmac_f32_16x16x64_bf16 v[92:95], v[60:63], a[112:119], v17\n" \
    "v_max3_u32 v104, v104, v60, v62\n" \
    "v_smfmac_f32_16x16x64_bf16 v[96:99], v[60:63], a[104:111], v17\n" \
    "v_smfmac_f32_16x16x64_bf16 v[88:91], v[60:63], a[96:103], v17\n" \
    "v_smfmac_f32_16x16x64_bf16 v[100:103], v[60:63], a[120:127], v17\n" \
    "s_waitcnt vmcnt(3)\n" \
    "v_smfmac_f32_16x16x64_bf16 v[92:95], v[64:67], a[144:151], v17\n" \
    "v_max3_u32 v104, v104, v64, v66\n" \
    "v_smfmac_f32_16x16x64_bf16 v[96:99], v[64:67], a[136:143], v17\n" \
    "v_smfmac_f32_16x16x64_bf16 v[88:91], v[64:67], a[128:135], v17\n" \
    "v_smfmac_f32_16x16x64_bf16 v[100:103], v[64:67], a[152:159], v17\n" \
    "s_waitcnt vmcnt(2)\n" \
    "v_smfmac_f32_16x16x64_bf16 v[92:95], v[68:71], a[176:183], v17\n" \
    "v_max3_u32 v104, v104, v68, v70\n" \
    "v_smfmac_f32_16x16x64_bf16 v[96:99], v[68:71], a[168:175], v17\n" \
    "v_smfmac_f32_16x16x64_bf16 v[88:91], v[68:71], a[160:167], v17\n" \
    "v_smfmac_f32_16x16x64_bf16 v[100:103], v[68:71], a[184:191], v17\n" \
    "s_waitcnt vmcnt(1)\n" \
    "v_smfmac_f32_16x16x64_bf16 v[92:95], v[72:75], a[208:215], v17\n" \
    "v_max3_u32 v104, v104, v72, v74\n" \
    "v_smfmac_f32_16x16x64_bf16 v[96:99], v[72:75], a[200:207], v17\n" \
    "v_smfmac_f32_16x16x64_bf16 v[88:91], v[72:75], a[192:199], v17\n" \
    "v_smfmac_f32_16x16x64_bf16 v[100:103], v[72:75], a[216:223], v17\n" \
    "s_waitcnt vmcnt(0)\n" \
    "v_smfmac_f32_16x16x64_bf16 v[92:95], v[76:79], a[240:247], v17\n" \
    "v_max3_u32 v104, v104, v76, v78\n" \
    "v_smfmac_f32_16x16x64_bf16 v[96:99], v[76:79], a[232:239], v17\n" \
    "v_smfmac_f32_16x16x64_bf16 v[88:91], v[76:79], a[224:231], v17\n" \
    "v_smfmac_f32_16x16x64_bf16 v[100:103], v[76:79], a[248:255], v17\n" \
    "v_cmp_eq_u32 vcc, -1, v104\n" \
    "s_cbranch_vccnz C_retry_%=\n" \
    "s_nop 3\n" \
    "v_permlane32_swap_b32 v92, v96\n" \
    "v_permlane32_swap_b32 v93, v97\n" \
    "v_permlane32_swap_b32 v94, v98\n" \
    "v_permlane32_swap_b32 v95, v99\n" \
    "v_cndmask_b32 v88, v100, v88, s[44:45]\n" \
    "v_cndmask_b32 v89, v101, v89, s[44:45]\n" \
    "v_cndmask_b32 v90, v102, v90, s[44:45]\n" \
    "v_cndmask_b32 v91, v103, v91, s[44:45]\n" \
    "v_cndmask_b32 v92, v92, v96, s[44:45]\n" \
    "v_cndmask_b32 v93, v93, v97, s[44:45]\n" \
    "v_cndmask_b32 v94, v94, v98, s[44:45]\n" \
    "v_cndmask_b32 v95, v95, v99, s[44:45]\n" \
    "v_pk_add_f32 v[88:89], v[88:89], v[92:93]\n" \
    "v_pk_add_f32 v[90:91], v[90:91], v[94:95]\n" \
    "s_nop 0\n" \
    "v_permlane16_swap_b32 v88, v89\n" \
    "v_permlane16_swap_b32 v90, v91\n" \
    "v_add_f32 v0, v88, v89\n" \
    "v_add_f32 v1, v90, v91\n" \
    "ds_write_b32 v30, v0\n" \
    "ds_write_b32 v30, v1 offset:256\n" \
    "s_cmp_lg_u32 s62, 0\n" \
    "s_waitcnt lgkmcnt(0)\n" \
    "s_barrier\n" \
    "s_cbranch_scc1 C_pub_done_%=\n" \
    "ds_read_b128 v[4:7], v31\n" \
    "ds_read_b128 v[8:11], v31 offset:512\n" \
    "ds_read_b128 v[12:15], v31 offset:1024\n" \
    "ds_read_b128 v[24:27], v31 offset:1536\n" \
    "v_mov_b32 v28, -1\n" \
    "v_mov_b32 v29, -1\n" \
    "s_waitcnt lgkmcnt(2)\n" \
    "v_pk_add_f32 v[4:5], v[4:5], v[8:9]\n" \
    "v_pk_add_f32 v[6:7], v[6:7], v[10:11]\n" \
    "s_waitcnt lgkmcnt(0)\n" \
    "v_pk_add_f32 v[12:13], v[12:13], v[24:25]\n" \
    "v_pk_add_f32 v[14:15], v[14:15], v[26:27]\n" \
    "v_pk_add_f32 v[4:5], v[4:5], v[12:13]\n" \
    "v_pk_add_f32 v[6:7], v[6:7], v[14:15]\n" \
    "v_cvt_pk_bf16_f32 v4, v4, v5\n" \
    "v_cvt_pk_bf16_f32 v5, v6, v7\n" \
    "v_min_u32 v4, -2, v4\n" \
    "v_min_u32 v5, -2, v5\n" \
    "buffer_store_dwordx2 v[4:5], v18, s[36:39], s73 offen sc1\n" \
    "buffer_store_dwordx2 v[28:29], v18, s[36:39], s72 offen sc1\n" \
    "C_pub_done_%=:\n" \
    "s_memrealtime s[80:81]\n" \
    "s_waitcnt lgkmcnt(0)\n" \
    "s_mov_b32 s58, s80\n" \
    "v_mov_b32 v2, s76\n" \
    "ds_read_b128 v[4:7], v2 offset:4096\n" \
    "ds_read_b128 v[8:11], v2 offset:4112\n" \
    "s_waitcnt lgkmcnt(0)\n" \
    "v_readfirstlane_b32 s68, v4\n" \
    "v_readfirstlane_b32 s69, v5\n" \
    "s_sub_i32 s85, s68, s71\n" \
    "s_cmp_gt_i32 s85, 0\n" \
    "s_cselect_b32 s71, s68, s71\n" \
    "s_or_b32 s78, s78, s69\n" \
    "v_readfirstlane_b32 s68, v6\n" \
    "v_readfirstlane_b32 s69, v7\n" \
    "s_sub_i32 s85, s68, s71\n" \
    "s_cmp_gt_i32 s85, 0\n" \
    "s_cselect_b32 s71, s68, s71\n" \
    "s_or_b32 s78, s78, s69\n" \
    "v_readfirstlane_b32 s68, v8\n" \
    "v_readfirstlane_b32 s69, v9\n" \
    "s_sub_i32 s85, s68, s71\n" \
    "s_cmp_gt_i32 s85, 0\n" \
    "s_cselect_b32 s71, s68, s71\n" \
    "s_or_b32 s78, s78, s69\n" \
    "v_readfirstlane_b32 s68, v10\n" \
    "v_readfirstlane_b32 s69, v11\n" \
    "s_sub_i32 s85, s68, s71\n" \
    "s_cmp_gt_i32 s85, 0\n" \
    "s_cselect_b32 s71, s68, s71\n" \
    "s_or_b32 s78, s78, s69\n" \
    "s_lshl_b32 s68, s70, 3\n" \
    "s_lshr_b32 s68, 791592, s68\n" \
    "s_and_b32 s68, s68, 255\n" \
    "s_cmp_lt_u32 s70, 4\n" \
    "s_cselect_b32 s68, s68, 0\n" \
    "s_sub_u32 s58, s58, s71\n" \
    "s_add_u32 s71, s71, s68\n" \
    "s_add_u32 s71, s71, s65\n" \
    "s_add_u32 s77, s70, 1\n" \
    "v_mov_b32 v4, s63\n" \
    "v_mov_b32 v5, s77\n" \
    "v_mov_b32 v6, s78\n" \
    "v_mov_b32 v7, s58\n" \
    "buffer_store_dwordx4 v[4:7], v22, s[36:39], 0 offen sc1\n" \
    "s_nop 0\n" \
    "s_cmp_eq_u32 s70, 62\n" \
    "s_cbranch_scc1 C_fin_%=\n" \
    "s_cmp_lg_u32 s78, 0\n" \
    "s_cbranch_scc1 C_pw_d_%=\n" \
    "C_pw_%=:\n" \
    "s_memrealtime s[80:81]\n" \
    "s_waitcnt lgkmcnt(0)\n" \
    "s_sub_i32 s85, s80, s71\n" \
    "s_cmp_lt_i32 s85, 56\n" \
    "s_cbranch_scc1 C_pw_%=\n" \
    "C_pw_d_%=:\n" \
    "s_sub_u32 s85, s59, 4096\n" \
    "buffer_load_dwordx2 v[0:1], off, s[36:39], s85 sc1\n" \
    "s_waitcnt vmcnt(0)\n" \
    "v_readfirstlane_b32 s68, v0\n" \
    "v_readfirstlane_b32 s69, v1\n" \
    "s_and_b32 s85, s69, 255\n" \
    "s_sub_u32 s85, s85, s70\n" \
    "s_xor_b32 s68, s68, s63\n" \
    "s_xor_b32 s85, s85, 1\n" \
    "s_or_b32 s68, s68, s85\n" \
    "s_bfe_u32 s85, s69, 786440\n" \
    "s_lshr_b32 s69, s69, 20\n" \
    "s_cmp_eq_u32 s68, 0\n" \
    "s_cselect_b32 s64, s85, s64\n" \
    "s_cselect_b32 s65, s69, s65\n" \
    "s_lshl_b32 s61, s77, 3\n" \
    "s_lshr_b32 s61, 791592, s61\n" \
    "s_and_b32 s61, s61, 255\n" \
    "s_cmp_lt_u32 s77, 4\n" \
    "s_cselect_b32 s61, s61, 0\n" \
    "s_cmp_lg_u32 s78, 0\n" \
    "s_cbranch_scc1 C_mh_%=\n" \
    "C_mh_b_%=:\n" \
    "s_add_u32 s71, s71, s61\n" \
    "s_add_u32 s71, s71, s64\n" \
    "s_mov_b32 s72, s73\n" \
    "s_add_u32 s73, s73, 163840\n" \
    "s_cmp_eq_u32 s73, 491520\n" \
    "s_cselect_b32 s73, 0, s73\n" \
    "s_xor_b32 s76, s76, 2048\n" \
    "s_add_u32 s70, s70, 1\n" \
    "s_cmp_lt_u32 s70, 63\n" \
    "s_cbranch_scc1 C_next_%=\n" \
    "C_fin_%=:\n" \
    "s_endpgm\n" \
    "C_late_%=:\n" \
    "s_branch C_pass_%=\n" \
    "C_retry_%=:\n" \
    "s_nop 7\n" \
    "v_mov_b64 v[88:89], 0\n" \
    "v_mov_b64 v[90:91], 0\n" \
    "v_mov_b64 v[92:93], 0\n" \
    "v_mov_b64 v[94:95], 0\n" \
    "v_mov_b64 v[96:97], 0\n" \
    "v_mov_b64 v[98:99], 0\n" \
    "v_mov_b64 v[100:101], 0\n" \
    "v_mov_b64 v[102:103], 0\n" \
    "v_mov_b32 v104, 0\n" \
    "v_add_u32 v30, s76, v19\n" \
    "v_add_u32 v31, s76, v20\n" \
    "s_add_u32 s82, s82, 8\n" \
    "s_and_b32 s85, s82, 63\n" \
    "s_cmp_lg_u32 s85, 0\n" \
    "s_cbranch_scc1 C_g0_ok_%=\n" \
    "s_mov_b32 s85, 1048832\n" \
    "buffer_load_dword v0, off, s[36:39], s85 sc1\n" \
    "s_waitcnt vmcnt(0)\n" \
    "v_readfirstlane_b32 s85, v0\n" \
    "s_cmp_eq_u32 s85, s63\n" \
    "s_cbranch_scc1 C_end_%=\n" \
    "s_memrealtime s[80:81]\n" \
    "s_waitcnt lgkmcnt(0)\n" \
    "s_sub_u32 s85, s80, s83\n" \
    "s_cmp_gt_u32 s85, 3000000\n" \
    "s_cbranch_scc1 C_giveup_%=\n" \
    "C_g0_ok_%=:\n" \
    "C_mc_%=:\n" \
    "buffer_load_dwordx4 v[0:3], off, s[36:39], s57 sc1\n" \
    "s_waitcnt vmcnt(0)\n" \
    "v_readfirstlane_b32 s68, v0\n" \
    "v_readfirstlane_b32 s69, v1\n" \
    "v_readfirstlane_b32 s71, v2\n" \
    "v_readfirstlane_b32 s79, v3\n" \
    "s_cmp_eq_u32 s68, s63\n" \
    "s_cselect_b32 s69, s69, -1\n" \
    "s_cmp_ge_i32 s69, s70\n" \
    "s_cbranch_scc1 C_mc_d_%=\n" \
    "s_add_u32 s82, s82, 2\n" \
    "s_and_b32 s85, s82, 63\n" \
    "s_cmp_lg_u32 s85, 0\n" \
    "s_cbranch_scc1 C_mc_g_ok_%=\n" \
    "s_mov_b32 s85, 1048832\n" \
    "buffer_load_dword v0, off, s[36:39], s85 sc1\n" \
    "s_waitcnt vmcnt(0)\n" \
    "v_readfirstlane_b32 s85, v0\n" \
    "s_cmp_eq_u32 s85, s63\n" \
    "s_cbranch_scc1 C_end_%=\n" \
    "s_memrealtime s[80:81]\n" \
    "s_waitcnt lgkmcnt(0)\n" \
    "s_sub_u32 s85, s80, s83\n" \
    "s_cmp_gt_u32 s85, 3000000\n" \
    "s_cbranch_scc1 C_giveup_%=\n" \
    "C_mc_g_ok_%=:\n" \
    "s_sleep 1\n" \
    "s_branch C_mc_%=\n" \
    "C_mc_d_%=:\n" \
    "s_mov_b32 s78, 1\n" \
    "s_branch C_clock_%=\n" \
    "C_mh_%=:\n" \
    "buffer_load_dwordx4 v[0:3], off, s[36:39], s59 sc1\n" \
    "s_waitcnt vmcnt(0)\n" \
    "v_readfirstlane_b32 s68, v0\n" \
    "v_readfirstlane_b32 s69, v1\n" \
    "v_readfirstlane_b32 s71, v2\n" \
    "v_readfirstlane_b32 s79, v3\n" \
    "s_cmp_eq_u32 s68, s63\n" \
    "s_cselect_b32 s69, s69, -1\n" \
    "s_cmp_ge_i32 s69, s77\n" \
    "s_cbranch_scc1 C_mh_d_%=\n" \
    "s_add_u32 s82, s82, 2\n" \
    "s_and_b32 s85, s82, 63\n" \
    "s_cmp_lg_u32 s85, 0\n" \
    "s_cbranch_scc1 C_mh_g_ok_%=\n" \
    "s_mov_b32 s85, 1048832\n" \
    "buffer_load_dword v0, off, s[36:39], s85 sc1\n" \
    "s_waitcnt vmcnt(0)\n" \
    "v_readfirstlane_b32 s85, v0\n" \
    "s_cmp_eq_u32 s85, s63\n" \
    "s_cbranch_scc1 C_end_%=\n" \
    "s_memrealtime s[80:81]\n" \
    "s_waitcnt lgkmcnt(0)\n" \
    "s_sub_u32 s85, s80, s83\n" \
    "s_cmp_gt_u32 s85, 3000000\n" \
    "s_cbranch_scc1 C_giveup_%=\n" \
    "C_mh_g_ok_%=:\n" \
    "s_sleep 1\n" \
    "s_branch C_mh_%=\n" \
    "C_mh_d_%=:\n" \
    "s_bfe_u32 s64, s79, 786440\n" \
    "s_lshr_b32 s65, s79, 20\n" \
    "s_and_b32 s79, s79, 1\n" \
    "s_xor_b32 s78, s79, 1\n" \
    "s_branch C_mh_b_%=\n" \
    "C_giveup_%=:\n" \
    "s_mov_b32 s85, 1048832\n" \
    "v_mov_b32 v0, s63\n" \
    "buffer_store_dword v0, off, s[36:39], s85 sc1\n" \
    "C_end_%=:\n" \
    "s_endpgm\n"

#define PROJ0_ASM \
    "s_nop 4\n" \
    "s_mov_b32 s68, 0\n" \
    "s_add_u32 m0, s52, 0\n" \
    "s_nop 0\n" \
    "buffer_load_dwordx4 v0, s[40:43], s68 offen lds\n" \
    "s_add_u32 m0, s52, 1024\n" \
    "s_nop 0\n" \
    "buffer_load_dwordx4 v1, s[40:43], s68 offen lds\n" \
    "s_add_u32 m0, s52, 2048\n" \
    "s_nop 0\n" \
    "buffer_load_dwordx4 v2, s[40:43], s68 offen lds\n" \
    "s_add_u32 m0, s52, 3072\n" \
    "s_nop 0\n" \
    "buffer_load_dwordx4 v3, s[40:43], s68 offen lds\n" \
    "s_mov_b32 s68, 262144\n" \
    "s_add_u32 m0, s52, 4096\n" \
    "s_nop 0\n" \
    "buffer_load_dwordx4 v0, s[40:43], s68 offen lds\n" \
    "s_add_u32 m0, s52, 5120\n" \
    "s_nop 0\n" \
    "buffer_load_dwordx4 v1, s[40:43], s68 offen lds\n" \
    "s_add_u32 m0, s52, 6144\n" \
    "s_nop 0\n" \
    "buffer_load_dwordx4 v2, s[40:43], s68 offen lds\n" \
    "s_add_u32 m0, s52, 7168\n" \
    "s_nop 0\n" \
    "buffer_load_dwordx4 v3, s[40:43], s68 offen lds\n" \
    "s_mov_b32 s68, 524288\n" \
    "s_add_u32 m0, s52, 8192\n" \
    "s_nop 0\n" \
    "buffer_load_dwordx4 v0, s[40:43], s68 offen lds\n" \
    "s_add_u32 m0, s52, 9216\n" \
    "s_nop 0\n" \
    "buffer_load_dwordx4 v1, s[40:43], s68 offen lds\n" \
    "s_add_u32 m0, s52, 10240\n" \
    "s_nop 0\n" \
    "buffer_load_dwordx4 v2, s[40:43], s68 offen lds\n" \
    "s_add_u32 m0, s52, 11264\n" \
    "s_nop 0\n" \
    "buffer_load_dwordx4 v3, s[40:43], s68 offen lds\n" \
    "s_mov_b32 s68, 786432\n" \
    "s_add_u32 m0, s52, 12288\n" \
    "s_nop 0\n" \
    "buffer_load_dwordx4 v0, s[40:43], s68 offen lds\n" \
    "s_add_u32 m0, s52, 13312\n" \
    "s_nop 0\n" \
    "buffer_load_dwordx4 v1, s[40:43], s68 offen lds\n" \
    "s_add_u32 m0, s52, 14336\n" \
    "s_nop 0\n" \
    "buffer_load_dwordx4 v2, s[40:43], s68 offen lds\n" \
    "s_add_u32 m0, s52, 15360\n" \
    "s_nop 0\n" \
    "buffer_load_dwordx4 v3, s[40:43], s68 offen lds\n" \
    "s_mov_b32 s68, 1048576\n" \
    "s_add_u32 m0, s52, 16384\n" \
    "s_nop 0\n" \
    "buffer_load_dwordx4 v0, s[40:43], s68 offen lds\n" \
    "s_add_u32 m0, s52, 17408\n" \
    "s_nop 0\n" \
    "buffer_load_dwordx4 v1, s[40:43], s68 offen lds\n" \
    "s_add_u32 m0, s52, 18432\n" \
    "s_nop 0\n" \
    "buffer_load_dwordx4 v2, s[40:43], s68 offen lds\n" \
    "s_add_u32 m0, s52, 19456\n" \
    "s_nop 0\n" \
    "buffer_load_dwordx4 v3, s[40:43], s68 offen lds\n" \
    "s_mov_b32 s68, 1310720\n" \
    "s_add_u32 m0, s52, 20480\n" \
    "s_nop 0\n" \
    "buffer_load_dwordx4 v0, s[40:43], s68 offen lds\n" \
    "s_add_u32 m0, s52, 21504\n" \
    "s_nop 0\n" \
    "buffer_load_dwordx4 v1, s[40:43], s68 offen lds\n" \
    "s_add_u32 m0, s52, 22528\n" \
    "s_nop 0\n" \
    "buffer_load_dwordx4 v2, s[40:43], s68 offen lds\n" \
    "s_add_u32 m0, s52, 23552\n" \
    "s_nop 0\n" \
    "buffer_load_dwordx4 v3, s[40:43], s68 offen lds\n" \
    "s_mov_b32 s68, 1572864\n" \
    "s_add_u32 m0, s52, 24576\n" \
    "s_nop 0\n" \
    "buffer_load_dwordx4 v0, s[40:43], s68 offen lds\n" \
    "s_add_u32 m0, s52, 25600\n" \
    "s_nop 0\n" \
    "buffer_load_dwordx4 v1, s[40:43], s68 offen lds\n" \
    "s_add_u32 m0, s52, 26624\n" \
    "s_nop 0\n" \
    "buffer_load_dwordx4 v2, s[40:43], s68 offen lds\n" \
    "s_add_u32 m0, s52, 27648\n" \
    "s_nop 0\n" \
    "buffer_load_dwordx4 v3, s[40:43], s68 offen lds\n" \
    "s_waitcnt vmcnt(24)\n" \
    "ds_read_b64_tr_b16 a[0:1], v4 offset:0\n" \
    "ds_read_b64_tr_b16 a[2:3], v5 offset:0\n" \
    "ds_read_b64_tr_b16 a[4:5], v6 offset:0\n" \
    "ds_read_b64_tr_b16 a[6:7], v7 offset:0\n" \
    "ds_read_b64_tr_b16 a[8:9], v8 offset:0\n" \
    "ds_read_b64_tr_b16 a[10:11], v9 offset:0\n" \
    "ds_read_b64_tr_b16 a[12:13], v10 offset:0\n" \
    "ds_read_b64_tr_b16 a[14:15], v11 offset:0\n" \
    "s_waitcnt lgkmcnt(0)\n" \
    "s_mov_b32 s68, 1835008\n" \
    "s_add_u32 m0, s52, 0\n" \
    "s_nop 0\n" \
    "buffer_load_dwordx4 v0, s[40:43], s68 offen lds\n" \
    "s_add_u32 m0, s52, 1024\n" \
    "s_nop 0\n" \
    "buffer_load_dwordx4 v1, s[40:43], s68 offen lds\n" \
    "s_add_u32 m0, s52, 2048\n" \
    "s_nop 0\n" \
    "buffer_load_dwordx4 v2, s[40:43], s68 offen lds\n" \
    "s_add_u32 m0, s52, 3072\n" \
    "s_nop 0\n" \
    "buffer_load_dwordx4 v3, s[40:43], s68 offen lds\n" \
    "s_waitcnt vmcnt(24)\n" \
    "ds_read_b64_tr_b16 a[16:17], v4 offset:4096\n" \
    "ds_read_b64_tr_b16 a[18:19], v5 offset:4096\n" \
    "ds_read_b64_tr_b16 a[20:21], v6 offset:4096\n" \
    "ds_read_b64_tr_b16 a[22:23], v7 offset:4096\n" \
    "ds_read_b64_tr_b16 a[24:25], v8 offset:4096\n" \
    "ds_read_b64_tr_b16 a[26:27], v9 offset:4096\n" \
    "ds_read_b64_tr_b16 a[28:29], v10 offset:4096\n" \
    "ds_read_b64_tr_b16 a[30:31], v11 offset:4096\n" \
    "s_waitcnt lgkmcnt(0)\n" \
    "s_mov_b32 s68, 2097152\n" \
    "s_add_u32 m0, s52, 4096\n" \
    "s_nop 0\n" \
    "buffer_load_dwordx4 v0, s[40:43], s68 offen lds\n" \
    "s_add_u32 m0, s52, 5120\n" \
    "s_nop 0\n" \
    "buffer_load_dwordx4 v1, s[40:43], s68 offen lds\n" \
    "s_add_u32 m0, s52, 6144\n" \
    "s_nop 0\n" \
    "buffer_load_dwordx4 v2, s[40:43], s68 offen lds\n" \
    "s_add_u32 m0, s52, 7168\n" \
    "s_nop 0\n" \
    "buffer_load_dwordx4 v3, s[40:43], s68 offen lds\n" \
    "s_waitcnt vmcnt(24)\n" \
    "ds_read_b64_tr_b16 a[32:33], v4 offset:8192\n" \
    "ds_read_b64_tr_b16 a[34:35], v5 offset:8192\n" \
    "ds_read_b64_tr_b16 a[36:37], v6 offset:8192\n" \
    "ds_read_b64_tr_b16 a[38:39], v7 offset:8192\n" \
    "ds_read_b64_tr_b16 a[40:41], v8 offset:8192\n" \
    "ds_read_b64_tr_b16 a[42:43], v9 offset:8192\n" \
    "ds_read_b64_tr_b16 a[44:45], v10 offset:8192\n" \
    "ds_read_b64_tr_b16 a[46:47], v11 offset:8192\n" \
    "s_waitcnt lgkmcnt(0)\n" \
    "s_mov_b32 s68, 2359296\n" \
    "s_add_u32 m0, s52, 8192\n" \
    "s_nop 0\n" \
    "buffer_load_dwordx4 v0, s[40:43], s68 offen lds\n" \
    "s_add_u32 m0, s52, 9216\n" \
    "s_nop 0\n" \
    "buffer_load_dwordx4 v1, s[40:43], s68 offen lds\n" \
    "s_add_u32 m0, s52, 10240\n" \
    "s_nop 0\n" \
    "buffer_load_dwordx4 v2, s[40:43], s68 offen lds\n" \
    "s_add_u32 m0, s52, 11264\n" \
    "s_nop 0\n" \
    "buffer_load_dwordx4 v3, s[40:43], s68 offen lds\n" \
    "s_waitcnt vmcnt(24)\n" \
    "ds_read_b64_tr_b16 a[48:49], v4 offset:12288\n" \
    "ds_read_b64_tr_b16 a[50:51], v5 offset:12288\n" \
    "ds_read_b64_tr_b16 a[52:53], v6 offset:12288\n" \
    "ds_read_b64_tr_b16 a[54:55], v7 offset:12288\n" \
    "ds_read_b64_tr_b16 a[56:57], v8 offset:12288\n" \
    "ds_read_b64_tr_b16 a[58:59], v9 offset:12288\n" \
    "ds_read_b64_tr_b16 a[60:61], v10 offset:12288\n" \
    "ds_read_b64_tr_b16 a[62:63], v11 offset:12288\n" \
    "s_waitcnt lgkmcnt(0)\n" \
    "s_mov_b32 s68, 2621440\n" \
    "s_add_u32 m0, s52, 12288\n" \
    "s_nop 0\n" \
    "buffer_load_dwordx4 v0, s[40:43], s68 offen lds\n" \
    "s_add_u32 m0, s52, 13312\n" \
    "s_nop 0\n" \
    "buffer_load_dwordx4 v1, s[40:43], s68 offen lds\n" \
    "s_add_u32 m0, s52, 14336\n" \
    "s_nop 0\n" \
    "buffer_load_dwordx4 v2, s[40:43], s68 offen lds\n" \
    "s_add_u32 m0, s52, 15360\n" \
    "s_nop 0\n" \
    "buffer_load_dwordx4 v3, s[40:43], s68 offen lds\n" \
    "s_waitcnt vmcnt(24)\n" \
    "ds_read_b64_tr_b16 a[64:65], v4 offset:16384\n" \
    "ds_read_b64_tr_b16 a[66:67], v5 offset:16384\n" \
    "ds_read_b64_tr_b16 a[68:69], v6 offset:16384\n" \
    "ds_read_b64_tr_b16 a[70:71], v7 offset:16384\n" \
    "ds_read_b64_tr_b16 a[72:73], v8 offset:16384\n" \
    "ds_read_b64_tr_b16 a[74:75], v9 offset:16384\n" \
    "ds_read_b64_tr_b16 a[76:77], v10 offset:16384\n" \
    "ds_read_b64_tr_b16 a[78:79], v11 offset:16384\n" \
    "s_waitcnt lgkmcnt(0)\n" \
    "s_mov_b32 s68, 2883584\n" \
    "s_add_u32 m0, s52, 16384\n" \
    "s_nop 0\n" \
    "buffer_load_dwordx4 v0, s[40:43], s68 offen lds\n" \
    "s_add_u32 m0, s52, 17408\n" \
    "s_nop 0\n" \
    "buffer_load_dwordx4 v1, s[40:43], s68 offen lds\n" \
    "s_add_u32 m0, s52, 18432\n" \
    "s_nop 0\n" \
    "buffer_load_dwordx4 v2, s[40:43], s68 offen lds\n" \
    "s_add_u32 m0, s52, 19456\n" \
    "s_nop 0\n" \
    "buffer_load_dwordx4 v3, s[40:43], s68 offen lds\n" \
    "s_waitcnt vmcnt(24)\n" \
    "ds_read_b64_tr_b16 a[80:81], v4 offset:20480\n" \
    "ds_read_b64_tr_b16 a[82:83], v5 offset:20480\n" \
    "ds_read_b64_tr_b16 a[84:85], v6 offset:20480\n" \
    "ds_read_b64_tr_b16 a[86:87], v7 offset:20480\n" \
    "ds_read_b64_tr_b16 a[88:89], v8 offset:20480\n" \
    "ds_read_b64_tr_b16 a[90:91], v9 offset:20480\n" \
    "ds_read_b64_tr_b16 a[92:93], v10 offset:20480\n" \
    "ds_read_b64_tr_b16 a[94:95], v11 offset:20480\n" \
    "s_waitcnt lgkmcnt(0)\n" \
    "s_mov_b32 s68, 3145728\n" \
    "s_add_u32 m0, s52, 20480\n" \
    "s_nop 0\n" \
    "buffer_load_dwordx4 v0, s[40:43], s68 offen lds\n" \
    "s_add_u32 m0, s52, 21504\n" \
    "s_nop 0\n" \
    "buffer_load_dwordx4 v1, s[40:43], s68 offen lds\n" \
    "s_add_u32 m0, s52, 22528\n" \
    "s_nop 0\n" \
    "buffer_load_dwordx4 v2, s[40:43], s68 offen lds\n" \
    "s_add_u32 m0, s52, 23552\n" \
    "s_nop 0\n" \
    "buffer_load_dwordx4 v3, s[40:43], s68 offen lds\n" \
    "s_waitcnt vmcnt(24)\n" \
    "ds_read_b64_tr_b16 a[96:97], v4 offset:24576\n" \
    "ds_read_b64_tr_b16 a[98:99], v5 offset:24576\n" \
    "ds_read_b64_tr_b16 a[100:101], v6 offset:24576\n" \
    "ds_read_b64_tr_b16 a[102:103], v7 offset:24576\n" \
    "ds_read_b64_tr_b16 a[104:105], v8 offset:24576\n" \
    "ds_read_b64_tr_b16 a[106:107], v9 offset:24576\n" \
    "ds_read_b64_tr_b16 a[108:109], v10 offset:24576\n" \
    "ds_read_b64_tr_b16 a[110:111], v11 offset:24576\n" \
    "s_waitcnt lgkmcnt(0)\n" \
    "s_mov_b32 s68, 3407872\n" \
    "s_add_u32 m0, s52, 24576\n" \
    "s_nop 0\n" \
    "buffer_load_dwordx4 v0, s[40:43], s68 offen lds\n" \
    "s_add_u32 m0, s52, 25600\n" \
    "s_nop 0\n" \
    "buffer_load_dwordx4 v1, s[40:43], s68 offen lds\n" \
    "s_add_u32 m0, s52, 26624\n" \
    "s_nop 0\n" \
    "buffer_load_dwordx4 v2, s[40:43], s68 offen lds\n" \
    "s_add_u32 m0, s52, 27648\n" \
    "s_nop 0\n" \
    "buffer_load_dwordx4 v3, s[40:43], s68 offen lds\n" \
    "s_waitcnt vmcnt(24)\n" \
    "ds_read_b64_tr_b16 a[112:113], v4 offset:0\n" \
    "ds_read_b64_tr_b16 a[114:115], v5 offset:0\n" \
    "ds_read_b64_tr_b16 a[116:117], v6 offset:0\n" \
    "ds_read_b64_tr_b16 a[118:119], v7 offset:0\n" \
    "ds_read_b64_tr_b16 a[120:121], v8 offset:0\n" \
    "ds_read_b64_tr_b16 a[122:123], v9 offset:0\n" \
    "ds_read_b64_tr_b16 a[124:125], v10 offset:0\n" \
    "ds_read_b64_tr_b16 a[126:127], v11 offset:0\n" \
    "s_waitcnt lgkmcnt(0)\n" \
    "s_mov_b32 s68, 3670016\n" \
    "s_add_u32 m0, s52, 0\n" \
    "s_nop 0\n" \
    "buffer_load_dwordx4 v0, s[40:43], s68 offen lds\n" \
    "s_add_u32 m0, s52, 1024\n" \
    "s_nop 0\n" \
    "buffer_load_dwordx4 v1, s[40:43], s68 offen lds\n" \
    "s_add_u32 m0, s52, 2048\n" \
    "s_nop 0\n" \
    "buffer_load_dwordx4 v2, s[40:43], s68 offen lds\n" \
    "s_add_u32 m0, s52, 3072\n" \
    "s_nop 0\n" \
    "buffer_load_dwordx4 v3, s[40:43], s68 offen lds\n" \
    "s_waitcnt vmcnt(24)\n" \
    "ds_read_b64_tr_b16 a[128:129], v4 offset:4096\n" \
    "ds_read_b64_tr_b16 a[130:131], v5 offset:4096\n" \
    "ds_read_b64_tr_b16 a[132:133], v6 offset:4096\n" \
    "ds_read_b64_tr_b16 a[134:135], v7 offset:4096\n" \
    "ds_read_b64_tr_b16 a[136:137], v8 offset:4096\n" \
    "ds_read_b64_tr_b16 a[138:139], v9 offset:4096\n" \
    "ds_read_b64_tr_b16 a[140:141], v10 offset:4096\n" \
    "ds_read_b64_tr_b16 a[142:143], v11 offset:4096\n" \
    "s_waitcnt lgkmcnt(0)\n" \
    "s_mov_b32 s68, 3932160\n" \
    "s_add_u32 m0, s52, 4096\n" \
    "s_nop 0\n" \
    "buffer_load_dwordx4 v0, s[40:43], s68 offen lds\n" \
    "s_add_u32 m0, s52, 5120\n" \
    "s_nop 0\n" \
    "buffer_load_dwordx4 v1, s[40:43], s68 offen lds\n" \
    "s_add_u32 m0, s52, 6144\n" \
    "s_nop 0\n" \
    "buffer_load_dwordx4 v2, s[40:43], s68 offen lds\n" \
    "s_add_u32 m0, s52, 7168\n" \
    "s_nop 0\n" \
    "buffer_load_dwordx4 v3, s[40:43], s68 offen lds\n" \
    "s_waitcnt vmcnt(24)\n" \
    "ds_read_b64_tr_b16 a[144:145], v4 offset:8192\n" \
    "ds_read_b64_tr_b16 a[146:147], v5 offset:8192\n" \
    "ds_read_b64_tr_b16 a[148:149], v6 offset:8192\n" \
    "ds_read_b64_tr_b16 a[150:151], v7 offset:8192\n" \
    "ds_read_b64_tr_b16 a[152:153], v8 offset:8192\n" \
    "ds_read_b64_tr_b16 a[154:155], v9 offset:8192\n" \
    "ds_read_b64_tr_b16 a[156:157], v10 offset:8192\n" \
    "ds_read_b64_tr_b16 a[158:159], v11 offset:8192\n" \
    "s_waitcnt lgkmcnt(0)\n" \
    "s_mov_b32 s68, 4194304\n" \
    "s_add_u32 m0, s52, 8192\n" \
    "s_nop 0\n" \
    "buffer_load_dwordx4 v0, s[40:43], s68 offen lds\n" \
    "s_add_u32 m0, s52, 9216\n" \
    "s_nop 0\n" \
    "buffer_load_dwordx4 v1, s[40:43], s68 offen lds\n" \
    "s_add_u32 m0, s52, 10240\n" \
    "s_nop 0\n" \
    "buffer_load_dwordx4 v2, s[40:43], s68 offen lds\n" \
    "s_add_u32 m0, s52, 11264\n" \
    "s_nop 0\n" \
    "buffer_load_dwordx4 v3, s[40:43], s68 offen lds\n" \
    "s_waitcnt vmcnt(24)\n" \
    "ds_read_b64_tr_b16 a[160:161], v4 offset:12288\n" \
    "ds_read_b64_tr_b16 a[162:163], v5 offset:12288\n" \
    "ds_read_b64_tr_b16 a[164:165], v6 offset:12288\n" \
    "ds_read_b64_tr_b16 a[166:167], v7 offset:12288\n" \
    "ds_read_b64_tr_b16 a[168:169], v8 offset:12288\n" \
    "ds_read_b64_tr_b16 a[170:171], v9 offset:12288\n" \
    "ds_read_b64_tr_b16 a[172:173], v10 offset:12288\n" \
    "ds_read_b64_tr_b16 a[174:175], v11 offset:12288\n" \
    "s_waitcnt lgkmcnt(0)\n" \
    "s_mov_b32 s68, 4456448\n" \
    "s_add_u32 m0, s52, 12288\n" \
    "s_nop 0\n" \
    "buffer_load_dwordx4 v0, s[40:43], s68 offen lds\n" \
    "s_add_u32 m0, s52, 13312\n" \
    "s_nop 0\n" \
    "buffer_load_dwordx4 v1, s[40:43], s68 offen lds\n" \
    "s_add_u32 m0, s52, 14336\n" \
    "s_nop 0\n" \
    "buffer_load_dwordx4 v2, s[40:43], s68 offen lds\n" \
    "s_add_u32 m0, s52, 15360\n" \
    "s_nop 0\n" \
    "buffer_load_dwordx4 v3, s[40:43], s68 offen lds\n" \
    "s_waitcnt vmcnt(24)\n" \
    "ds_read_b64_tr_b16 a[176:177], v4 offset:16384\n" \
    "ds_read_b64_tr_b16 a[178:179], v5 offset:16384\n" \
    "ds_read_b64_tr_b16 a[180:181], v6 offset:16384\n" \
    "ds_read_b64_tr_b16 a[182:183], v7 offset:16384\n" \
    "ds_read_b64_tr_b16 a[184:185], v8 offset:16384\n" \
    "ds_read_b64_tr_b16 a[186:187], v9 offset:16384\n" \
    "ds_read_b64_tr_b16 a[188:189], v10 offset:16384\n" \
    "ds_read_b64_tr_b16 a[190:191], v11 offset:16384\n" \
    "s_waitcnt lgkmcnt(0)\n" \
    "s_mov_b32 s68, 4718592\n" \
    "s_add_u32 m0, s52, 16384\n" \
    "s_nop 0\n" \
    "buffer_load_dwordx4 v0, s[40:43], s68 offen lds\n" \
    "s_add_u32 m0, s52, 17408\n" \
    "s_nop 0\n" \
    "buffer_load_dwordx4 v1, s[40:43], s68 offen lds\n" \
    "s_add_u32 m0, s52, 18432\n" \
    "s_nop 0\n" \
    "buffer_load_dwordx4 v2, s[40:43], s68 offen lds\n" \
    "s_add_u32 m0, s52, 19456\n" \
    "s_nop 0\n" \
    "buffer_load_dwordx4 v3, s[40:43], s68 offen lds\n" \
    "s_waitcnt vmcnt(24)\n" \
    "ds_read_b64_tr_b16 a[192:193], v4 offset:20480\n" \
    "ds_read_b64_tr_b16 a[194:195], v5 offset:20480\n" \
    "ds_read_b64_tr_b16 a[196:197], v6 offset:20480\n" \
    "ds_read_b64_tr_b16 a[198:199], v7 offset:20480\n" \
    "ds_read_b64_tr_b16 a[200:201], v8 offset:20480\n" \
    "ds_read_b64_tr_b16 a[202:203], v9 offset:20480\n" \
    "ds_read_b64_tr_b16 a[204:205], v10 offset:20480\n" \
    "ds_read_b64_tr_b16 a[206:207], v11 offset:20480\n" \
    "s_waitcnt lgkmcnt(0)\n" \
    "s_mov_b32 s68, 4980736\n" \
    "s_add_u32 m0, s52, 20480\n" \
    "s_nop 0\n" \
    "buffer_load_dwordx4 v0, s[40:43], s68 offen lds\n" \
    "s_add_u32 m0, s52, 21504\n" \
    "s_nop 0\n" \
    "buffer_load_dwordx4 v1, s[40:43], s68 offen lds\n" \
    "s_add_u32 m0, s52, 22528\n" \
    "s_nop 0\n" \
    "buffer_load_dwordx4 v2, s[40:43], s68 offen lds\n" \
    "s_add_u32 m0, s52, 23552\n" \
    "s_nop 0\n" \
    "buffer_load_dwordx4 v3, s[40:43], s68 offen lds\n" \
    "s_memrealtime s[80:81]\n" \
    "s_waitcnt lgkmcnt(0)\n" \
    "s_mov_b32 s83, s80\n" \
    "s_mov_b32 s82, 0\n" \
    "P0_ln_%=:\n" \
    "buffer_load_dwordx2 v[22:23], v19, s[36:39], 0 offen sc1\n" \
    "s_waitcnt vmcnt(0)\n" \
    "v_cmp_eq_u32 vcc, s63, v22\n" \
    "v_cmp_eq_u32 s[76:77], s63, v23\n" \
    "s_and_b64 vcc, vcc, s[76:77]\n" \
    "s_and_b64 vcc, vcc, 7\n" \
    "s_cmp_eq_u64 vcc, 7\n" \
    "s_cbranch_scc0 P0_ln_no_%=\n" \
    "s_add_u32 s40, s54, 2097152\n" \
    "s_add_u32 s41, s40, 4096\n" \
    "s_add_u32 s42, s40, 8192\n" \
    "s_add_u32 s43, s54, 114688\n" \
    "s_add_u32 m0, s43, 0\n" \
    "s_nop 0\n" \
    "buffer_load_dwordx4 v16, s[36:39], s40 offen offset:0 sc1 lds\n" \
    "s_add_u32 m0, s43, 0\n" \
    "s_nop 0\n" \
    "buffer_load_dwordx4 v16, s[36:39], s40 offen offset:1024 sc1 lds\n" \
    "s_add_u32 m0, s43, 0\n" \
    "s_nop 0\n" \
    "buffer_load_dwordx4 v16, s[36:39], s40 offen offset:2048 sc1 lds\n" \
    "s_add_u32 m0, s43, 0\n" \
    "s_nop 0\n" \
    "buffer_load_dwordx4 v16, s[36:39], s40 offen offset:3072 sc1 lds\n" \
    "s_add_u32 m0, s43, 4096\n" \
    "s_nop 0\n" \
    "buffer_load_dwordx4 v16, s[36:39], s41 offen offset:0 sc1 lds\n" \
    "s_add_u32 m0, s43, 4096\n" \
    "s_nop 0\n" \
    "buffer_load_dwordx4 v16, s[36:39], s41 offen offset:1024 sc1 lds\n" \
    "s_add_u32 m0, s43, 4096\n" \
    "s_nop 0\n" \
    "buffer_load_dwordx4 v16, s[36:39], s41 offen offset:2048 sc1 lds\n" \
    "s_add_u32 m0, s43, 4096\n" \
    "s_nop 0\n" \
    "buffer_load_dwordx4 v16, s[36:39], s41 offen offset:3072 sc1 lds\n" \
    "s_add_u32 m0, s43, 8192\n" \
    "s_nop 0\n" \
    "buffer_load_dwordx4 v16, s[36:39], s42 offen offset:0 sc1 lds\n" \
    "s_add_u32 m0, s43, 8192\n" \
    "s_nop 0\n" \
    "buffer_load_dwordx4 v16, s[36:39], s42 offen offset:1024 sc1 lds\n" \
    "v_add_u32 v21, 114688, v16\n" \
    "ds_read_b64_tr_b16 a[208:209], v4 offset:24576\n" \
    "ds_read_b64_tr_b16 a[210:211], v5 offset:24576\n" \
    "ds_read_b64_tr_b16 a[212:213], v6 offset:24576\n" \
    "ds_read_b64_tr_b16 a[214:215], v7 offset:24576\n" \
    "ds_read_b64_tr_b16 a[216:217], v8 offset:24576\n" \
    "ds_read_b64_tr_b16 a[218:219], v9 offset:24576\n" \
    "ds_read_b64_tr_b16 a[220:221], v10 offset:24576\n" \
    "ds_read_b64_tr_b16 a[222:223], v11 offset:24576\n" \
    "s_waitcnt lgkmcnt(0)\n" \
    "ds_read_b64_tr_b16 a[224:225], v4 offset:0\n" \
    "ds_read_b64_tr_b16 a[226:227], v5 offset:0\n" \
    "ds_read_b64_tr_b16 a[228:229], v6 offset:0\n" \
    "ds_read_b64_tr_b16 a[230:231], v7 offset:0\n" \
    "ds_read_b64_tr_b16 a[232:233], v8 offset:0\n" \
    "ds_read_b64_tr_b16 a[234:235], v9 offset:0\n" \
    "ds_read_b64_tr_b16 a[236:237], v10 offset:0\n" \
    "ds_read_b64_tr_b16 a[238:239], v11 offset:0\n" \
    "s_waitcnt lgkmcnt(0)\n" \
    "ds_read_b64_tr_b16 a[240:241], v4 offset:4096\n" \
    "ds_read_b64_tr_b16 a[242:243], v5 offset:4096\n" \
    "ds_read_b64_tr_b16 a[244:245], v6 offset:4096\n" \
    "ds_read_b64_tr_b16 a[246:247], v7 offset:4096\n" \
    "ds_read_b64_tr_b16 a[248:249], v8 offset:4096\n" \
    "ds_read_b64_tr_b16 a[250:251], v9 offset:4096\n" \
    "ds_read_b64_tr_b16 a[252:253], v10 offset:4096\n" \
    "ds_read_b64_tr_b16 a[254:255], v11 offset:4096\n" \
    "s_waitcnt lgkmcnt(0)\n" \
    "ds_read_b64_tr_b16 v[192:193], v4 offset:8192\n" \
    "ds_read_b64_tr_b16 v[194:195], v5 offset:8192\n" \
    "ds_read_b64_tr_b16 v[196:197], v6 offset:8192\n" \
    "ds_read_b64_tr_b16 v[198:199], v7 offset:8192\n" \
    "ds_read_b64_tr_b16 v[200:201], v8 offset:8192\n" \
    "ds_read_b64_tr_b16 v[202:203], v9 offset:8192\n" \
    "ds_read_b64_tr_b16 v[204:205], v10 offset:8192\n" \
    "ds_read_b64_tr_b16 v[206:207], v11 offset:8192\n" \
    "s_waitcnt lgkmcnt(0)\n" \
    "ds_read_b64_tr_b16 v[208:209], v4 offset:12288\n" \
    "ds_read_b64_tr_b16 v[210:211], v5 offset:12288\n" \
    "ds_read_b64_tr_b16 v[212:213], v6 offset:12288\n" \
    "ds_read_b64_tr_b16 v[214:215], v7 offset:12288\n" \
    "ds_read_b64_tr_b16 v[216:217], v8 offset:12288\n" \
    "ds_read_b64_tr_b16 v[218:219], v9 offset:12288\n" \
    "ds_read_b64_tr_b16 v[220:221], v10 offset:12288\n" \
    "ds_read_b64_tr_b16 v[222:223], v11 offset:12288\n" \
    "s_waitcnt lgkmcnt(0)\n" \
    "ds_read_b64_tr_b16 v[224:225], v4 offset:16384\n" \
    "ds_read_b64_tr_b16 v[226:227], v5 offset:16384\n" \
    "ds_read_b64_tr_b16 v[228:229], v6 offset:16384\n" \
    "ds_read_b64_tr_b16 v[230:231], v7 offset:16384\n" \
    "ds_read_b64_tr_b16 v[232:233], v8 offset:16384\n" \
    "ds_read_b64_tr_b16 v[234:235], v9 offset:16384\n" \
    "ds_read_b64_tr_b16 v[236:237], v10 offset:16384\n" \
    "ds_read_b64_tr_b16 v[238:239], v11 offset:16384\n" \
    "s_waitcnt lgkmcnt(0)\n" \
    "ds_read_b64_tr_b16 v[240:241], v4 offset:20480\n" \
    "ds_read_b64_tr_b16 v[242:243], v5 offset:20480\n" \
    "ds_read_b64_tr_b16 v[244:245], v6 offset:20480\n" \
    "ds_read_b64_tr_b16 v[246:247], v7 offset:20480\n" \
    "ds_read_b64_tr_b16 v[248:249], v8 offset:20480\n" \
    "ds_read_b64_tr_b16 v[250:251], v9 offset:20480\n" \
    "ds_read_b64_tr_b16 v[252:253], v10 offset:20480\n" \
    "ds_read_b64_tr_b16 v[254:255], v11 offset:20480\n" \
    "s_waitcnt lgkmcnt(0)\n" \
    "s_mov_b32 s70, 0\n" \
    "s_mov_b32 s72, 114688\n" \
    "s_waitcnt vmcnt(0)\n" \
    "s_barrier\n" \
    "ds_read_b128 v[96:99], v21 offset:0\n" \
    "ds_read_b128 v[100:103], v21 offset:20480\n" \
    "ds_read_b128 v[104:107], v21 offset:1024\n" \
    "ds_read_b128 v[108:111], v21 offset:21504\n" \
    "s_add_u32 s40, s54, 2138112\n" \
    "s_add_u32 s41, s40, 4096\n" \
    "s_add_u32 s42, s40, 8192\n" \
    "s_add_u32 s43, s54, 0\n" \
    "s_add_u32 m0, s43, 0\n" \
    "s_nop 0\n" \
    "buffer_load_dwordx4 v16, s[36:39], s40 offen offset:0 sc1 lds\n" \
    "s_add_u32 m0, s43, 0\n" \
    "s_nop 0\n" \
    "buffer_load_dwordx4 v16, s[36:39], s40 offen offset:1024 sc1 lds\n" \
    "s_add_u32 m0, s43, 0\n" \
    "s_nop 0\n" \
    "buffer_load_dwordx4 v16, s[36:39], s40 offen offset:2048 sc1 lds\n" \
    "s_add_u32 m0, s43, 0\n" \
    "s_nop 0\n" \
    "buffer_load_dwordx4 v16, s[36:39], s40 offen offset:3072 sc1 lds\n" \
    "s_add_u32 m0, s43, 4096\n" \
    "s_nop 0\n" \
    "buffer_load_dwordx4 v16, s[36:39], s41 offen offset:0 sc1 lds\n" \
    "s_add_u32 m0, s43, 4096\n" \
    "s_nop 0\n" \
    "buffer_load_dwordx4 v16, s[36:39], s41 offen offset:1024 sc1 lds\n" \
    "s_add_u32 m0, s43, 4096\n" \
    "s_nop 0\n" \
    "buffer_load_dwordx4 v16, s[36:39], s41 offen offset:2048 sc1 lds\n" \
    "s_add_u32 m0, s43, 4096\n" \
    "s_nop 0\n" \
    "buffer_load_dwordx4 v16, s[36:39], s41 offen offset:3072 sc1 lds\n" \
    "s_add_u32 m0, s43, 8192\n" \
    "s_nop 0\n" \
    "buffer_load_dwordx4 v16, s[36:39], s42 offen offset:0 sc1 lds\n" \
    "s_add_u32 m0, s43, 8192\n" \
    "s_nop 0\n" \
    "buffer_load_dwordx4 v16, s[36:39], s42 offen offset:1024 sc1 lds\n" \
    "s_nop 0\n" \
    "s_add_u32 s40, s54, 2179072\n" \
    "s_add_u32 s41, s40, 4096\n" \
    "s_add_u32 s42, s40, 8192\n" \
    "s_add_u32 s43, s54, 40960\n" \
    "s_waitcnt lgkmcnt(2)\n" \
    "v_mfma_f32_16x16x32_bf16 v[64:67], a[0:3], v[96:99], 0\n" \
    "ds_read_b128 v[112:115], v21 offset:2048\n" \
    "v_mfma_f32_16x16x32_bf16 v[80:83], a[0:3], v[100:103], 0\n" \
    "ds_read_b128 v[116:119], v21 offset:22528\n" \
    "v_mfma_f32_16x16x32_bf16 v[68:71], a[4:7], v[96:99], 0\n" \
    "s_add_u32 m0, s43, 0\n" \
    "v_mfma_f32_16x16x32_bf16 v[84:87], a[4:7], v[100:103], 0\n" \
    "buffer_load_dwordx4 v16, s[36:39], s40 offen offset:0 sc1 lds\n" \
    "v_mfma_f32_16x16x32_bf16 v[72:75], a[8:11], v[96:99], 0\n" \
    "v_mfma_f32_16x16x32_bf16 v[88:91], a[8:11], v[100:103], 0\n" \
    "v_mfma_f32_16x16x32_bf16 v[76:79], a[12:15], v[96:99], 0\n" \
    "v_mfma_f32_16x16x32_bf16 v[92:95], a[12:15], v[100:103], 0\n" \
    "s_waitcnt lgkmcnt(2)\n" \
    "v_mfma_f32_16x16x32_bf16 v[64:67], a[16:19], v[104:107], v[64:67]\n" \
    "ds_read_b128 v[120:123], v21 offset:3072\n" \
    "v_mfma_f32_16x16x32_bf16 v[80:83], a[16:19], v[108:111], v[80:83]\n" \
    "ds_read_b128 v[124:127], v21 offset:23552\n" \
    "v_mfma_f32_16x16x32_bf16 v[68:71], a[20:23], v[104:107], v[68:71]\n" \
    "s_add_u32 m0, s43, 0\n" \
    "v_mfma_f32_16x16x32_bf16 v[84:87], a[20:23], v[108:111], v[84:87]\n" \
    "buffer_load_dwordx4 v16, s[36:39], s40 offen offset:1024 sc1 lds\n" \
    "v_mfma_f32_16x16x32_bf16 v[72:75], a[24:27], v[104:107], v[72:75]\n" \
    "v_mfma_f32_16x16x32_bf16 v[88:91], a[24:27], v[108:111], v[88:91]\n" \
    "v_mfma_f32_16x16x32_bf16 v[76:79], a[28:31], v[104:107], v[76:79]\n" \
    "v_mfma_f32_16x16x32_bf16 v[92:95], a[28:31], v[108:111], v[92:95]\n" \
    "s_waitcnt lgkmcnt(2)\n" \
    "v_mfma_f32_16x16x32_bf16 v[64:67], a[32:35], v[112:115], v[64:67]\n" \
    "ds_read_b128 v[96:99], v21 offset:4096\n" \
    "v_mfma_f32_16x16x32_bf16 v[80:83], a[32:35], v[116:119], v[80:83]\n" \
    "ds_read_b128 v[100:103], v21 offset:24576\n" \
    "v_mfma_f32_16x16x32_bf16 v[68:71], a[36:39], v[112:115], v[68:71]\n" \
    "s_add_u32 m0, s43, 0\n" \
    "v_mfma_f32_16x16x32_bf16 v[84:87], a[36:39], v[116:119], v[84:87]\n" \
    "buffer_load_dwordx4 v16, s[36:39], s40 offen offset:2048 sc1 lds\n" \
    "v_mfma_f32_16x16x32_bf16 v[72:75], a[40:43], v[112:115], v[72:75]\n" \
    "v_mfma_f32_16x16x32_bf16 v[88:91], a[40:43], v[116:119], v[88:91]\n" \
    "v_mfma_f32_16x16x32_bf16 v[76:79], a[44:47], v[112:115], v[76:79]\n" \
    "v_mfma_f32_16x16x32_bf16 v[92:95], a[44:47], v[116:119], v[92:95]\n" \
    "s_waitcnt lgkmcnt(2)\n" \
    "v_mfma_f32_16x16x32_bf16 v[64:67], a[48:51], v[120:123], v[64:67]\n" \
    "ds_read_b128 v[104:107], v21 offset:5120\n" \
    "v_mfma_f32_16x16x32_bf16 v[80:83], a[48:51], v[124:127], v[80:83]\n" \
    "ds_read_b128 v[108:111], v21 offset:25600\n" \
    "v_mfma_f32_16x16x32_bf16 v[68:71], a[52:55], v[120:123], v[68:71]\n" \
    "s_add_u32 m0, s43, 0\n" \
    "v_mfma_f32_16x16x32_bf16 v[84:87], a[52:55], v[124:127], v[84:87]\n" \
    "buffer_load_dwordx4 v16, s[36:39], s40 offen offset:3072 sc1 lds\n" \
    "v_mfma_f32_16x16x32_bf16 v[72:75], a[56:59], v[120:123], v[72:75]\n" \
    "v_mfma_f32_16x16x32_bf16 v[88:91], a[56:59], v[124:127], v[88:91]\n" \
    "v_mfma_f32_16x16x32_bf16 v[76:79], a[60:63], v[120:123], v[76:79]\n" \
    "v_mfma_f32_16x16x32_bf16 v[92:95], a[60:63], v[124:127], v[92:95]\n" \
    "s_waitcnt lgkmcnt(2)\n" \
    "v_mfma_f32_16x16x32_bf16 v[64:67], a[64:67], v[96:99], v[64:67]\n" \
    "ds_read_b128 v[112:115], v21 offset:6144\n" \
    "v_mfma_f32_16x16x32_bf16 v[80:83], a[64:67], v[100:103], v[80:83]\n" \
    "ds_read_b128 v[116:119], v21 offset:26624\n" \
    "v_mfma_f32_16x16x32_bf16 v[68:71], a[68:71], v[96:99], v[68:71]\n" \
    "s_add_u32 m0, s43, 4096\n" \
    "v_mfma_f32_16x16x32_bf16 v[84:87], a[68:71], v[100:103], v[84:87]\n" \
    "buffer_load_dwordx4 v16, s[36:39], s41 offen offset:0 sc1 lds\n" \
    "v_mfma_f32_16x16x32_bf16 v[72:75], a[72:75], v[96:99], v[72:75]\n" \
    "v_mfma_f32_16x16x32_bf16 v[88:91], a[72:75], v[100:103], v[88:91]\n" \
    "v_mfma_f32_16x16x32_bf16 v[76:79], a[76:79], v[96:99], v[76:79]\n" \
    "v_mfma_f32_16x16x32_bf16 v[92:95], a[76:79], v[100:103], v[92:95]\n" \
    "s_waitcnt lgkmcnt(2)\n" \
    "v_mfma_f32_16x16x32_bf16 v[64:67], a[80:83], v[104:107], v[64:67]\n" \
    "ds_read_b128 v[120:123], v21 offset:7168\n" \
    "v_mfma_f32_16x16x32_bf16 v[80:83], a[80:83], v[108:111], v[80:83]\n" \
    "ds_read_b128 v[124:127], v21 offset:27648\n" \
    "v_mfma_f32_16x16x32_bf16 v[68:71], a[84:87], v[104:107], v[68:71]\n" \
    "s_add_u32 m0, s43, 4096\n" \
    "v_mfma_f32_16x16x32_bf16 v[84:87], a[84:87], v[108:111], v[84:87]\n" \
    "buffer_load_dwordx4 v16, s[36:39], s41 offen offset:1024 sc1 lds\n" \
    "v_mfma_f32_16x16x32_bf16 v[72:75], a[88:91], v[104:107], v[72:75]\n" \
    "v_mfma_f32_16x16x32_bf16 v[88:91], a[88:91], v[108:111], v[88:91]\n" \
    "v_mfma_f32_16x16x32_bf16 v[76:79], a[92:95], v[104:107], v[76:79]\n" \
    "v_mfma_f32_16x16x32_bf16 v[92:95], a[92:95], v[108:111], v[92:95]\n" \
    "s_waitcnt lgkmcnt(2)\n" \
    "v_mfma_f32_16x16x32_bf16 v[64:67], a[96:99], v[112:115], v[64:67]\n" \
    "ds_read_b128 v[96:99], v21 offset:8192\n" \
    "v_mfma_f32_16x16x32_bf16 v[80:83], a[96:99], v[116:119], v[80:83]\n" \
    "ds_read_b128 v[100:103], v21 offset:28672\n" \
    "v_mfma_f32_16x16x32_bf16 v[68:71], a[100:103], v[112:115], v[68:71]\n" \
    "s_add_u32 m0, s43, 4096\n" \
    "v_mfma_f32_16x16x32_bf16 v[84:87], a[100:103], v[116:119], v[84:87]\n" \
    "buffer_load_dwordx4 v16, s[36:39], s41 offen offset:2048 sc1 lds\n" \
    "v_mfma_f32_16x16x32_bf16 v[72:75], a[104:107], v[112:115], v[72:75]\n" \
    "v_mfma_f32_16x16x32_bf16 v[88:91], a[104:107], v[116:119], v[88:91]\n" \
    "v_mfma_f32_16x16x32_bf16 v[76:79], a[108:111], v[112:115], v[76:79]\n" \
    "v_mfma_f32_16x16x32_bf16 v[92:95], a[108:111], v[116:119], v[92:95]\n" \
    "s_waitcnt lgkmcnt(2)\n" \
    "v_mfma_f32_16x16x32_bf16 v[64:67], a[112:115], v[120:123], v[64:67]\n" \
    "ds_read_b128 v[104:107], v21 offset:9216\n" \
    "v_mfma_f32_16x16x32_bf16 v[80:83], a[112:115], v[124:127], v[80:83]\n" \
    "ds_read_b128 v[108:111], v21 offset:29696\n" \
    "v_mfma_f32_16x16x32_bf16 v[68:71], a[116:119], v[120:123], v[68:71]\n" \
    "s_add_u32 m0, s43, 4096\n" \
    "v_mfma_f32_16x16x32_bf16 v[84:87], a[116:119], v[124:127], v[84:87]\n" \
    "buffer_load_dwordx4 v16, s[36:39], s41 offen offset:3072 sc1 lds\n" \
    "v_mfma_f32_16x16x32_bf16 v[72:75], a[120:123], v[120:123], v[72:75]\n" \
    "v_mfma_f32_16x16x32_bf16 v[88:91], a[120:123], v[124:127], v[88:91]\n" \
    "v_mfma_f32_16x16x32_bf16 v[76:79], a[124:127], v[120:123], v[76:79]\n" \
    "v_mfma_f32_16x16x32_bf16 v[92:95], a[124:127], v[124:127], v[92:95]\n" \
    "s_waitcnt lgkmcnt(2)\n" \
    "v_mfma_f32_16x16x32_bf16 v[64:67], a[128:131], v[96:99], v[64:67]\n" \
    "ds_read_b128 v[112:115], v21 offset:10240\n" \
    "v_mfma_f32_16x16x32_bf16 v[80:83], a[128:131], v[100:103], v[80:83]\n" \
    "ds_read_b128 v[116:119], v21 offset:30720\n" \
    "v_mfma_f32_16x16x32_bf16 v[68:71], a[132:135], v[96:99], v[68:71]\n" \
    "s_add_u32 m0, s43, 8192\n" \
    "v_mfma_f32_16x16x32_bf16 v[84:87], a[132:135], v[100:103], v[84:87]\n" \
    "buffer_load_dwordx4 v16, s[36:39], s42 offen offset:0 sc1 lds\n" \
    "v_mfma_f32_16x16x32_bf16 v[72:75], a[136:139], v[96:99], v[72:75]\n" \
    "v_mfma_f32_16x16x32_bf16 v[88:91], a[136:139], v[100:103], v[88:91]\n" \
    "v_mfma_f32_16x16x32_bf16 v[76:79], a[140:143], v[96:99], v[76:79]\n" \
    "v_mfma_f32_16x16x32_bf16 v[92:95], a[140:143], v[100:103], v[92:95]\n" \
    "s_waitcnt lgkmcnt(2)\n" \
    "v_mfma_f32_16x16x32_bf16 v[64:67], a[144:147], v[104:107], v[64:67]\n" \
    "ds_read_b128 v[120:123], v21 offset:11264\n" \
    "v_mfma_f32_16x16x32_bf16 v[80:83], a[144:147], v[108:111], v[80:83]\n" \
    "ds_read_b128 v[124:127], v21 offset:31744\n" \
    "v_mfma_f32_16x16x32_bf16 v[68:71], a[148:151], v[104:107], v[68:71]\n" \
    "s_add_u32 m0, s43, 8192\n" \
    "v_mfma_f32_16x16x32_bf16 v[84:87], a[148:151], v[108:111], v[84:87]\n" \
    "buffer_load_dwordx4 v16, s[36:39], s42 offen offset:1024 sc1 lds\n" \
    "v_mfma_f32_16x16x32_bf16 v[72:75], a[152:155], v[104:107], v[72:75]\n" \
    "v_mfma_f32_16x16x32_bf16 v[88:91], a[152:155], v[108:111], v[88:91]\n" \
    "v_mfma_f32_16x16x32_bf16 v[76:79], a[156:159], v[104:107], v[76:79]\n" \
    "v_mfma_f32_16x16x32_bf16 v[92:95], a[156:159], v[108:111], v[92:95]\n" \
    "s_waitcnt lgkmcnt(2)\n" \
    "v_mfma_f32_16x16x32_bf16 v[64:67], a[160:163], v[112:115], v[64:67]\n" \
    "ds_read_b128 v[96:99], v21 offset:12288\n" \
    "v_mfma_f32_16x16x32_bf16 v[80:83], a[160:163], v[116:119], v[80:83]\n" \
    "ds_read_b128 v[100:103], v21 offset:32768\n" \
    "v_mfma_f32_16x16x32_bf16 v[68:71], a[164:167], v[112:115], v[68:71]\n" \
    "v_mfma_f32_16x16x32_bf16 v[84:87], a[164:167], v[116:119], v[84:87]\n" \
    "v_mfma_f32_16x16x32_bf16 v[72:75], a[168:171], v[112:115], v[72:75]\n" \
    "v_mfma_f32_16x16x32_bf16 v[88:91], a[168:171], v[116:119], v[88:91]\n" \
    "v_mfma_f32_16x16x32_bf16 v[76:79], a[172:175], v[112:115], v[76:79]\n" \
    "v_mfma_f32_16x16x32_bf16 v[92:95], a[172:175], v[116:119], v[92:95]\n" \
    "s_waitcnt lgkmcnt(2)\n" \
    "v_mfma_f32_16x16x32_bf16 v[64:67], a[176:179], v[120:123], v[64:67]\n" \
    "ds_read_b128 v[104:107], v21 offset:13312\n" \
    "v_mfma_f32_16x16x32_bf16 v[80:83], a[176:179], v[124:127], v[80:83]\n" \
    "ds_read_b128 v[108:111], v21 offset:33792\n" \
    "v_mfma_f32_16x16x32_bf16 v[68:71], a[180:183], v[120:123], v[68:71]\n" \
    "v_mfma_f32_16x16x32_bf16 v[84:87], a[180:183], v[124:127], v[84:87]\n" \
    "v_mfma_f32_16x16x32_bf16 v[72:75], a[184:187], v[120:123], v[72:75]\n" \
    "v_mfma_f32_16x16x32_bf16 v[88:91], a[184:187], v[124:127], v[88:91]\n" \
    "v_mfma_f32_16x16x32_bf16 v[76:79], a[188:191], v[120:123], v[76:79]\n" \
    "v_mfma_f32_16x16x32_bf16 v[92:95], a[188:191], v[124:127], v[92:95]\n" \
    "s_waitcnt lgkmcnt(2)\n" \
    "v_mfma_f32_16x16x32_bf16 v[64:67], a[192:195], v[96:99], v[64:67]\n" \
    "ds_read_b128 v[112:115], v21 offset:14336\n" \
    "v_mfma_f32_16x16x32_bf16 v[80:83], a[192:195], v[100:103], v[80:83]\n" \
    "ds_read_b128 v[116:119], v21 offset:34816\n" \
    "v_mfma_f32_16x16x32_bf16 v[68:71], a[196:199], v[96:99], v[68:71]\n" \
    "v_mfma_f32_16x16x32_bf16 v[84:87], a[196:199], v[100:103], v[84:87]\n" \
    "v_mfma_f32_16x16x32_bf16 v[72:75], a[200:203], v[96:99], v[72:75]\n" \
    "v_mfma_f32_16x16x32_bf16 v[88:91], a[200:203], v[100:103], v[88:91]\n" \
    "v_mfma_f32_16x16x32_bf16 v[76:79], a[204:207], v[96:99], v[76:79]\n" \
    "v_mfma_f32_16x16x32_bf16 v[92:95], a[204:207], v[100:103], v[92:95]\n" \
    "s_waitcnt lgkmcnt(2)\n" \
    "v_mfma_f32_16x16x32_bf16 v[64:67], a[208:211], v[104:107], v[64:67]\n" \
    "ds_read_b128 v[120:123], v21 offset:15360\n" \
    "v_mfma_f32_16x16x32_bf16 v[80:83], a[208:211], v[108:111], v[80:83]\n" \
    "ds_read_b128 v[124:127], v21 offset:35840\n" \
    "v_mfma_f32_16x16x32_bf16 v[68:71], a[212:215], v[104:107], v[68:71]\n" \
    "v_mfma_f32_16x16x32_bf16 v[84:87], a[212:215], v[108:111], v[84:87]\n" \
    "v_mfma_f32_16x16x32_bf16 v[72:75], a[216:219], v[104:107], v[72:75]\n" \
    "v_mfma_f32_16x16x32_bf16 v[88:91], a[216:219], v[108:111], v[88:91]\n" \
    "v_mfma_f32_16x16x32_bf16 v[76:79], a[220:223], v[104:107], v[76:79]\n" \
    "v_mfma_f32_16x16x32_bf16 v[92:95], a[220:223], v[108:111], v[92:95]\n" \
    "s_waitcnt lgkmcnt(2)\n" \
    "v_mfma_f32_16x16x32_bf16 v[64:67], a[224:227], v[112:115], v[64:67]\n" \
    "ds_read_b128 v[96:99], v21 offset:16384\n" \
    "v_mfma_f32_16x16x32_bf16 v[80:83], a[224:227], v[116:119], v[80:83]\n" \
    "ds_read_b128 v[100:103], v21 offset:36864\n" \
    "v_mfma_f32_16x16x32_bf16 v[68:71], a[228:231], v[112:115], v[68:71]\n" \
    "v_mfma_f32_16x16x32_bf16 v[84:87], a[228:231], v[116:119], v[84:87]\n" \
    "v_mfma_f32_16x16x32_bf16 v[72:75], a[232:235], v[112:115], v[72:75]\n" \
    "v_mfma_f32_16x16x32_bf16 v[88:91], a[232:235], v[116:119], v[88:91]\n" \
    "v_mfma_f32_16x16x32_bf16 v[76:79], a[236:239], v[112:115], v[76:79]\n" \
    "v_mfma_f32_16x16x32_bf16 v[92:95], a[236:239], v[116:119], v[92:95]\n" \
    "s_waitcnt lgkmcnt(2)\n" \
    "v_mfma_f32_16x16x32_bf16 v[64:67], a[240:243], v[120:123], v[64:67]\n" \
    "ds_read_b128 v[104:107], v21 offset:17408\n" \
    "v_mfma_f32_16x16x32_bf16 v[80:83], a[240:243], v[124:127], v[80:83]\n" \
    "ds_read_b128 v[108:111], v21 offset:37888\n" \
    "v_mfma_f32_16x16x32_bf16 v[68:71], a[244:247], v[120:123], v[68:71]\n" \
    "v_mfma_f32_16x16x32_bf16 v[84:87], a[244:247], v[124:127], v[84:87]\n" \
    "v_mfma_f32_16x16x32_bf16 v[72:75], a[248:251], v[120:123], v[72:75]\n" \
    "v_mfma_f32_16x16x32_bf16 v[88:91], a[248:251], v[124:127], v[88:91]\n" \
    "v_mfma_f32_16x16x32_bf16 v[76:79], a[252:255], v[120:123], v[76:79]\n" \
    "v_mfma_f32_16x16x32_bf16 v[92:95], a[252:255], v[124:127], v[92:95]\n" \
    "s_waitcnt lgkmcnt(2)\n" \
    "v_mfma_f32_16x16x32_bf16 v[64:67], v[192:195], v[96:99], v[64:67]\n" \
    "ds_read_b128 v[112:115], v21 offset:18432\n" \
    "v_mfma_f32_16x16x32_bf16 v[80:83], v[192:195], v[100:103], v[80:83]\n" \
    "ds_read_b128 v[116:119], v21 offset:38912\n" \
    "v_mfma_f32_16x16x32_bf16 v[68:71], v[196:199], v[96:99], v[68:71]\n" \
    "v_mfma_f32_16x16x32_bf16 v[84:87], v[196:199], v[100:103], v[84:87]\n" \
    "v_mfma_f32_16x16x32_bf16 v[72:75], v[200:203], v[96:99], v[72:75]\n" \
    "v_mfma_f32_16x16x32_bf16 v[88:91], v[200:203], v[100:103], v[88:91]\n" \
    "v_mfma_f32_16x16x32_bf16 v[76:79], v[204:207], v[96:99], v[76:79]\n" \
    "v_mfma_f32_16x16x32_bf16 v[92:95], v[204:207], v[100:103], v[92:95]\n" \
    "s_waitcnt lgkmcnt(2)\n" \
    "v_mfma_f32_16x16x32_bf16 v[64:67], v[208:211], v[104:107], v[64:67]\n" \
    "ds_read_b128 v[120:123], v21 offset:19456\n" \
    "v_mfma_f32_16x16x32_bf16 v[80:83], v[208:211], v[108:111], v[80:83]\n" \
    "ds_read_b128 v[124:127], v21 offset:39936\n" \
    "v_mfma_f32_16x16x32_bf16 v[68:71], v[212:215], v[104:107], v[68:71]\n" \
    "v_mfma_f32_16x16x32_bf16 v[84:87], v[212:215], v[108:111], v[84:87]\n" \
    "v_mfma_f32_16x16x32_bf16 v[72:75], v[216:219], v[104:107], v[72:75]\n" \
    "v_mfma_f32_16x16x32_bf16 v[88:91], v[216:219], v[108:111], v[88:91]\n" \
    "v_mfma_f32_16x16x32_bf16 v[76:79], v[220:223], v[104:107], v[76:79]\n" \
    "v_mfma_f32_16x16x32_bf16 v[92:95], v[220:223], v[108:111], v[92:95]\n" \
    "s_add_u32 s75, s72, s54\n" \
    "s_add_u32 s72, s72, 40960\n" \
    "s_cmp_eq_u32 s72, 81920\n" \
    "s_cselect_b32 s72, 114688, s72\n" \
    "s_cmp_eq_u32 s72, 155648\n" \
    "s_cselect_b32 s72, 0, s72\n" \
    "v_add_u32 v21, s72, v16\n" \
    "s_waitcnt lgkmcnt(0) vmcnt(10)\n" \
    "s_barrier\n" \
    "v_mfma_f32_16x16x32_bf16 v[64:67], v[224:227], v[112:115], v[64:67]\n" \
    "ds_read_b128 v[96:99], v21 offset:0\n" \
    "v_mfma_f32_16x16x32_bf16 v[80:83], v[224:227], v[116:119], v[80:83]\n" \
    "ds_read_b128 v[100:103], v21 offset:20480\n" \
    "v_mfma_f32_16x16x32_bf16 v[68:71], v[228:231], v[112:115], v[68:71]\n" \
    "v_mfma_f32_16x16x32_bf16 v[84:87], v[228:231], v[116:119], v[84:87]\n" \
    "v_mfma_f32_16x16x32_bf16 v[72:75], v[232:235], v[112:115], v[72:75]\n" \
    "v_mfma_f32_16x16x32_bf16 v[88:91], v[232:235], v[116:119], v[88:91]\n" \
    "v_mfma_f32_16x16x32_bf16 v[76:79], v[236:239], v[112:115], v[76:79]\n" \
    "v_mfma_f32_16x16x32_bf16 v[92:95], v[236:239], v[116:119], v[92:95]\n" \
    "v_mfma_f32_16x16x32_bf16 v[64:67], v[240:243], v[120:123], v[64:67]\n" \
    "ds_read_b128 v[104:107], v21 offset:1024\n" \
    "v_mfma_f32_16x16x32_bf16 v[80:83], v[240:243], v[124:127], v[80:83]\n" \
    "ds_read_b128 v[108:111], v21 offset:21504\n" \
    "v_mfma_f32_16x16x32_bf16 v[68:71], v[244:247], v[120:123], v[68:71]\n" \
    "v_mfma_f32_16x16x32_bf16 v[84:87], v[244:247], v[124:127], v[84:87]\n" \
    "v_mfma_f32_16x16x32_bf16 v[72:75], v[248:251], v[120:123], v[72:75]\n" \
    "v_mfma_f32_16x16x32_bf16 v[88:91], v[248:251], v[124:127], v[88:91]\n" \
    "v_mfma_f32_16x16x32_bf16 v[76:79], v[252:255], v[120:123], v[76:79]\n" \
    "v_mfma_f32_16x16x32_bf16 v[92:95], v[252:255], v[124:127], v[92:95]\n" \
    "s_mov_b32 s74, s53\n" \
    "s_nop 7\n" \
    "v_cvt_pk_bf16_f32 v160, v64, v65\n" \
    "v_cvt_pk_bf16_f32 v161, v66, v67\n" \
    "v_cvt_pk_bf16_f32 v162, v68, v69\n" \
    "v_cvt_pk_bf16_f32 v163, v70, v71\n" \
    "buffer_store_dwordx4 v[160:163], v18, s[36:39], s74 offen offset:0 sc1\n" \
    "v_cvt_pk_bf16_f32 v164, v72, v73\n" \
    "v_cvt_pk_bf16_f32 v165, v74, v75\n" \
    "v_cvt_pk_bf16_f32 v166, v76, v77\n" \
    "v_cvt_pk_bf16_f32 v167, v78, v79\n" \
    "buffer_store_dwordx4 v[164:167], v18, s[36:39], s74 offen offset:1024 sc1\n" \
    "v_cvt_pk_bf16_f32 v168, v80, v81\n" \
    "v_cvt_pk_bf16_f32 v169, v82, v83\n" \
    "v_cvt_pk_bf16_f32 v170, v84, v85\n" \
    "v_cvt_pk_bf16_f32 v171, v86, v87\n" \
    "s_add_u32 s74, s74, 4096\n" \
    "buffer_store_dwordx4 v[168:171], v18, s[36:39], s74 offen offset:0 sc1\n" \
    "v_cvt_pk_bf16_f32 v172, v88, v89\n" \
    "v_cvt_pk_bf16_f32 v173, v90, v91\n" \
    "v_cvt_pk_bf16_f32 v174, v92, v93\n" \
    "v_cvt_pk_bf16_f32 v175, v94, v95\n" \
    "buffer_store_dwordx4 v[172:175], v18, s[36:39], s74 offen offset:1024 sc1\n" \
    "v_mov_b32 v22, s63\n" \
    "v_mov_b32 v23, 1\n" \
    "s_waitcnt vmcnt(0)\n" \
    "buffer_store_dwordx2 v[22:23], v17, s[36:39], 0 offen sc1\n" \
    "P0_la_%=:\n" \
    "buffer_load_dwordx2 v[22:23], v19, s[36:39], 0 offen sc1\n" \
    "s_waitcnt vmcnt(0)\n" \
    "v_cmp_eq_u32 vcc, s63, v22\n" \
    "v_cmp_eq_u32 s[76:77], s63, v23\n" \
    "s_and_b64 vcc, vcc, s[76:77]\n" \
    "s_and_b64 vcc, vcc, exec\n" \
    "s_cmp_eq_u64 vcc, exec\n" \
    "s_cbranch_scc0 P0_la_no_%=\n" \
    "P0_loop_%=:\n" \
    "s_mul_i32 s68, s70, 24576\n" \
    "s_add_u32 s74, s68, s53\n" \
    "s_add_u32 s70, s70, 1\n" \
    "s_add_u32 s68, s70, 2\n" \
    "s_min_u32 s68, s68, 63\n" \
    "s_mul_i32 s68, s68, 40960\n" \
    "s_add_u32 s40, s68, s54\n" \
    "s_add_u32 s40, s40, 2097152\n" \
    "s_add_u32 s41, s40, 4096\n" \
    "s_add_u32 s42, s40, 8192\n" \
    "s_mov_b32 s43, s75\n" \
    "s_waitcnt lgkmcnt(2)\n" \
    "v_mfma_f32_16x16x32_bf16 v[128:131], a[0:3], v[96:99], 0\n" \
    "ds_read_b128 v[112:115], v21 offset:2048\n" \
    "v_mfma_f32_16x16x32_bf16 v[144:147], a[0:3], v[100:103], 0\n" \
    "ds_read_b128 v[116:119], v21 offset:22528\n" \
    "v_mfma_f32_16x16x32_bf16 v[132:135], a[4:7], v[96:99], 0\n" \
    "s_add_u32 m0, s43, 0\n" \
    "v_mfma_f32_16x16x32_bf16 v[148:151], a[4:7], v[100:103], 0\n" \
    "buffer_load_dwordx4 v16, s[36:39], s40 offen offset:0 sc1 lds\n" \
    "v_mfma_f32_16x16x32_bf16 v[136:139], a[8:11], v[96:99], 0\n" \
    "v_mfma_f32_16x16x32_bf16 v[152:155], a[8:11], v[100:103], 0\n" \
    "v_mfma_f32_16x16x32_bf16 v[140:143], a[12:15], v[96:99], 0\n" \
    "v_mfma_f32_16x16x32_bf16 v[156:159], a[12:15], v[100:103], 0\n" \
    "s_waitcnt lgkmcnt(2)\n" \
    "v_mfma_f32_16x16x32_bf16 v[128:131], a[16:19], v[104:107], v[128:131]\n" \
    "ds_read_b128 v[120:123], v21 offset:3072\n" \
    "v_mfma_f32_16x16x32_bf16 v[144:147], a[16:19], v[108:111], v[144:147]\n" \
    "ds_read_b128 v[124:127], v21 offset:23552\n" \
    "v_mfma_f32_16x16x32_bf16 v[132:135], a[20:23], v[104:107], v[132:135]\n" \
    "s_add_u32 m0, s43, 0\n" \
    "v_mfma_f32_16x16x32_bf16 v[148:151], a[20:23], v[108:111], v[148:151]\n" \
    "buffer_load_dwordx4 v16, s[36:39], s40 offen offset:1024 sc1 lds\n" \
    "v_mfma_f32_16x16x32_bf16 v[136:139], a[24:27], v[104:107], v[136:139]\n" \
    "v_cvt_pk_bf16_f32 v160, v64, v65\n" \
    "v_cvt_pk_bf16_f32 v161, v66, v67\n" \
    "v_mfma_f32_16x16x32_bf16 v[152:155], a[24:27], v[108:111], v[152:155]\n" \
    "v_cvt_pk_bf16_f32 v162, v68, v69\n" \
    "v_mfma_f32_16x16x32_bf16 v[140:143], a[28:31], v[104:107], v[140:143]\n" \
    "v_cvt_pk_bf16_f32 v163, v70, v71\n" \
    "buffer_store_dwordx4 v[160:163], v18, s[36:39], s74 offen offset:0 sc1\n" \
    "v_mfma_f32_16x16x32_bf16 v[156:159], a[28:31], v[108:111], v[156:159]\n" \
    "v_cvt_pk_bf16_f32 v164, v72, v73\n" \
    "s_waitcnt lgkmcnt(2)\n" \
    "v_mfma_f32_16x16x32_bf16 v[128:131], a[32:35], v[112:115], v[128:131]\n" \
    "ds_read_b128 v[96:99], v21 offset:4096\n" \
    "v_mfma_f32_16x16x32_bf16 v[144:147], a[32:35], v[116:119], v[144:147]\n" \
    "ds_read_b128 v[100:103], v21 offset:24576\n" \
    "v_mfma_f32_16x16x32_bf16 v[132:135], a[36:39], v[112:115], v[132:135]\n" \
    "s_add_u32 m0, s43, 0\n" \
    "v_mfma_f32_16x16x32_bf16 v[148:151], a[36:39], v[116:119], v[148:151]\n" \
    "buffer_load_dwordx4 v16, s[36:39], s40 offen offset:2048 sc1 lds\n" \
    "v_mfma_f32_16x16x32_bf16 v[136:139], a[40:43], v[112:115], v[136:139]\n" \
    "v_cvt_pk_bf16_f32 v165, v74, v75\n" \
    "v_cvt_pk_bf16_f32 v166, v76, v77\n" \
    "v_mfma_f32_16x16x32_bf16 v[152:155], a[40:43], v[116:119], v[152:155]\n" \
    "v_cvt_pk_bf16_f32 v167, v78, v79\n" \
    "v_mfma_f32_16x16x32_bf16 v[140:143], a[44:47], v[112:115], v[140:143]\n" \
    "buffer_store_dwordx4 v[164:167], v18, s[36:39], s74 offen offset:1024 sc1\n" \
    "v_cvt_pk_bf16_f32 v168, v80, v81\n" \
    "v_mfma_f32_16x16x32_bf16 v[156:159], a[44:47], v[116:119], v[156:159]\n" \
    "v_cvt_pk_bf16_f32 v169, v82, v83\n" \
    "s_waitcnt lgkmcnt(2)\n" \
    "v_mfma_f32_16x16x32_bf16 v[128:131], a[48:51], v[120:123], v[128:131]\n" \
    "ds_read_b128 v[104:107], v21 offset:5120\n" \
    "v_mfma_f32_16x16x32_bf16 v[144:147], a[48:51], v[124:127], v[144:147]\n" \
    "ds_read_b128 v[108:111], v21 offset:25600\n" \
    "v_mfma_f32_16x16x32_bf16 v[132:135], a[52:55], v[120:123], v[132:135]\n" \
    "s_add_u32 m0, s43, 0\n" \
    "v_mfma_f32_16x16x32_bf16 v[148:151], a[52:55], v[124:127], v[148:151]\n" \
    "buffer_load_dwordx4 v16, s[36:39], s40 offen offset:3072 sc1 lds\n" \
    "v_mfma_f32_16x16x32_bf16 v[136:139], a[56:59], v[120:123], v[136:139]\n" \
    "v_cvt_pk_bf16_f32 v170, v84, v85\n" \
    "v_cvt_pk_bf16_f32 v171, v86, v87\n" \
    "v_mfma_f32_16x16x32_bf16 v[152:155], a[56:59], v[124:127], v[152:155]\n" \
    "s_add_u32 s74, s74, 4096\n" \
    "v_mfma_f32_16x16x32_bf16 v[140:143], a[60:63], v[120:123], v[140:143]\n" \
    "buffer_store_dwordx4 v[168:171], v18, s[36:39], s74 offen offset:0 sc1\n" \
    "v_cvt_pk_bf16_f32 v172, v88, v89\n" \
    "v_mfma_f32_16x16x32_bf16 v[156:159], a[60:63], v[124:127], v[156:159]\n" \
    "v_cvt_pk_bf16_f32 v173, v90, v91\n" \
    "s_waitcnt lgkmcnt(2)\n" \
    "v_mfma_f32_16x16x32_bf16 v[128:131], a[64:67], v[96:99], v[128:131]\n" \
    "ds_read_b128 v[112:115], v21 offset:6144\n" \
    "v_mfma_f32_16x16x32_bf16 v[144:147], a[64:67], v[100:103], v[144:147]\n" \
    "ds_read_b128 v[116:119], v21 offset:26624\n" \
    "v_mfma_f32_16x16x32_bf16 v[132:135], a[68:71], v[96:99], v[132:135]\n" \
    "s_add_u32 m0, s43, 4096\n" \
    "v_mfma_f32_16x16x32_bf16 v[148:151], a[68:71], v[100:103], v[148:151]\n" \
    "buffer_load_dwordx4 v16, s[36:39], s41 offen offset:0 sc1 lds\n" \
    "v_mfma_f32_16x16x32_bf16 v[136:139], a[72:75], v[96:99], v[136:139]\n" \
    "v_cvt_pk_bf16_f32 v174, v92, v93\n" \
    "v_mfma_f32_16x16x32_bf16 v[152:155], a[72:75], v[100:103], v[152:155]\n" \
    "v_cvt_pk_bf16_f32 v175, v94, v95\n" \
    "v_mfma_f32_16x16x32_bf16 v[140:143], a[76:79], v[96:99], v[140:143]\n" \
    "buffer_store_dwordx4 v[172:175], v18, s[36:39], s74 offen offset:1024 sc1\n" \
    "v_mfma_f32_16x16x32_bf16 v[156:159], a[76:79], v[100:103], v[156:159]\n" \
    "s_waitcnt lgkmcnt(2)\n" \
    "v_mfma_f32_16x16x32_bf16 v[128:131], a[80:83], v[104:107], v[128:131]\n" \
    "ds_read_b128 v[120:123], v21 offset:7168\n" \
    "v_mfma_f32_16x16x32_bf16 v[144:147], a[80:83], v[108:111], v[144:147]\n" \
    "ds_read_b128 v[124:127], v21 offset:27648\n" \
    "v_mfma_f32_16x16x32_bf16 v[132:135], a[84:87], v[104:107], v[132:135]\n" \
    "s_add_u32 m0, s43, 4096\n" \
    "v_mfma_f32_16x16x32_bf16 v[148:151], a[84:87], v[108:111], v[148:151]\n" \
    "buffer_load_dwordx4 v16, s[36:39], s41 offen offset:1024 sc1 lds\n" \
    "v_mfma_f32_16x16x32_bf16 v[136:139], a[88:91], v[104:107], v[136:139]\n" \
    "v_mfma_f32_16x16x32_bf16 v[152:155], a[88:91], v[108:111], v[152:155]\n" \
    "v_mfma_f32_16x16x32_bf16 v[140:143], a[92:95], v[104:107], v[140:143]\n" \
    "v_mfma_f32_16x16x32_bf16 v[156:159], a[92:95], v[108:111], v[156:159]\n" \
    "s_waitcnt lgkmcnt(2)\n" \
    "v_mfma_f32_16x16x32_bf16 v[128:131], a[96:99], v[112:115], v[128:131]\n" \
    "ds_read_b128 v[96:99], v21 offset:8192\n" \
    "v_mfma_f32_16x16x32_bf16 v[144:147], a[96:99], v[116:119], v[144:147]\n" \
    "ds_read_b128 v[100:103], v21 offset:28672\n" \
    "v_mfma_f32_16x16x32_bf16 v[132:135], a[100:103], v[112:115], v[132:135]\n" \
    "s_add_u32 m0, s43, 4096\n" \
    "v_mfma_f32_16x16x32_bf16 v[148:151], a[100:103], v[116:119], v[148:151]\n" \
    "buffer_load_dwordx4 v16, s[36:39], s41 offen offset:2048 sc1 lds\n" \
    "v_mfma_f32_16x16x32_bf16 v[136:139], a[104:107], v[112:115], v[136:139]\n" \
    "v_mfma_f32_16x16x32_bf16 v[152:155], a[104:107], v[116:119], v[152:155]\n" \
    "v_mfma_f32_16x16x32_bf16 v[140:143], a[108:111], v[112:115], v[140:143]\n" \
    "v_mfma_f32_16x16x32_bf16 v[156:159], a[108:111], v[116:119], v[156:159]\n" \
    "s_waitcnt lgkmcnt(2)\n" \
    "v_mfma_f32_16x16x32_bf16 v[128:131], a[112:115], v[120:123], v[128:131]\n" \
    "ds_read_b128 v[104:107], v21 offset:9216\n" \
    "v_mfma_f32_16x16x32_bf16 v[144:147], a[112:115], v[124:127], v[144:147]\n" \
    "ds_read_b128 v[108:111], v21 offset:29696\n" \
    "v_mfma_f32_16x16x32_bf16 v[132:135], a[116:119], v[120:123], v[132:135]\n" \
    "s_add_u32 m0, s43, 4096\n" \
    "v_mfma_f32_16x16x32_bf16 v[148:151], a[116:119], v[124:127], v[148:151]\n" \
    "buffer_load_dwordx4 v16, s[36:39], s41 offen offset:3072 sc1 lds\n" \
    "v_mfma_f32_16x16x32_bf16 v[136:139], a[120:123], v[120:123], v[136:139]\n" \
    "v_mfma_f32_16x16x32_bf16 v[152:155], a[120:123], v[124:127], v[152:155]\n" \
    "v_mfma_f32_16x16x32_bf16 v[140:143], a[124:127], v[120:123], v[140:143]\n" \
    "v_mfma_f32_16x16x32_bf16 v[156:159], a[124:127], v[124:127], v[156:159]\n" \
    "s_waitcnt lgkmcnt(2)\n" \
    "v_mfma_f32_16x16x32_bf16 v[128:131], a[128:131], v[96:99], v[128:131]\n" \
    "ds_read_b128 v[112:115], v21 offset:10240\n" \
    "v_mfma_f32_16x16x32_bf16 v[144:147], a[128:131], v[100:103], v[144:147]\n" \
    "ds_read_b128 v[116:119], v21 offset:30720\n" \
    "v_mfma_f32_16x16x32_bf16 v[132:135], a[132:135], v[96:99], v[132:135]\n" \
    "s_add_u32 m0, s43, 8192\n" \
    "v_mfma_f32_16x16x32_bf16 v[148:151], a[132:135], v[100:103], v[148:151]\n" \
    "buffer_load_dwordx4 v16, s[36:39], s42 offen offset:0 sc1 lds\n" \
    "v_mfma_f32_16x16x32_bf16 v[136:139], a[136:139], v[96:99], v[136:139]\n" \
    "v_mfma_f32_16x16x32_bf16 v[152:155], a[136:139], v[100:103], v[152:155]\n" \
    "v_mfma_f32_16x16x32_bf16 v[140:143], a[140:143], v[96:99], v[140:143]\n" \
    "v_mfma_f32_16x16x32_bf16 v[156:159], a[140:143], v[100:103], v[156:159]\n" \
    "s_waitcnt lgkmcnt(2)\n" \
    "v_mfma_f32_16x16x32_bf16 v[128:131], a[144:147], v[104:107], v[128:131]\n" \
    "ds_read_b128 v[120:123], v21 offset:11264\n" \
    "v_mfma_f32_16x16x32_bf16 v[144:147], a[144:147], v[108:111], v[144:147]\n" \
    "ds_read_b128 v[124:127], v21 offset:31744\n" \
    "v_mfma_f32_16x16x32_bf16 v[132:135], a[148:151], v[104:107], v[132:135]\n" \
    "s_add_u32 m0, s43, 8192\n" \
    "v_mfma_f32_16x16x32_bf16 v[148:151], a[148:151], v[108:111], v[148:151]\n" \
    "buffer_load_dwordx4 v16, s[36:39], s42 offen offset:1024 sc1 lds\n" \
    "v_mfma_f32_16x16x32_bf16 v[136:139], a[152:155], v[104:107], v[136:139]\n" \
    "v_mfma_f32_16x16x32_bf16 v[152:155], a[152:155], v[108:111], v[152:155]\n" \
    "v_mfma_f32_16x16x32_bf16 v[140:143], a[156:159], v[104:107], v[140:143]\n" \
    "v_mfma_f32_16x16x32_bf16 v[156:159], a[156:159], v[108:111], v[156:159]\n" \
    "s_waitcnt lgkmcnt(2)\n" \
    "v_mfma_f32_16x16x32_bf16 v[128:131], a[160:163], v[112:115], v[128:131]\n" \
    "ds_read_b128 v[96:99], v21 offset:12288\n" \
    "v_mfma_f32_16x16x32_bf16 v[144:147], a[160:163], v[116:119], v[144:147]\n" \
    "ds_read_b128 v[100:103], v21 offset:32768\n" \
    "v_mfma_f32_16x16x32_bf16 v[132:135], a[164:167], v[112:115], v[132:135]\n" \
    "v_mfma_f32_16x16x32_bf16 v[148:151], a[164:167], v[116:119], v[148:151]\n" \
    "v_mfma_f32_16x16x32_bf16 v[136:139], a[168:171], v[112:115], v[136:139]\n" \
    "v_mov_b32 v22, s63\n" \
    "v_mov_b32 v23, s70\n" \
    "v_mfma_f32_16x16x32_bf16 v[152:155], a[168:171], v[116:119], v[152:155]\n" \
    "s_waitcnt vmcnt(5)\n" \
    "v_mfma_f32_16x16x32_bf16 v[140:143], a[172:175], v[112:115], v[140:143]\n" \
    "buffer_store_dwordx2 v[22:23], v17, s[36:39], 0 offen sc1\n" \
    "v_mfma_f32_16x16x32_bf16 v[156:159], a[172:175], v[116:119], v[156:159]\n" \
    "s_waitcnt lgkmcnt(2)\n" \
    "v_mfma_f32_16x16x32_bf16 v[128:131], a[176:179], v[120:123], v[128:131]\n" \
    "ds_read_b128 v[104:107], v21 offset:13312\n" \
    "v_mfma_f32_16x16x32_bf16 v[144:147], a[176:179], v[124:127], v[144:147]\n" \
    "ds_read_b128 v[108:111], v21 offset:33792\n" \
    "v_mfma_f32_16x16x32_bf16 v[132:135], a[180:183], v[120:123], v[132:135]\n" \
    "v_mfma_f32_16x16x32_bf16 v[148:151], a[180:183], v[124:127], v[148:151]\n" \
    "v_mfma_f32_16x16x32_bf16 v[136:139], a[184:187], v[120:123], v[136:139]\n" \
    "v_mfma_f32_16x16x32_bf16 v[152:155], a[184:187], v[124:127], v[152:155]\n" \
    "v_mfma_f32_16x16x32_bf16 v[140:143], a[188:191], v[120:123], v[140:143]\n" \
    "v_mfma_f32_16x16x32_bf16 v[156:159], a[188:191], v[124:127], v[156:159]\n" \
    "s_waitcnt lgkmcnt(2)\n" \
    "v_mfma_f32_16x16x32_bf16 v[128:131], a[192:195], v[96:99], v[128:131]\n" \
    "ds_read_b128 v[112:115], v21 offset:14336\n" \
    "v_mfma_f32_16x16x32_bf16 v[144:147], a[192:195], v[100:103], v[144:147]\n" \
    "ds_read_b128 v[116:119], v21 offset:34816\n" \
    "v_mfma_f32_16x16x32_bf16 v[132:135], a[196:199], v[96:99], v[132:135]\n" \
    "v_mfma_f32_16x16x32_bf16 v[148:151], a[196:199], v[100:103], v[148:151]\n" \
    "v_mfma_f32_16x16x32_bf16 v[136:139], a[200:203], v[96:99], v[136:139]\n" \
    "v_mfma_f32_16x16x32_bf16 v[152:155], a[200:203], v[100:103], v[152:155]\n" \
    "v_mfma_f32_16x16x32_bf16 v[140:143], a[204:207], v[96:99], v[140:143]\n" \
    "v_mfma_f32_16x16x32_bf16 v[156:159], a[204:207], v[100:103], v[156:159]\n" \
    "s_waitcnt lgkmcnt(2)\n" \
    "v_mfma_f32_16x16x32_bf16 v[128:131], a[208:211], v[104:107], v[128:131]\n" \
    "ds_read_b128 v[120:123], v21 offset:15360\n" \
    "v_mfma_f32_16x16x32_bf16 v[144:147], a[208:211], v[108:111], v[144:147]\n" \
    "ds_read_b128 v[124:127], v21 offset:35840\n" \
    "v_mfma_f32_16x16x32_bf16 v[132:135], a[212:215], v[104:107], v[132:135]\n" \
    "v_mfma_f32_16x16x32_bf16 v[148:151], a[212:215], v[108:111], v[148:151]\n" \
    "v_mfma_f32_16x16x32_bf16 v[136:139], a[216:219], v[104:107], v[136:139]\n" \
    "v_mfma_f32_16x16x32_bf16 v[152:155], a[216:219], v[108:111], v[152:155]\n" \
    "v_mfma_f32_16x16x32_bf16 v[140:143], a[220:223], v[104:107], v[140:143]\n" \
    "v_mfma_f32_16x16x32_bf16 v[156:159], a[220:223], v[108:111], v[156:159]\n" \
    "s_waitcnt lgkmcnt(2)\n" \
    "v_mfma_f32_16x16x32_bf16 v[128:131], a[224:227], v[112:115], v[128:131]\n" \
    "ds_read_b128 v[96:99], v21 offset:16384\n" \
    "v_mfma_f32_16x16x32_bf16 v[144:147], a[224:227], v[116:119], v[144:147]\n" \
    "ds_read_b128 v[100:103], v21 offset:36864\n" \
    "v_mfma_f32_16x16x32_bf16 v[132:135], a[228:231], v[112:115], v[132:135]\n" \
    "v_mfma_f32_16x16x32_bf16 v[148:151], a[228:231], v[116:119], v[148:151]\n" \
    "v_mfma_f32_16x16x32_bf16 v[136:139], a[232:235], v[112:115], v[136:139]\n" \
    "v_mfma_f32_16x16x32_bf16 v[152:155], a[232:235], v[116:119], v[152:155]\n" \
    "v_mfma_f32_16x16x32_bf16 v[140:143], a[236:239], v[112:115], v[140:143]\n" \
    "v_mfma_f32_16x16x32_bf16 v[156:159], a[236:239], v[116:119], v[156:159]\n" \
    "s_waitcnt lgkmcnt(2)\n" \
    "v_mfma_f32_16x16x32_bf16 v[128:131], a[240:243], v[120:123], v[128:131]\n" \
    "ds_read_b128 v[104:107], v21 offset:17408\n" \
    "v_mfma_f32_16x16x32_bf16 v[144:147], a[240:243], v[124:127], v[144:147]\n" \
    "ds_read_b128 v[108:111], v21 offset:37888\n" \
    "v_mfma_f32_16x16x32_bf16 v[132:135], a[244:247], v[120:123], v[132:135]\n" \
    "v_mfma_f32_16x16x32_bf16 v[148:151], a[244:247], v[124:127], v[148:151]\n" \
    "v_mfma_f32_16x16x32_bf16 v[136:139], a[248:251], v[120:123], v[136:139]\n" \
    "v_mfma_f32_16x16x32_bf16 v[152:155], a[248:251], v[124:127], v[152:155]\n" \
    "v_mfma_f32_16x16x32_bf16 v[140:143], a[252:255], v[120:123], v[140:143]\n" \
    "v_mfma_f32_16x16x32_bf16 v[156:159], a[252:255], v[124:127], v[156:159]\n" \
    "s_waitcnt lgkmcnt(2)\n" \
    "v_mfma_f32_16x16x32_bf16 v[128:131], v[192:195], v[96:99], v[128:131]\n" \
    "ds_read_b128 v[112:115], v21 offset:18432\n" \
    "v_mfma_f32_16x16x32_bf16 v[144:147], v[192:195], v[100:103], v[144:147]\n" \
    "ds_read_b128 v[116:119], v21 offset:38912\n" \
    "v_mfma_f32_16x16x32_bf16 v[132:135], v[196:199], v[96:99], v[132:135]\n" \
    "v_mfma_f32_16x16x32_bf16 v[148:151], v[196:199], v[100:103], v[148:151]\n" \
    "v_mfma_f32_16x16x32_bf16 v[136:139], v[200:203], v[96:99], v[136:139]\n" \
    "v_mfma_f32_16x16x32_bf16 v[152:155], v[200:203], v[100:103], v[152:155]\n" \
    "v_mfma_f32_16x16x32_bf16 v[140:143], v[204:207], v[96:99], v[140:143]\n" \
    "v_mfma_f32_16x16x32_bf16 v[156:159], v[204:207], v[100:103], v[156:159]\n" \
    "s_waitcnt lgkmcnt(2)\n" \
    "v_mfma_f32_16x16x32_bf16 v[128:131], v[208:211], v[104:107], v[128:131]\n" \
    "ds_read_b128 v[120:123], v21 offset:19456\n" \
    "v_mfma_f32_16x16x32_bf16 v[144:147], v[208:211], v[108:111], v[144:147]\n" \
    "ds_read_b128 v[124:127], v21 offset:39936\n" \
    "v_mfma_f32_16x16x32_bf16 v[132:135], v[212:215], v[104:107], v[132:135]\n" \
    "v_mfma_f32_16x16x32_bf16 v[148:151], v[212:215], v[108:111], v[148:151]\n" \
    "v_mfma_f32_16x16x32_bf16 v[136:139], v[216:219], v[104:107], v[136:139]\n" \
    "v_mfma_f32_16x16x32_bf16 v[152:155], v[216:219], v[108:111], v[152:155]\n" \
    "v_mfma_f32_16x16x32_bf16 v[140:143], v[220:223], v[104:107], v[140:143]\n" \
    "v_mfma_f32_16x16x32_bf16 v[156:159], v[220:223], v[108:111], v[156:159]\n" \
    "s_add_u32 s75, s72, s54\n" \
    "s_add_u32 s72, s72, 40960\n" \
    "s_cmp_eq_u32 s72, 81920\n" \
    "s_cselect_b32 s72, 114688, s72\n" \
    "s_cmp_eq_u32 s72, 155648\n" \
    "s_cselect_b32 s72, 0, s72\n" \
    "v_add_u32 v21, s72, v16\n" \
    "s_waitcnt lgkmcnt(0)\n" \
    "s_barrier\n" \
    "v_mfma_f32_16x16x32_bf16 v[128:131], v[224:227], v[112:115], v[128:131]\n" \
    "ds_read_b128 v[96:99], v21 offset:0\n" \
    "v_mfma_f32_16x16x32_bf16 v[144:147], v[224:227], v[116:119], v[144:147]\n" \
    "ds_read_b128 v[100:103], v21 offset:20480\n" \
    "v_mfma_f32_16x16x32_bf16 v[132:135], v[228:231], v[112:115], v[132:135]\n" \
    "v_mfma_f32_16x16x32_bf16 v[148:151], v[228:231], v[116:119], v[148:151]\n" \
    "v_mfma_f32_16x16x32_bf16 v[136:139], v[232:235], v[112:115], v[136:139]\n" \
    "v_mfma_f32_16x16x32_bf16 v[152:155], v[232:235], v[116:119], v[152:155]\n" \
    "v_mfma_f32_16x16x32_bf16 v[140:143], v[236:239], v[112:115], v[140:143]\n" \
    "v_mfma_f32_16x16x32_bf16 v[156:159], v[236:239], v[116:119], v[156:159]\n" \
    "v_mfma_f32_16x16x32_bf16 v[128:131], v[240:243], v[120:123], v[128:131]\n" \
    "ds_read_b128 v[104:107], v21 offset:1024\n" \
    "v_mfma_f32_16x16x32_bf16 v[144:147], v[240:243], v[124:127], v[144:147]\n" \
    "ds_read_b128 v[108:111], v21 offset:21504\n" \
    "v_mfma_f32_16x16x32_bf16 v[132:135], v[244:247], v[120:123], v[132:135]\n" \
    "v_mfma_f32_16x16x32_bf16 v[148:151], v[244:247], v[124:127], v[148:151]\n" \
    "v_mfma_f32_16x16x32_bf16 v[136:139], v[248:251], v[120:123], v[136:139]\n" \
    "v_mfma_f32_16x16x32_bf16 v[152:155], v[248:251], v[124:127], v[152:155]\n" \
    "v_mfma_f32_16x16x32_bf16 v[140:143], v[252:255], v[120:123], v[140:143]\n" \
    "v_mfma_f32_16x16x32_bf16 v[156:159], v[252:255], v[124:127], v[156:159]\n" \
    "s_cmp_eq_u32 s70, 63\n" \
    "s_cbranch_scc1 P0_fin_%=\n" \
    "s_mul_i32 s68, s70, 24576\n" \
    "s_add_u32 s74, s68, s53\n" \
    "s_add_u32 s70, s70, 1\n" \
    "s_add_u32 s68, s70, 2\n" \
    "s_min_u32 s68, s68, 63\n" \
    "s_mul_i32 s68, s68, 40960\n" \
    "s_add_u32 s40, s68, s54\n" \
    "s_add_u32 s40, s40, 2097152\n" \
    "s_add_u32 s41, s40, 4096\n" \
    "s_add_u32 s42, s40, 8192\n" \
    "s_mov_b32 s43, s75\n" \
    "s_waitcnt lgkmcnt(2)\n" \
    "v_mfma_f32_16x16x32_bf16 v[64:67], a[0:3], v[96:99], 0\n" \
    "ds_read_b128 v[112:115], v21 offset:2048\n" \
    "v_mfma_f32_16x16x32_bf16 v[80:83], a[0:3], v[100:103], 0\n" \
    "ds_read_b128 v[116:119], v21 offset:22528\n" \
    "v_mfma_f32_16x16x32_bf16 v[68:71], a[4:7], v[96:99], 0\n" \
    "s_add_u32 m0, s43, 0\n" \
    "v_mfma_f32_16x16x32_bf16 v[84:87], a[4:7], v[100:103], 0\n" \
    "buffer_load_dwordx4 v16, s[36:39], s40 offen offset:0 sc1 lds\n" \
    "v_mfma_f32_16x16x32_bf16 v[72:75], a[8:11], v[96:99], 0\n" \
    "v_mfma_f32_16x16x32_bf16 v[88:91], a[8:11], v[100:103], 0\n" \
    "v_mfma_f32_16x16x32_bf16 v[76:79], a[12:15], v[96:99], 0\n" \
    "v_mfma_f32_16x16x32_bf16 v[92:95], a[12:15], v[100:103], 0\n" \
    "s_waitcnt lgkmcnt(2)\n" \
    "v_mfma_f32_16x16x32_bf16 v[64:67], a[16:19], v[104:107], v[64:67]\n" \
    "ds_read_b128 v[120:123], v21 offset:3072\n" \
    "v_mfma_f32_16x16x32_bf16 v[80:83], a[16:19], v[108:111], v[80:83]\n" \
    "ds_read_b128 v[124:127], v21 offset:23552\n" \
    "v_mfma_f32_16x16x32_bf16 v[68:71], a[20:23], v[104:107], v[68:71]\n" \
    "s_add_u32 m0, s43, 0\n" \
    "v_mfma_f32_16x16x32_bf16 v[84:87], a[20:23], v[108:111], v[84:87]\n" \
    "buffer_load_dwordx4 v16, s[36:39], s40 offen offset:1024 sc1 lds\n" \
    "v_mfma_f32_16x16x32_bf16 v[72:75], a[24:27], v[104:107], v[72:75]\n" \
    "v_cvt_pk_bf16_f32 v160, v128, v129\n" \
    "v_cvt_pk_bf16_f32 v161, v130, v131\n" \
    "v_mfma_f32_16x16x32_bf16 v[88:91], a[24:27], v[108:111], v[88:91]\n" \
    "v_cvt_pk_bf16_f32 v162, v132, v133\n" \
    "v_mfma_f32_16x16x32_bf16 v[76:79], a[28:31], v[104:107], v[76:79]\n" \
    "v_cvt_pk_bf16_f32 v163, v134, v135\n" \
    "buffer_store_dwordx4 v[160:163], v18, s[36:39], s74 offen offset:0 sc1\n" \
    "v_mfma_f32_16x16x32_bf16 v[92:95], a[28:31], v[108:111], v[92:95]\n" \
    "v_cvt_pk_bf16_f32 v164, v136, v137\n" \
    "s_waitcnt lgkmcnt(2)\n" \
    "v_mfma_f32_16x16x32_bf16 v[64:67], a[32:35], v[112:115], v[64:67]\n" \
    "ds_read_b128 v[96:99], v21 offset:4096\n" \
    "v_mfma_f32_16x16x32_bf16 v[80:83], a[32:35], v[116:119], v[80:83]\n" \
    "ds_read_b128 v[100:103], v21 offset:24576\n" \
    "v_mfma_f32_16x16x32_bf16 v[68:71], a[36:39], v[112:115], v[68:71]\n" \
    "s_add_u32 m0, s43, 0\n" \
    "v_mfma_f32_16x16x32_bf16 v[84:87], a[36:39], v[116:119], v[84:87]\n" \
    "buffer_load_dwordx4 v16, s[36:39], s40 offen offset:2048 sc1 lds\n" \
    "v_mfma_f32_16x16x32_bf16 v[72:75], a[40:43], v[112:115], v[72:75]\n" \
    "v_cvt_pk_bf16_f32 v165, v138, v139\n" \
    "v_cvt_pk_bf16_f32 v166, v140, v141\n" \
    "v_mfma_f32_16x16x32_bf16 v[88:91], a[40:43], v[116:119], v[88:91]\n" \
    "v_cvt_pk_bf16_f32 v167, v142, v143\n" \
    "v_mfma_f32_16x16x32_bf16 v[76:79], a[44:47], v[112:115], v[76:79]\n" \
    "buffer_store_dwordx4 v[164:167], v18, s[36:39], s74 offen offset:1024 sc1\n" \
    "v_cvt_pk_bf16_f32 v168, v144, v145\n" \
    "v_mfma_f32_16x16x32_bf16 v[92:95], a[44:47], v[116:119], v[92:95]\n" \
    "v_cvt_pk_bf16_f32 v169, v146, v147\n" \
    "s_waitcnt lgkmcnt(2)\n" \
    "v_mfma_f32_16x16x32_bf16 v[64:67], a[48:51], v[120:123], v[64:67]\n" \
    "ds_read_b128 v[104:107], v21 offset:5120\n" \
    "v_mfma_f32_16x16x32_bf16 v[80:83], a[48:51], v[124:127], v[80:83]\n" \
    "ds_read_b128 v[108:111], v21 offset:25600\n" \
    "v_mfma_f32_16x16x32_bf16 v[68:71], a[52:55], v[120:123], v[68:71]\n" \
    "s_add_u32 m0, s43, 0\n" \
    "v_mfma_f32_16x16x32_bf16 v[84:87], a[52:55], v[124:127], v[84:87]\n" \
    "buffer_load_dwordx4 v16, s[36:39], s40 offen offset:3072 sc1 lds\n" \
    "v_mfma_f32_16x16x32_bf16 v[72:75], a[56:59], v[120:123], v[72:75]\n" \
    "v_cvt_pk_bf16_f32 v170, v148, v149\n" \
    "v_cvt_pk_bf16_f32 v171, v150, v151\n" \
    "v_mfma_f32_16x16x32_bf16 v[88:91], a[56:59], v[124:127], v[88:91]\n" \
    "s_add_u32 s74, s74, 4096\n" \
    "v_mfma_f32_16x16x32_bf16 v[76:79], a[60:63], v[120:123], v[76:79]\n" \
    "buffer_store_dwordx4 v[168:171], v18, s[36:39], s74 offen offset:0 sc1\n" \
    "v_cvt_pk_bf16_f32 v172, v152, v153\n" \
    "v_mfma_f32_16x16x32_bf16 v[92:95], a[60:63], v[124:127], v[92:95]\n" \
    "v_cvt_pk_bf16_f32 v173, v154, v155\n" \
    "s_waitcnt lgkmcnt(2)\n" \
    "v_mfma_f32_16x16x32_bf16 v[64:67], a[64:67], v[96:99], v[64:67]\n" \
    "ds_read_b128 v[112:115], v21 offset:6144\n" \
    "v_mfma_f32_16x16x32_bf16 v[80:83], a[64:67], v[100:103], v[80:83]\n" \
    "ds_read_b128 v[116:119], v21 offset:26624\n" \
    "v_mfma_f32_16x16x32_bf16 v[68:71], a[68:71], v[96:99], v[68:71]\n" \
    "s_add_u32 m0, s43, 4096\n" \
    "v_mfma_f32_16x16x32_bf16 v[84:87], a[68:71], v[100:103], v[84:87]\n" \
    "buffer_load_dwordx4 v16, s[36:39], s41 offen offset:0 sc1 lds\n" \
    "v_mfma_f32_16x16x32_bf16 v[72:75], a[72:75], v[96:99], v[72:75]\n" \
    "v_cvt_pk_bf16_f32 v174, v156, v157\n" \
    "v_mfma_f32_16x16x32_bf16 v[88:91], a[72:75], v[100:103], v[88:91]\n" \
    "v_cvt_pk_bf16_f32 v175, v158, v159\n" \
    "v_mfma_f32_16x16x32_bf16 v[76:79], a[76:79], v[96:99], v[76:79]\n" \
    "buffer_store_dwordx4 v[172:175], v18, s[36:39], s74 offen offset:1024 sc1\n" \
    "v_mfma_f32_16x16x32_bf16 v[92:95], a[76:79], v[100:103], v[92:95]\n" \
    "s_waitcnt lgkmcnt(2)\n" \
    "v_mfma_f32_16x16x32_bf16 v[64:67], a[80:83], v[104:107], v[64:67]\n" \
    "ds_read_b128 v[120:123], v21 offset:7168\n" \
    "v_mfma_f32_16x16x32_bf16 v[80:83], a[80:83], v[108:111], v[80:83]\n" \
    "ds_read_b128 v[124:127], v21 offset:27648\n" \
    "v_mfma_f32_16x16x32_bf16 v[68:71], a[84:87], v[104:107], v[68:71]\n" \
    "s_add_u32 m0, s43, 4096\n" \
    "v_mfma_f32_16x16x32_bf16 v[84:87], a[84:87], v[108:111], v[84:87]\n" \
    "buffer_load_dwordx4 v16, s[36:39], s41 offen offset:1024 sc1 lds\n" \
    "v_mfma_f32_16x16x32_bf16 v[72:75], a[88:91], v[104:107], v[72:75]\n" \
    "v_mfma_f32_16x16x32_bf16 v[88:91], a[88:91], v[108:111], v[88:91]\n" \
    "v_mfma_f32_16x16x32_bf16 v[76:79], a[92:95], v[104:107], v[76:79]\n" \
    "v_mfma_f32_16x16x32_bf16 v[92:95], a[92:95], v[108:111], v[92:95]\n" \
    "s_waitcnt lgkmcnt(2)\n" \
    "v_mfma_f32_16x16x32_bf16 v[64:67], a[96:99], v[112:115], v[64:67]\n" \
    "ds_read_b128 v[96:99], v21 offset:8192\n" \
    "v_mfma_f32_16x16x32_bf16 v[80:83], a[96:99], v[116:119], v[80:83]\n" \
    "ds_read_b128 v[100:103], v21 offset:28672\n" \
    "v_mfma_f32_16x16x32_bf16 v[68:71], a[100:103], v[112:115], v[68:71]\n" \
    "s_add_u32 m0, s43, 4096\n" \
    "v_mfma_f32_16x16x32_bf16 v[84:87], a[100:103], v[116:119], v[84:87]\n" \
    "buffer_load_dwordx4 v16, s[36:39], s41 offen offset:2048 sc1 lds\n" \
    "v_mfma_f32_16x16x32_bf16 v[72:75], a[104:107], v[112:115], v[72:75]\n" \
    "v_mfma_f32_16x16x32_bf16 v[88:91], a[104:107], v[116:119], v[88:91]\n" \
    "v_mfma_f32_16x16x32_bf16 v[76:79], a[108:111], v[112:115], v[76:79]\n" \
    "v_mfma_f32_16x16x32_bf16 v[92:95], a[108:111], v[116:119], v[92:95]\n" \
    "s_waitcnt lgkmcnt(2)\n" \
    "v_mfma_f32_16x16x32_bf16 v[64:67], a[112:115], v[120:123], v[64:67]\n" \
    "ds_read_b128 v[104:107], v21 offset:9216\n" \
    "v_mfma_f32_16x16x32_bf16 v[80:83], a[112:115], v[124:127], v[80:83]\n" \
    "ds_read_b128 v[108:111], v21 offset:29696\n" \
    "v_mfma_f32_16x16x32_bf16 v[68:71], a[116:119], v[120:123], v[68:71]\n" \
    "s_add_u32 m0, s43, 4096\n" \
    "v_mfma_f32_16x16x32_bf16 v[84:87], a[116:119], v[124:127], v[84:87]\n" \
    "buffer_load_dwordx4 v16, s[36:39], s41 offen offset:3072 sc1 lds\n" \
    "v_mfma_f32_16x16x32_bf16 v[72:75], a[120:123], v[120:123], v[72:75]\n" \
    "v_mfma_f32_16x16x32_bf16 v[88:91], a[120:123], v[124:127], v[88:91]\n" \
    "v_mfma_f32_16x16x32_bf16 v[76:79], a[124:127], v[120:123], v[76:79]\n" \
    "v_mfma_f32_16x16x32_bf16 v[92:95], a[124:127], v[124:127], v[92:95]\n" \
    "s_waitcnt lgkmcnt(2)\n" \
    "v_mfma_f32_16x16x32_bf16 v[64:67], a[128:131], v[96:99], v[64:67]\n" \
    "ds_read_b128 v[112:115], v21 offset:10240\n" \
    "v_mfma_f32_16x16x32_bf16 v[80:83], a[128:131], v[100:103], v[80:83]\n" \
    "ds_read_b128 v[116:119], v21 offset:30720\n" \
    "v_mfma_f32_16x16x32_bf16 v[68:71], a[132:135], v[96:99], v[68:71]\n" \
    "s_add_u32 m0, s43, 8192\n" \
    "v_mfma_f32_16x16x32_bf16 v[84:87], a[132:135], v[100:103], v[84:87]\n" \
    "buffer_load_dwordx4 v16, s[36:39], s42 offen offset:0 sc1 lds\n" \
    "v_mfma_f32_16x16x32_bf16 v[72:75], a[136:139], v[96:99], v[72:75]\n" \
    "v_mfma_f32_16x16x32_bf16 v[88:91], a[136:139], v[100:103], v[88:91]\n" \
    "v_mfma_f32_16x16x32_bf16 v[76:79], a[140:143], v[96:99], v[76:79]\n" \
    "v_mfma_f32_16x16x32_bf16 v[92:95], a[140:143], v[100:103], v[92:95]\n" \
    "s_waitcnt lgkmcnt(2)\n" \
    "v_mfma_f32_16x16x32_bf16 v[64:67], a[144:147], v[104:107], v[64:67]\n" \
    "ds_read_b128 v[120:123], v21 offset:11264\n" \
    "v_mfma_f32_16x16x32_bf16 v[80:83], a[144:147], v[108:111], v[80:83]\n" \
    "ds_read_b128 v[124:127], v21 offset:31744\n" \
    "v_mfma_f32_16x16x32_bf16 v[68:71], a[148:151], v[104:107], v[68:71]\n" \
    "s_add_u32 m0, s43, 8192\n" \
    "v_mfma_f32_16x16x32_bf16 v[84:87], a[148:151], v[108:111], v[84:87]\n" \
    "buffer_load_dwordx4 v16, s[36:39], s42 offen offset:1024 sc1 lds\n" \
    "v_mfma_f32_16x16x32_bf16 v[72:75], a[152:155], v[104:107], v[72:75]\n" \
    "v_mfma_f32_16x16x32_bf16 v[88:91], a[152:155], v[108:111], v[88:91]\n" \
    "v_mfma_f32_16x16x32_bf16 v[76:79], a[156:159], v[104:107], v[76:79]\n" \
    "v_mfma_f32_16x16x32_bf16 v[92:95], a[156:159], v[108:111], v[92:95]\n" \
    "s_waitcnt lgkmcnt(2)\n" \
    "v_mfma_f32_16x16x32_bf16 v[64:67], a[160:163], v[112:115], v[64:67]\n" \
    "ds_read_b128 v[96:99], v21 offset:12288\n" \
    "v_mfma_f32_16x16x32_bf16 v[80:83], a[160:163], v[116:119], v[80:83]\n" \
    "ds_read_b128 v[100:103], v21 offset:32768\n" \
    "v_mfma_f32_16x16x32_bf16 v[68:71], a[164:167], v[112:115], v[68:71]\n" \
    "v_mfma_f32_16x16x32_bf16 v[84:87], a[164:167], v[116:119], v[84:87]\n" \
    "v_mfma_f32_16x16x32_bf16 v[72:75], a[168:171], v[112:115], v[72:75]\n" \
    "v_mov_b32 v22, s63\n" \
    "v_mov_b32 v23, s70\n" \
    "v_mfma_f32_16x16x32_bf16 v[88:91], a[168:171], v[116:119], v[88:91]\n" \
    "s_waitcnt vmcnt(5)\n" \
    "v_mfma_f32_16x16x32_bf16 v[76:79], a[172:175], v[112:115], v[76:79]\n" \
    "buffer_store_dwordx2 v[22:23], v17, s[36:39], 0 offen sc1\n" \
    "v_mfma_f32_16x16x32_bf16 v[92:95], a[172:175], v[116:119], v[92:95]\n" \
    "s_waitcnt lgkmcnt(2)\n" \
    "v_mfma_f32_16x16x32_bf16 v[64:67], a[176:179], v[120:123], v[64:67]\n" \
    "ds_read_b128 v[104:107], v21 offset:13312\n" \
    "v_mfma_f32_16x16x32_bf16 v[80:83], a[176:179], v[124:127], v[80:83]\n" \
    "ds_read_b128 v[108:111], v21 offset:33792\n" \
    "v_mfma_f32_16x16x32_bf16 v[68:71], a[180:183], v[120:123], v[68:71]\n" \
    "v_mfma_f32_16x16x32_bf16 v[84:87], a[180:183], v[124:127], v[84:87]\n" \
    "v_mfma_f32_16x16x32_bf16 v[72:75], a[184:187], v[120:123], v[72:75]\n" \
    "v_mfma_f32_16x16x32_bf16 v[88:91], a[184:187], v[124:127], v[88:91]\n" \
    "v_mfma_f32_16x16x32_bf16 v[76:79], a[188:191], v[120:123], v[76:79]\n" \
    "v_mfma_f32_16x16x32_bf16 v[92:95], a[188:191], v[124:127], v[92:95]\n" \
    "s_waitcnt lgkmcnt(2)\n" \
    "v_mfma_f32_16x16x32_bf16 v[64:67], a[192:195], v[96:99], v[64:67]\n" \
    "ds_read_b128 v[112:115], v21 offset:14336\n" \
    "v_mfma_f32_16x16x32_bf16 v[80:83], a[192:195], v[100:103], v[80:83]\n" \
    "ds_read_b128 v[116:119], v21 offset:34816\n" \
    "v_mfma_f32_16x16x32_bf16 v[68:71], a[196:199], v[96:99], v[68:71]\n" \
    "v_mfma_f32_16x16x32_bf16 v[84:87], a[196:199], v[100:103], v[84:87]\n" \
    "v_mfma_f32_16x16x32_bf16 v[72:75], a[200:203], v[96:99], v[72:75]\n" \
    "v_mfma_f32_16x16x32_bf16 v[88:91], a[200:203], v[100:103], v[88:91]\n" \
    "v_mfma_f32_16x16x32_bf16 v[76:79], a[204:207], v[96:99], v[76:79]\n" \
    "v_mfma_f32_16x16x32_bf16 v[92:95], a[204:207], v[100:103], v[92:95]\n" \
    "s_waitcnt lgkmcnt(2)\n" \
    "v_mfma_f32_16x16x32_bf16 v[64:67], a[208:211], v[104:107], v[64:67]\n" \
    "ds_read_b128 v[120:123], v21 offset:15360\n" \
    "v_mfma_f32_16x16x32_bf16 v[80:83], a[208:211], v[108:111], v[80:83]\n" \
    "ds_read_b128 v[124:127], v21 offset:35840\n" \
    "v_mfma_f32_16x16x32_bf16 v[68:71], a[212:215], v[104:107], v[68:71]\n" \
    "v_mfma_f32_16x16x32_bf16 v[84:87], a[212:215], v[108:111], v[84:87]\n" \
    "v_mfma_f32_16x16x32_bf16 v[72:75], a[216:219], v[104:107], v[72:75]\n" \
    "v_mfma_f32_16x16x32_bf16 v[88:91], a[216:219], v[108:111], v[88:91]\n" \
    "v_mfma_f32_16x16x32_bf16 v[76:79], a[220:223], v[104:107], v[76:79]\n" \
    "v_mfma_f32_16x16x32_bf16 v[92:95], a[220:223], v[108:111], v[92:95]\n" \
    "s_waitcnt lgkmcnt(2)\n" \
    "v_mfma_f32_16x16x32_bf16 v[64:67], a[224:227], v[112:115], v[64:67]\n" \
    "ds_read_b128 v[96:99], v21 offset:16384\n" \
    "v_mfma_f32_16x16x32_bf16 v[80:83], a[224:227], v[116:119], v[80:83]\n" \
    "ds_read_b128 v[100:103], v21 offset:36864\n" \
    "v_mfma_f32_16x16x32_bf16 v[68:71], a[228:231], v[112:115], v[68:71]\n" \
    "v_mfma_f32_16x16x32_bf16 v[84:87], a[228:231], v[116:119], v[84:87]\n" \
    "v_mfma_f32_16x16x32_bf16 v[72:75], a[232:235], v[112:115], v[72:75]\n" \
    "v_mfma_f32_16x16x32_bf16 v[88:91], a[232:235], v[116:119], v[88:91]\n" \
    "v_mfma_f32_16x16x32_bf16 v[76:79], a[236:239], v[112:115], v[76:79]\n" \
    "v_mfma_f32_16x16x32_bf16 v[92:95], a[236:239], v[116:119], v[92:95]\n" \
    "s_waitcnt lgkmcnt(2)\n" \
    "v_mfma_f32_16x16x32_bf16 v[64:67], a[240:243], v[120:123], v[64:67]\n" \
    "ds_read_b128 v[104:107], v21 offset:17408\n" \
    "v_mfma_f32_16x16x32_bf16 v[80:83], a[240:243], v[124:127], v[80:83]\n" \
    "ds_read_b128 v[108:111], v21 offset:37888\n" \
    "v_mfma_f32_16x16x32_bf16 v[68:71], a[244:247], v[120:123], v[68:71]\n" \
    "v_mfma_f32_16x16x32_bf16 v[84:87], a[244:247], v[124:127], v[84:87]\n" \
    "v_mfma_f32_16x16x32_bf16 v[72:75], a[248:251], v[120:123], v[72:75]\n" \
    "v_mfma_f32_16x16x32_bf16 v[88:91], a[248:251], v[124:127], v[88:91]\n" \
    "v_mfma_f32_16x16x32_bf16 v[76:79], a[252:255], v[120:123], v[76:79]\n" \
    "v_mfma_f32_16x16x32_bf16 v[92:95], a[252:255], v[124:127], v[92:95]\n" \
    "s_waitcnt lgkmcnt(2)\n" \
    "v_mfma_f32_16x16x32_bf16 v[64:67], v[192:195], v[96:99], v[64:67]\n" \
    "ds_read_b128 v[112:115], v21 offset:18432\n" \
    "v_mfma_f32_16x16x32_bf16 v[80:83], v[192:195], v[100:103], v[80:83]\n" \
    "ds_read_b128 v[116:119], v21 offset:38912\n" \
    "v_mfma_f32_16x16x32_bf16 v[68:71], v[196:199], v[96:99], v[68:71]\n" \
    "v_mfma_f32_16x16x32_bf16 v[84:87], v[196:199], v[100:103], v[84:87]\n" \
    "v_mfma_f32_16x16x32_bf16 v[72:75], v[200:203], v[96:99], v[72:75]\n" \
    "v_mfma_f32_16x16x32_bf16 v[88:91], v[200:203], v[100:103], v[88:91]\n" \
    "v_mfma_f32_16x16x32_bf16 v[76:79], v[204:207], v[96:99], v[76:79]\n" \
    "v_mfma_f32_16x16x32_bf16 v[92:95], v[204:207], v[100:103], v[92:95]\n" \
    "s_waitcnt lgkmcnt(2)\n" \
    "v_mfma_f32_16x16x32_bf16 v[64:67], v[208:211], v[104:107], v[64:67]\n" \
    "ds_read_b128 v[120:123], v21 offset:19456\n" \
    "v_mfma_f32_16x16x32_bf16 v[80:83], v[208:211], v[108:111], v[80:83]\n" \
    "ds_read_b128 v[124:127], v21 offset:39936\n" \
    "v_mfma_f32_16x16x32_bf16 v[68:71], v[212:215], v[104:107], v[68:71]\n" \
    "v_mfma_f32_16x16x32_bf16 v[84:87], v[212:215], v[108:111], v[84:87]\n" \
    "v_mfma_f32_16x16x32_bf16 v[72:75], v[216:219], v[104:107], v[72:75]\n" \
    "v_mfma_f32_16x16x32_bf16 v[88:91], v[216:219], v[108:111], v[88:91]\n" \
    "v_mfma_f32_16x16x32_bf16 v[76:79], v[220:223], v[104:107], v[76:79]\n" \
    "v_mfma_f32_16x16x32_bf16 v[92:95], v[220:223], v[108:111], v[92:95]\n" \
    "s_add_u32 s75, s72, s54\n" \
    "s_add_u32 s72, s72, 40960\n" \
    "s_cmp_eq_u32 s72, 81920\n" \
    "s_cselect_b32 s72, 114688, s72\n" \
    "s_cmp_eq_u32 s72, 155648\n" \
    "s_cselect_b32 s72, 0, s72\n" \
    "v_add_u32 v21, s72, v16\n" \
    "s_waitcnt lgkmcnt(0)\n" \
    "s_barrier\n" \
    "v_mfma_f32_16x16x32_bf16 v[64:67], v[224:227], v[112:115], v[64:67]\n" \
    "ds_read_b128 v[96:99], v21 offset:0\n" \
    "v_mfma_f32_16x16x32_bf16 v[80:83], v[224:227], v[116:119], v[80:83]\n" \
    "ds_read_b128 v[100:103], v21 offset:20480\n" \
    "v_mfma_f32_16x16x32_bf16 v[68:71], v[228:231], v[112:115], v[68:71]\n" \
    "v_mfma_f32_16x16x32_bf16 v[84:87], v[228:231], v[116:119], v[84:87]\n" \
    "v_mfma_f32_16x16x32_bf16 v[72:75], v[232:235], v[112:115], v[72:75]\n" \
    "v_mfma_f32_16x16x32_bf16 v[88:91], v[232:235], v[116:119], v[88:91]\n" \
    "v_mfma_f32_16x16x32_bf16 v[76:79], v[236:239], v[112:115], v[76:79]\n" \
    "v_mfma_f32_16x16x32_bf16 v[92:95], v[236:239], v[116:119], v[92:95]\n" \
    "v_mfma_f32_16x16x32_bf16 v[64:67], v[240:243], v[120:123], v[64:67]\n" \
    "ds_read_b128 v[104:107], v21 offset:1024\n" \
    "v_mfma_f32_16x16x32_bf16 v[80:83], v[240:243], v[124:127], v[80:83]\n" \
    "ds_read_b128 v[108:111], v21 offset:21504\n" \
    "v_mfma_f32_16x16x32_bf16 v[68:71], v[244:247], v[120:123], v[68:71]\n" \
    "v_mfma_f32_16x16x32_bf16 v[84:87], v[244:247], v[124:127], v[84:87]\n" \
    "v_mfma_f32_16x16x32_bf16 v[72:75], v[248:251], v[120:123], v[72:75]\n" \
    "v_mfma_f32_16x16x32_bf16 v[88:91], v[248:251], v[124:127], v[88:91]\n" \
    "v_mfma_f32_16x16x32_bf16 v[76:79], v[252:255], v[120:123], v[76:79]\n" \
    "v_mfma_f32_16x16x32_bf16 v[92:95], v[252:255], v[124:127], v[92:95]\n" \
    "s_branch P0_loop_%=\n" \
    "P0_fin_%=:\n" \
    "s_mul_i32 s68, s70, 24576\n" \
    "s_add_u32 s74, s68, s53\n" \
    "s_add_u32 s70, s70, 1\n" \
    "s_add_u32 s68, s70, 2\n" \
    "s_min_u32 s68, s68, 63\n" \
    "s_mul_i32 s68, s68, 40960\n" \
    "s_add_u32 s40, s68, s54\n" \
    "s_add_u32 s40, s40, 2097152\n" \
    "s_add_u32 s41, s40, 4096\n" \
    "s_add_u32 s42, s40, 8192\n" \
    "s_mov_b32 s43, s75\n" \
    "s_nop 7\n" \
    "v_cvt_pk_bf16_f32 v160, v128, v129\n" \
    "v_cvt_pk_bf16_f32 v161, v130, v131\n" \
    "v_cvt_pk_bf16_f32 v162, v132, v133\n" \
    "v_cvt_pk_bf16_f32 v163, v134, v135\n" \
    "buffer_store_dwordx4 v[160:163], v18, s[36:39], s74 offen offset:0 sc1\n" \
    "v_cvt_pk_bf16_f32 v164, v136, v137\n" \
    "v_cvt_pk_bf16_f32 v165, v138, v139\n" \
    "v_cvt_pk_bf16_f32 v166, v140, v141\n" \
    "v_cvt_pk_bf16_f32 v167, v142, v143\n" \
    "buffer_store_dwordx4 v[164:167], v18, s[36:39], s74 offen offset:1024 sc1\n" \
    "v_cvt_pk_bf16_f32 v168, v144, v145\n" \
    "v_cvt_pk_bf16_f32 v169, v146, v147\n" \
    "v_cvt_pk_bf16_f32 v170, v148, v149\n" \
    "v_cvt_pk_bf16_f32 v171, v150, v151\n" \
    "s_add_u32 s74, s74, 4096\n" \
    "buffer_store_dwordx4 v[168:171], v18, s[36:39], s74 offen offset:0 sc1\n" \
    "v_cvt_pk_bf16_f32 v172, v152, v153\n" \
    "v_cvt_pk_bf16_f32 v173, v154, v155\n" \
    "v_cvt_pk_bf16_f32 v174, v156, v157\n" \
    "v_cvt_pk_bf16_f32 v175, v158, v159\n" \
    "buffer_store_dwordx4 v[172:175], v18, s[36:39], s74 offen offset:1024 sc1\n" \
    "v_mov_b32 v22, s63\n" \
    "v_mov_b32 v23, 64\n" \
    "s_waitcnt vmcnt(0)\n" \
    "buffer_store_dwordx2 v[22:23], v17, s[36:39], 0 offen sc1\n" \
    "s_endpgm\n" \
    "P0_ln_no_%=:\n" \
    "s_add_u32 s82, s82, 1\n" \
    "s_and_b32 s85, s82, 63\n" \
    "s_cmp_lg_u32 s85, 0\n" \
    "s_cbranch_scc1 P0_g0_ok_%=\n" \
    "s_mov_b32 s85, 1048832\n" \
    "buffer_load_dword v22, off, s[36:39], s85 sc1\n" \
    "s_waitcnt vmcnt(0)\n" \
    "v_readfirstlane_b32 s85, v22\n" \
    "s_cmp_eq_u32 s85, s63\n" \
    "s_cbranch_scc1 P0_end_%=\n" \
    "s_memrealtime s[80:81]\n" \
    "s_waitcnt lgkmcnt(0)\n" \
    "s_sub_u32 s85, s80, s83\n" \
    "s_cmp_gt_u32 s85, 3000000\n" \
    "s_cbranch_scc1 P0_giveup_%=\n" \
    "P0_g0_ok_%=:\n" \
    "s_sleep 1\n" \
    "s_branch P0_ln_%=\n" \
    "P0_la_no_%=:\n" \
    "s_add_u32 s82, s82, 1\n" \
    "s_and_b32 s85, s82, 63\n" \
    "s_cmp_lg_u32 s85, 0\n" \
    "s_cbranch_scc1 P0_g1_ok_%=\n" \
    "s_mov_b32 s85, 1048832\n" \
    "buffer_load_dword v22, off, s[36:39], s85 sc1\n" \
    "s_waitcnt vmcnt(0)\n" \
    "v_readfirstlane_b32 s85, v22\n" \
    "s_cmp_eq_u32 s85, s63\n" \
    "s_cbranch_scc1 P0_end_%=\n" \
    "s_memrealtime s[80:81]\n" \
    "s_waitcnt lgkmcnt(0)\n" \
    "s_sub_u32 s85, s80, s83\n" \
    "s_cmp_gt_u32 s85, 3000000\n" \
    "s_cbranch_scc1 P0_giveup_%=\n" \
    "P0_g1_ok_%=:\n" \
    "s_sleep 1\n" \
    "s_branch P0_la_%=\n" \
    "P0_giveup_%=:\n" \
    "s_mov_b32 s85, 1048832\n" \
    "v_mov_b32 v22, s63\n" \
    "buffer_store_dword v22, off, s[36:39], s85 sc1\n" \
    "P0_end_%=:\n" \
    "s_endpgm\n"

#define PROJ1_ASM \
    "s_nop 4\n" \
    "s_mov_b32 s68, 0\n" \
    "s_add_u32 m0, s52, 0\n" \
    "s_nop 0\n" \
    "buffer_load_dwordx4 v0, s[40:43], s68 offen lds\n" \
    "s_add_u32 m0, s52, 1024\n" \
    "s_nop 0\n" \
    "buffer_load_dwordx4 v1, s[40:43], s68 offen lds\n" \
    "s_add_u32 m0, s52, 2048\n" \
    "s_nop 0\n" \
    "buffer_load_dwordx4 v2, s[40:43], s68 offen lds\n" \
    "s_add_u32 m0, s52, 3072\n" \
    "s_nop 0\n" \
    "buffer_load_dwordx4 v3, s[40:43], s68 offen lds\n" \
    "s_mov_b32 s68, 262144\n" \
    "s_add_u32 m0, s52, 4096\n" \
    "s_nop 0\n" \
    "buffer_load_dwordx4 v0, s[40:43], s68 offen lds\n" \
    "s_add_u32 m0, s52, 5120\n" \
    "s_nop 0\n" \
    "buffer_load_dwordx4 v1, s[40:43], s68 offen lds\n" \
    "s_add_u32 m0, s52, 6144\n" \
    "s_nop 0\n" \
    "buffer_load_dwordx4 v2, s[40:43], s68 offen lds\n" \
    "s_add_u32 m0, s52, 7168\n" \
    "s_nop 0\n" \
    "buffer_load_dwordx4 v3, s[40:43], s68 offen lds\n" \
    "s_mov_b32 s68, 524288\n" \
    "s_add_u32 m0, s52, 8192\n" \
    "s_nop 0\n" \
    "buffer_load_dwordx4 v0, s[40:43], s68 offen lds\n" \
    "s_add_u32 m0, s52, 9216\n" \
    "s_nop 0\n" \
    "buffer_load_dwordx4 v1, s[40:43], s68 offen lds\n" \
    "s_add_u32 m0, s52, 10240\n" \
    "s_nop 0\n" \
    "buffer_load_dwordx4 v2, s[40:43], s68 offen lds\n" \
    "s_add_u32 m0, s52, 11264\n" \
    "s_nop 0\n" \
    "buffer_load_dwordx4 v3, s[40:43], s68 offen lds\n" \
    "s_mov_b32 s68, 786432\n" \
    "s_add_u32 m0, s52, 12288\n" \
    "s_nop 0\n" \
    "buffer_load_dwordx4 v0, s[40:43], s68 offen lds\n" \
    "s_add_u32 m0, s52, 13312\n" \
    "s_nop 0\n" \
    "buffer_load_dwordx4 v1, s[40:43], s68 offen lds\n" \
    "s_add_u32 m0, s52, 14336\n" \
    "s_nop 0\n" \
    "buffer_load_dwordx4 v2, s[40:43], s68 offen lds\n" \
    "s_add_u32 m0, s52, 15360\n" \
    "s_nop 0\n" \
    "buffer_load_dwordx4 v3, s[40:43], s68 offen lds\n" \
    "s_mov_b32 s68, 1048576\n" \
    "s_add_u32 m0, s52, 16384\n" \
    "s_nop 0\n" \
    "buffer_load_dwordx4 v0, s[40:43], s68 offen lds\n" \
    "s_add_u32 m0, s52, 17408\n" \
    "s_nop 0\n" \
    "buffer_load_dwordx4 v1, s[40:43], s68 offen lds\n" \
    "s_add_u32 m0, s52, 18432\n" \
    "s_nop 0\n" \
    "buffer_load_dwordx4 v2, s[40:43], s68 offen lds\n" \
    "s_add_u32 m0, s52, 19456\n" \
    "s_nop 0\n" \
    "buffer_load_dwordx4 v3, s[40:43], s68 offen lds\n" \
    "s_mov_b32 s68, 1310720\n" \
    "s_add_u32 m0, s52, 20480\n" \
    "s_nop 0\n" \
    "buffer_load_dwordx4 v0, s[40:43], s68 offen lds\n" \
    "s_add_u32 m0, s52, 21504\n" \
    "s_nop 0\n" \
    "buffer_load_dwordx4 v1, s[40:43], s68 offen lds\n" \
    "s_add_u32 m0, s52, 22528\n" \
    "s_nop 0\n" \
    "buffer_load_dwordx4 v2, s[40:43], s68 offen lds\n" \
    "s_add_u32 m0, s52, 23552\n" \
    "s_nop 0\n" \
    "buffer_load_dwordx4 v3, s[40:43], s68 offen lds\n" \
    "s_mov_b32 s68, 1572864\n" \
    "s_add_u32 m0, s52, 24576\n" \
    "s_nop 0\n" \
    "buffer_load_dwordx4 v0, s[40:43], s68 offen lds\n" \
    "s_add_u32 m0, s52, 25600\n" \
    "s_nop 0\n" \
    "buffer_load_dwordx4 v1, s[40:43], s68 offen lds\n" \
    "s_add_u32 m0, s52, 26624\n" \
    "s_nop 0\n" \
    "buffer_load_dwordx4 v2, s[40:43], s68 offen lds\n" \
    "s_add_u32 m0, s52, 27648\n" \
    "s_nop 0\n" \
    "buffer_load_dwordx4 v3, s[40:43], s68 offen lds\n" \
    "s_waitcnt vmcnt(24)\n" \
    "ds_read_b64_tr_b16 a[0:1], v4 offset:0\n" \
    "ds_read_b64_tr_b16 a[2:3], v5 offset:0\n" \
    "ds_read_b64_tr_b16 a[4:5], v6 offset:0\n" \
    "ds_read_b64_tr_b16 a[6:7], v7 offset:0\n" \
    "ds_read_b64_tr_b16 a[8:9], v8 offset:0\n" \
    "ds_read_b64_tr_b16 a[10:11], v9 offset:0\n" \
    "ds_read_b64_tr_b16 a[12:13], v10 offset:0\n" \
    "ds_read_b64_tr_b16 a[14:15], v11 offset:0\n" \
    "s_waitcnt lgkmcnt(0)\n" \
    "s_mov_b32 s68, 1835008\n" \
    "s_add_u32 m0, s52, 0\n" \
    "s_nop 0\n" \
    "buffer_load_dwordx4 v0, s[40:43], s68 offen lds\n" \
    "s_add_u32 m0, s52, 1024\n" \
    "s_nop 0\n" \
    "buffer_load_dwordx4 v1, s[40:43], s68 offen lds\n" \
    "s_add_u32 m0, s52, 2048\n" \
    "s_nop 0\n" \
    "buffer_load_dwordx4 v2, s[40:43], s68 offen lds\n" \
    "s_add_u32 m0, s52, 3072\n" \
    "s_nop 0\n" \
    "buffer_load_dwordx4 v3, s[40:43], s68 offen lds\n" \
    "s_waitcnt vmcnt(24)\n" \
    "ds_read_b64_tr_b16 a[16:17], v4 offset:4096\n" \
    "ds_read_b64_tr_b16 a[18:19], v5 offset:4096\n" \
    "ds_read_b64_tr_b16 a[20:21], v6 offset:4096\n" \
    "ds_read_b64_tr_b16 a[22:23], v7 offset:4096\n" \
    "ds_read_b64_tr_b16 a[24:25], v8 offset:4096\n" \
    "ds_read_b64_tr_b16 a[26:27], v9 offset:4096\n" \
    "ds_read_b64_tr_b16 a[28:29], v10 offset:4096\n" \
    "ds_read_b64_tr_b16 a[30:31], v11 offset:4096\n" \
    "s_waitcnt lgkmcnt(0)\n" \
    "s_mov_b32 s68, 2097152\n" \
    "s_add_u32 m0, s52, 4096\n" \
    "s_nop 0\n" \
    "buffer_load_dwordx4 v0, s[40:43], s68 offen lds\n" \
    "s_add_u32 m0, s52, 5120\n" \
    "s_nop 0\n" \
    "buffer_load_dwordx4 v1, s[40:43], s68 offen lds\n" \
    "s_add_u32 m0, s52, 6144\n" \
    "s_nop 0\n" \
    "buffer_load_dwordx4 v2, s[40:43], s68 offen lds\n" \
    "s_add_u32 m0, s52, 7168\n" \
    "s_nop 0\n" \
    "buffer_load_dwordx4 v3, s[40:43], s68 offen lds\n" \
    "s_waitcnt vmcnt(24)\n" \
    "ds_read_b64_tr_b16 a[32:33], v4 offset:8192\n" \
    "ds_read_b64_tr_b16 a[34:35], v5 offset:8192\n" \
    "ds_read_b64_tr_b16 a[36:37], v6 offset:8192\n" \
    "ds_read_b64_tr_b16 a[38:39], v7 offset:8192\n" \
    "ds_read_b64_tr_b16 a[40:41], v8 offset:8192\n" \
    "ds_read_b64_tr_b16 a[42:43], v9 offset:8192\n" \
    "ds_read_b64_tr_b16 a[44:45], v10 offset:8192\n" \
    "ds_read_b64_tr_b16 a[46:47], v11 offset:8192\n" \
    "s_waitcnt lgkmcnt(0)\n" \
    "s_mov_b32 s68, 2359296\n" \
    "s_add_u32 m0, s52, 8192\n" \
    "s_nop 0\n" \
    "buffer_load_dwordx4 v0, s[40:43], s68 offen lds\n" \
    "s_add_u32 m0, s52, 9216\n" \
    "s_nop 0\n" \
    "buffer_load_dwordx4 v1, s[40:43], s68 offen lds\n" \
    "s_add_u32 m0, s52, 10240\n" \
    "s_nop 0\n" \
    "buffer_load_dwordx4 v2, s[40:43], s68 offen lds\n" \
    "s_add_u32 m0, s52, 11264\n" \
    "s_nop 0\n" \
    "buffer_load_dwordx4 v3, s[40:43], s68 offen lds\n" \
    "s_waitcnt vmcnt(24)\n" \
    "ds_read_b64_tr_b16 a[48:49], v4 offset:12288\n" \
    "ds_read_b64_tr_b16 a[50:51], v5 offset:12288\n" \
    "ds_read_b64_tr_b16 a[52:53], v6 offset:12288\n" \
    "ds_read_b64_tr_b16 a[54:55], v7 offset:12288\n" \
    "ds_read_b64_tr_b16 a[56:57], v8 offset:12288\n" \
    "ds_read_b64_tr_b16 a[58:59], v9 offset:12288\n" \
    "ds_read_b64_tr_b16 a[60:61], v10 offset:12288\n" \
    "ds_read_b64_tr_b16 a[62:63], v11 offset:12288\n" \
    "s_waitcnt lgkmcnt(0)\n" \
    "s_mov_b32 s68, 2621440\n" \
    "s_add_u32 m0, s52, 12288\n" \
    "s_nop 0\n" \
    "buffer_load_dwordx4 v0, s[40:43], s68 offen lds\n" \
    "s_add_u32 m0, s52, 13312\n" \
    "s_nop 0\n" \
    "buffer_load_dwordx4 v1, s[40:43], s68 offen lds\n" \
    "s_add_u32 m0, s52, 14336\n" \
    "s_nop 0\n" \
    "buffer_load_dwordx4 v2, s[40:43], s68 offen lds\n" \
    "s_add_u32 m0, s52, 15360\n" \
    "s_nop 0\n" \
    "buffer_load_dwordx4 v3, s[40:43], s68 offen lds\n" \
    "s_waitcnt vmcnt(24)\n" \
    "ds_read_b64_tr_b16 a[64:65], v4 offset:16384\n" \
    "ds_read_b64_tr_b16 a[66:67], v5 offset:16384\n" \
    "ds_read_b64_tr_b16 a[68:69], v6 offset:16384\n" \
    "ds_read_b64_tr_b16 a[70:71], v7 offset:16384\n" \
    "ds_read_b64_tr_b16 a[72:73], v8 offset:16384\n" \
    "ds_read_b64_tr_b16 a[74:75], v9 offset:16384\n" \
    "ds_read_b64_tr_b16 a[76:77], v10 offset:16384\n" \
    "ds_read_b64_tr_b16 a[78:79], v11 offset:16384\n" \
    "s_waitcnt lgkmcnt(0)\n" \
    "s_mov_b32 s68, 2883584\n" \
    "s_add_u32 m0, s52, 16384\n" \
    "s_nop 0\n" \
    "buffer_load_dwordx4 v0, s[40:43], s68 offen lds\n" \
    "s_add_u32 m0, s52, 17408\n" \
    "s_nop 0\n" \
    "buffer_load_dwordx4 v1, s[40:43], s68 offen lds\n" \
    "s_add_u32 m0, s52, 18432\n" \
    "s_nop 0\n" \
    "buffer_load_dwordx4 v2, s[40:43], s68 offen lds\n" \
    "s_add_u32 m0, s52, 19456\n" \
    "s_nop 0\n" \
    "buffer_load_dwordx4 v3, s[40:43], s68 offen lds\n" \
    "s_waitcnt vmcnt(24)\n" \
    "ds_read_b64_tr_b16 a[80:81], v4 offset:20480\n" \
    "ds_read_b64_tr_b16 a[82:83], v5 offset:20480\n" \
    "ds_read_b64_tr_b16 a[84:85], v6 offset:20480\n" \
    "ds_read_b64_tr_b16 a[86:87], v7 offset:20480\n" \
    "ds_read_b64_tr_b16 a[88:89], v8 offset:20480\n" \
    "ds_read_b64_tr_b16 a[90:91], v9 offset:20480\n" \
    "ds_read_b64_tr_b16 a[92:93], v10 offset:20480\n" \
    "ds_read_b64_tr_b16 a[94:95], v11 offset:20480\n" \
    "s_waitcnt lgkmcnt(0)\n" \
    "s_mov_b32 s68, 3145728\n" \
    "s_add_u32 m0, s52, 20480\n" \
    "s_nop 0\n" \
    "buffer_load_dwordx4 v0, s[40:43], s68 offen lds\n" \
    "s_add_u32 m0, s52, 21504\n" \
    "s_nop 0\n" \
    "buffer_load_dwordx4 v1, s[40:43], s68 offen lds\n" \
    "s_add_u32 m0, s52, 22528\n" \
    "s_nop 0\n" \
    "buffer_load_dwordx4 v2, s[40:43], s68 offen lds\n" \
    "s_add_u32 m0, s52, 23552\n" \
    "s_nop 0\n" \
    "buffer_load_dwordx4 v3, s[40:43], s68 offen lds\n" \
    "s_waitcnt vmcnt(24)\n" \
    "ds_read_b64_tr_b16 a[96:97], v4 offset:24576\n" \
    "ds_read_b64_tr_b16 a[98:99], v5 offset:24576\n" \
    "ds_read_b64_tr_b16 a[100:101], v6 offset:24576\n" \
    "ds_read_b64_tr_b16 a[102:103], v7 offset:24576\n" \
    "ds_read_b64_tr_b16 a[104:105], v8 offset:24576\n" \
    "ds_read_b64_tr_b16 a[106:107], v9 offset:24576\n" \
    "ds_read_b64_tr_b16 a[108:109], v10 offset:24576\n" \
    "ds_read_b64_tr_b16 a[110:111], v11 offset:24576\n" \
    "s_waitcnt lgkmcnt(0)\n" \
    "s_mov_b32 s68, 3407872\n" \
    "s_add_u32 m0, s52, 24576\n" \
    "s_nop 0\n" \
    "buffer_load_dwordx4 v0, s[40:43], s68 offen lds\n" \
    "s_add_u32 m0, s52, 25600\n" \
    "s_nop 0\n" \
    "buffer_load_dwordx4 v1, s[40:43], s68 offen lds\n" \
    "s_add_u32 m0, s52, 26624\n" \
    "s_nop 0\n" \
    "buffer_load_dwordx4 v2, s[40:43], s68 offen lds\n" \
    "s_add_u32 m0, s52, 27648\n" \
    "s_nop 0\n" \
    "buffer_load_dwordx4 v3, s[40:43], s68 offen lds\n" \
    "s_waitcnt vmcnt(24)\n" \
    "ds_read_b64_tr_b16 a[112:113], v4 offset:0\n" \
    "ds_read_b64_tr_b16 a[114:115], v5 offset:0\n" \
    "ds_read_b64_tr_b16 a[116:117], v6 offset:0\n" \
    "ds_read_b64_tr_b16 a[118:119], v7 offset:0\n" \
    "ds_read_b64_tr_b16 a[120:121], v8 offset:0\n" \
    "ds_read_b64_tr_b16 a[122:123], v9 offset:0\n" \
    "ds_read_b64_tr_b16 a[124:125], v10 offset:0\n" \
    "ds_read_b64_tr_b16 a[126:127], v11 offset:0\n" \
    "s_waitcnt lgkmcnt(0)\n" \
    "s_mov_b32 s68, 3670016\n" \
    "s_add_u32 m0, s52, 0\n" \
    "s_nop 0\n" \
    "buffer_load_dwordx4 v0, s[40:43], s68 offen lds\n" \
    "s_add_u32 m0, s52, 1024\n" \
    "s_nop 0\n" \
    "buffer_load_dwordx4 v1, s[40:43], s68 offen lds\n" \
    "s_add_u32 m0, s52, 2048\n" \
    "s_nop 0\n" \
    "buffer_load_dwordx4 v2, s[40:43], s68 offen lds\n" \
    "s_add_u32 m0, s52, 3072\n" \
    "s_nop 0\n" \
    "buffer_load_dwordx4 v3, s[40:43], s68 offen lds\n" \
    "s_waitcnt vmcnt(24)\n" \
    "ds_read_b64_tr_b16 a[128:129], v4 offset:4096\n" \
    "ds_read_b64_tr_b16 a[130:131], v5 offset:4096\n" \
    "ds_read_b64_tr_b16 a[132:133], v6 offset:4096\n" \
    "ds_read_b64_tr_b16 a[134:135], v7 offset:4096\n" \
    "ds_read_b64_tr_b16 a[136:137], v8 offset:4096\n" \
    "ds_read_b64_tr_b16 a[138:139], v9 offset:4096\n" \
    "ds_read_b64_tr_b16 a[140:141], v10 offset:4096\n" \
    "ds_read_b64_tr_b16 a[142:143], v11 offset:4096\n" \
    "s_waitcnt lgkmcnt(0)\n" \
    "s_mov_b32 s68, 3932160\n" \
    "s_add_u32 m0, s52, 4096\n" \
    "s_nop 0\n" \
    "buffer_load_dwordx4 v0, s[40:43], s68 offen lds\n" \
    "s_add_u32 m0, s52, 5120\n" \
    "s_nop 0\n" \
    "buffer_load_dwordx4 v1, s[40:43], s68 offen lds\n" \
    "s_add_u32 m0, s52, 6144\n" \
    "s_nop 0\n" \
    "buffer_load_dwordx4 v2, s[40:43], s68 offen lds\n" \
    "s_add_u32 m0, s52, 7168\n" \
    "s_nop 0\n" \
    "buffer_load_dwordx4 v3, s[40:43], s68 offen lds\n" \
    "s_waitcnt vmcnt(24)\n" \
    "ds_read_b64_tr_b16 a[144:145], v4 offset:8192\n" \
    "ds_read_b64_tr_b16 a[146:147], v5 offset:8192\n" \
    "ds_read_b64_tr_b16 a[148:149], v6 offset:8192\n" \
    "ds_read_b64_tr_b16 a[150:151], v7 offset:8192\n" \
    "ds_read_b64_tr_b16 a[152:153], v8 offset:8192\n" \
    "ds_read_b64_tr_b16 a[154:155], v9 offset:8192\n" \
    "ds_read_b64_tr_b16 a[156:157], v10 offset:8192\n" \
    "ds_read_b64_tr_b16 a[158:159], v11 offset:8192\n" \
    "s_waitcnt lgkmcnt(0)\n" \
    "s_mov_b32 s68, 4194304\n" \
    "s_add_u32 m0, s52, 8192\n" \
    "s_nop 0\n" \
    "buffer_load_dwordx4 v0, s[40:43], s68 offen lds\n" \
    "s_add_u32 m0, s52, 9216\n" \
    "s_nop 0\n" \
    "buffer_load_dwordx4 v1, s[40:43], s68 offen lds\n" \
    "s_add_u32 m0, s52, 10240\n" \
    "s_nop 0\n" \
    "buffer_load_dwordx4 v2, s[40:43], s68 offen lds\n" \
    "s_add_u32 m0, s52, 11264\n" \
    "s_nop 0\n" \
    "buffer_load_dwordx4 v3, s[40:43], s68 offen lds\n" \
    "s_waitcnt vmcnt(24)\n" \
    "ds_read_b64_tr_b16 a[160:161], v4 offset:12288\n" \
    "ds_read_b64_tr_b16 a[162:163], v5 offset:12288\n" \
    "ds_read_b64_tr_b16 a[164:165], v6 offset:12288\n" \
    "ds_read_b64_tr_b16 a[166:167], v7 offset:12288\n" \
    "ds_read_b64_tr_b16 a[168:169], v8 offset:12288\n" \
    "ds_read_b64_tr_b16 a[170:171], v9 offset:12288\n" \
    "ds_read_b64_tr_b16 a[172:173], v10 offset:12288\n" \
    "ds_read_b64_tr_b16 a[174:175], v11 offset:12288\n" \
    "s_waitcnt lgkmcnt(0)\n" \
    "s_mov_b32 s68, 4456448\n" \
    "s_add_u32 m0, s52, 12288\n" \
    "s_nop 0\n" \
    "buffer_load_dwordx4 v0, s[40:43], s68 offen lds\n" \
    "s_add_u32 m0, s52, 13312\n" \
    "s_nop 0\n" \
    "buffer_load_dwordx4 v1, s[40:43], s68 offen lds\n" \
    "s_add_u32 m0, s52, 14336\n" \
    "s_nop 0\n" \
    "buffer_load_dwordx4 v2, s[40:43], s68 offen lds\n" \
    "s_add_u32 m0, s52, 15360\n" \
    "s_nop 0\n" \
    "buffer_load_dwordx4 v3, s[40:43], s68 offen lds\n" \
    "s_waitcnt vmcnt(24)\n" \
    "ds_read_b64_tr_b16 a[176:177], v4 offset:16384\n" \
    "ds_read_b64_tr_b16 a[178:179], v5 offset:16384\n" \
    "ds_read_b64_tr_b16 a[180:181], v6 offset:16384\n" \
    "ds_read_b64_tr_b16 a[182:183], v7 offset:16384\n" \
    "ds_read_b64_tr_b16 a[184:185], v8 offset:16384\n" \
    "ds_read_b64_tr_b16 a[186:187], v9 offset:16384\n" \
    "ds_read_b64_tr_b16 a[188:189], v10 offset:16384\n" \
    "ds_read_b64_tr_b16 a[190:191], v11 offset:16384\n" \
    "s_waitcnt lgkmcnt(0)\n" \
    "s_mov_b32 s68, 4718592\n" \
    "s_add_u32 m0, s52, 16384\n" \
    "s_nop 0\n" \
    "buffer_load_dwordx4 v0, s[40:43], s68 offen lds\n" \
    "s_add_u32 m0, s52, 17408\n" \
    "s_nop 0\n" \
    "buffer_load_dwordx4 v1, s[40:43], s68 offen lds\n" \
    "s_add_u32 m0, s52, 18432\n" \
    "s_nop 0\n" \
    "buffer_load_dwordx4 v2, s[40:43], s68 offen lds\n" \
    "s_add_u32 m0, s52, 19456\n" \
    "s_nop 0\n" \
    "buffer_load_dwordx4 v3, s[40:43], s68 offen lds\n" \
    "s_waitcnt vmcnt(24)\n" \
    "ds_read_b64_tr_b16 a[192:193], v4 offset:20480\n" \
    "ds_read_b64_tr_b16 a[194:195], v5 offset:20480\n" \
    "ds_read_b64_tr_b16 a[196:197], v6 offset:20480\n" \
    "ds_read_b64_tr_b16 a[198:199], v7 offset:20480\n" \
    "ds_read_b64_tr_b16 a[200:201], v8 offset:20480\n" \
    "ds_read_b64_tr_b16 a[202:203], v9 offset:20480\n" \
    "ds_read_b64_tr_b16 a[204:205], v10 offset:20480\n" \
    "ds_read_b64_tr_b16 a[206:207], v11 offset:20480\n" \
    "s_waitcnt lgkmcnt(0)\n" \
    "s_mov_b32 s68, 4980736\n" \
    "s_add_u32 m0, s52, 20480\n" \
    "s_nop 0\n" \
    "buffer_load_dwordx4 v0, s[40:43], s68 offen lds\n" \
    "s_add_u32 m0, s52, 21504\n" \
    "s_nop 0\n" \
    "buffer_load_dwordx4 v1, s[40:43], s68 offen lds\n" \
    "s_add_u32 m0, s52, 22528\n" \
    "s_nop 0\n" \
    "buffer_load_dwordx4 v2, s[40:43], s68 offen lds\n" \
    "s_add_u32 m0, s52, 23552\n" \
    "s_nop 0\n" \
    "buffer_load_dwordx4 v3, s[40:43], s68 offen lds\n" \
    "s_memrealtime s[80:81]\n" \
    "s_waitcnt lgkmcnt(0)\n" \
    "s_mov_b32 s83, s80\n" \
    "s_mov_b32 s82, 0\n" \
    "P1_ln_%=:\n" \
    "buffer_load_dwordx2 v[22:23], v19, s[36:39], 0 offen sc1\n" \
    "s_waitcnt vmcnt(0)\n" \
    "v_cmp_eq_u32 vcc, s63, v22\n" \
    "v_cmp_eq_u32 s[76:77], s63, v23\n" \
    "s_and_b64 vcc, vcc, s[76:77]\n" \
    "s_and_b64 vcc, vcc, 7\n" \
    "s_cmp_eq_u64 vcc, 7\n" \
    "s_cbranch_scc0 P1_ln_no_%=\n" \
    "s_add_u32 s40, s54, 2097152\n" \
    "s_add_u32 s41, s40, 4096\n" \
    "s_add_u32 s42, s40, 8192\n" \
    "s_add_u32 s43, s54, 114688\n" \
    "s_add_u32 m0, s43, 0\n" \
    "s_nop 0\n" \
    "buffer_load_dwordx4 v16, s[36:39], s40 offen offset:0 sc1 lds\n" \
    "s_add_u32 m0, s43, 0\n" \
    "s_nop 0\n" \
    "buffer_load_dwordx4 v16, s[36:39], s40 offen offset:1024 sc1 lds\n" \
    "s_add_u32 m0, s43, 0\n" \
    "s_nop 0\n" \
    "buffer_load_dwordx4 v16, s[36:39], s40 offen offset:2048 sc1 lds\n" \
    "s_add_u32 m0, s43, 0\n" \
    "s_nop 0\n" \
    "buffer_load_dwordx4 v16, s[36:39], s40 offen offset:3072 sc1 lds\n" \
    "s_add_u32 m0, s43, 4096\n" \
    "s_nop 0\n" \
    "buffer_load_dwordx4 v16, s[36:39], s41 offen offset:0 sc1 lds\n" \
    "s_add_u32 m0, s43, 4096\n" \
    "s_nop 0\n" \
    "buffer_load_dwordx4 v16, s[36:39], s41 offen offset:1024 sc1 lds\n" \
    "s_add_u32 m0, s43, 4096\n" \
    "s_nop 0\n" \
    "buffer_load_dwordx4 v16, s[36:39], s41 offen offset:2048 sc1 lds\n" \
    "s_add_u32 m0, s43, 4096\n" \
    "s_nop 0\n" \
    "buffer_load_dwordx4 v16, s[36:39], s41 offen offset:3072 sc1 lds\n" \
    "s_add_u32 m0, s43, 8192\n" \
    "s_nop 0\n" \
    "buffer_load_dwordx4 v16, s[36:39], s42 offen offset:0 sc1 lds\n" \
    "s_add_u32 m0, s43, 8192\n" \
    "s_nop 0\n" \
    "buffer_load_dwordx4 v16, s[36:39], s42 offen offset:1024 sc1 lds\n" \
    "v_add_u32 v21, 114688, v16\n" \
    "ds_read_b64_tr_b16 a[208:209], v4 offset:24576\n" \
    "ds_read_b64_tr_b16 a[210:211], v5 offset:24576\n" \
    "ds_read_b64_tr_b16 a[212:213], v6 offset:24576\n" \
    "ds_read_b64_tr_b16 a[214:215], v7 offset:24576\n" \
    "ds_read_b64_tr_b16 a[216:217], v8 offset:24576\n" \
    "ds_read_b64_tr_b16 a[218:219], v9 offset:24576\n" \
    "ds_read_b64_tr_b16 a[220:221], v10 offset:24576\n" \
    "ds_read_b64_tr_b16 a[222:223], v11 offset:24576\n" \
    "s_waitcnt lgkmcnt(0)\n" \
    "ds_read_b64_tr_b16 a[224:225], v4 offset:0\n" \
    "ds_read_b64_tr_b16 a[226:227], v5 offset:0\n" \
    "ds_read_b64_tr_b16 a[228:229], v6 offset:0\n" \
    "ds_read_b64_tr_b16 a[230:231], v7 offset:0\n" \
    "ds_read_b64_tr_b16 a[232:233], v8 offset:0\n" \
    "ds_read_b64_tr_b16 a[234:235], v9 offset:0\n" \
    "ds_read_b64_tr_b16 a[236:237], v10 offset:0\n" \
    "ds_read_b64_tr_b16 a[238:239], v11 offset:0\n" \
    "s_waitcnt lgkmcnt(0)\n" \
    "ds_read_b64_tr_b16 a[240:241], v4 offset:4096\n" \
    "ds_read_b64_tr_b16 a[242:243], v5 offset:4096\n" \
    "ds_read_b64_tr_b16 a[244:245], v6 offset:4096\n" \
    "ds_read_b64_tr_b16 a[246:247], v7 offset:4096\n" \
    "ds_read_b64_tr_b16 a[248:249], v8 offset:4096\n" \
    "ds_read_b64_tr_b16 a[250:251], v9 offset:4096\n" \
    "ds_read_b64_tr_b16 a[252:253], v10 offset:4096\n" \
    "ds_read_b64_tr_b16 a[254:255], v11 offset:4096\n" \
    "s_waitcnt lgkmcnt(0)\n" \
    "ds_read_b64_tr_b16 v[192:193], v4 offset:8192\n" \
    "ds_read_b64_tr_b16 v[194:195], v5 offset:8192\n" \
    "ds_read_b64_tr_b16 v[196:197], v6 offset:8192\n" \
    "ds_read_b64_tr_b16 v[198:199], v7 offset:8192\n" \
    "ds_read_b64_tr_b16 v[200:201], v8 offset:8192\n" \
    "ds_read_b64_tr_b16 v[202:203], v9 offset:8192\n" \
    "ds_read_b64_tr_b16 v[204:205], v10 offset:8192\n" \
    "ds_read_b64_tr_b16 v[206:207], v11 offset:8192\n" \
    "s_waitcnt lgkmcnt(0)\n" \
    "ds_read_b64_tr_b16 v[208:209], v4 offset:12288\n" \
    "ds_read_b64_tr_b16 v[210:211], v5 offset:12288\n" \
    "ds_read_b64_tr_b16 v[212:213], v6 offset:12288\n" \
    "ds_read_b64_tr_b16 v[214:215], v7 offset:12288\n" \
    "ds_read_b64_tr_b16 v[216:217], v8 offset:12288\n" \
    "ds_read_b64_tr_b16 v[218:219], v9 offset:12288\n" \
    "ds_read_b64_tr_b16 v[220:221], v10 offset:12288\n" \
    "ds_read_b64_tr_b16 v[222:223], v11 offset:12288\n" \
    "s_waitcnt lgkmcnt(0)\n" \
    "ds_read_b64_tr_b16 v[224:225], v4 offset:16384\n" \
    "ds_read_b64_tr_b16 v[226:227], v5 offset:16384\n" \
    "ds_read_b64_tr_b16 v[228:229], v6 offset:16384\n" \
    "ds_read_b64_tr_b16 v[230:231], v7 offset:16384\n" \
    "ds_read_b64_tr_b16 v[232:233], v8 offset:16384\n" \
    "ds_read_b64_tr_b16 v[234:235], v9 offset:16384\n" \
    "ds_read_b64_tr_b16 v[236:237], v10 offset:16384\n" \
    "ds_read_b64_tr_b16 v[238:239], v11 offset:16384\n" \
    "s_waitcnt lgkmcnt(0)\n" \
    "ds_read_b64_tr_b16 v[240:241], v4 offset:20480\n" \
    "ds_read_b64_tr_b16 v[242:243], v5 offset:20480\n" \
    "ds_read_b64_tr_b16 v[244:245], v6 offset:20480\n" \
    "ds_read_b64_tr_b16 v[246:247], v7 offset:20480\n" \
    "ds_read_b64_tr_b16 v[248:249], v8 offset:20480\n" \
    "ds_read_b64_tr_b16 v[250:251], v9 offset:20480\n" \
    "ds_read_b64_tr_b16 v[252:253], v10 offset:20480\n" \
    "ds_read_b64_tr_b16 v[254:255], v11 offset:20480\n" \
    "s_waitcnt lgkmcnt(0)\n" \
    "s_mov_b32 s70, 0\n" \
    "s_mov_b32 s72, 114688\n" \
    "s_waitcnt vmcnt(0)\n" \
    "s_barrier\n" \
    "ds_read_b128 v[96:99], v21 offset:0\n" \
    "ds_read_b128 v[100:103], v21 offset:20480\n" \
    "ds_read_b128 v[104:107], v21 offset:1024\n" \
    "ds_read_b128 v[108:111], v21 offset:21504\n" \
    "s_add_u32 s40, s54, 2138112\n" \
    "s_add_u32 s41, s40, 4096\n" \
    "s_add_u32 s42, s40, 8192\n" \
    "s_add_u32 s43, s54, 0\n" \
    "s_add_u32 m0, s43, 0\n" \
    "s_nop 0\n" \
    "buffer_load_dwordx4 v16, s[36:39], s40 offen offset:0 sc1 lds\n" \
    "s_add_u32 m0, s43, 0\n" \
    "s_nop 0\n" \
    "buffer_load_dwordx4 v16, s[36:39], s40 offen offset:1024 sc1 lds\n" \
    "s_add_u32 m0, s43, 0\n" \
    "s_nop 0\n" \
    "buffer_load_dwordx4 v16, s[36:39], s40 offen offset:2048 sc1 lds\n" \
    "s_add_u32 m0, s43, 0\n" \
    "s_nop 0\n" \
    "buffer_load_dwordx4 v16, s[36:39], s40 offen offset:3072 sc1 lds\n" \
    "s_add_u32 m0, s43, 4096\n" \
    "s_nop 0\n" \
    "buffer_load_dwordx4 v16, s[36:39], s41 offen offset:0 sc1 lds\n" \
    "s_add_u32 m0, s43, 4096\n" \
    "s_nop 0\n" \
    "buffer_load_dwordx4 v16, s[36:39], s41 offen offset:1024 sc1 lds\n" \
    "s_add_u32 m0, s43, 4096\n" \
    "s_nop 0\n" \
    "buffer_load_dwordx4 v16, s[36:39], s41 offen offset:2048 sc1 lds\n" \
    "s_add_u32 m0, s43, 4096\n" \
    "s_nop 0\n" \
    "buffer_load_dwordx4 v16, s[36:39], s41 offen offset:3072 sc1 lds\n" \
    "s_add_u32 m0, s43, 8192\n" \
    "s_nop 0\n" \
    "buffer_load_dwordx4 v16, s[36:39], s42 offen offset:0 sc1 lds\n" \
    "s_add_u32 m0, s43, 8192\n" \
    "s_nop 0\n" \
    "buffer_load_dwordx4 v16, s[36:39], s42 offen offset:1024 sc1 lds\n" \
    "s_nop 0\n" \
    "s_add_u32 s40, s54, 2179072\n" \
    "s_add_u32 s41, s40, 4096\n" \
    "s_add_u32 s42, s40, 8192\n" \
    "s_add_u32 s43, s54, 40960\n" \
    "s_waitcnt lgkmcnt(2)\n" \
    "v_mfma_f32_16x16x32_bf16 v[64:67], v[96:99], a[0:3], 0\n" \
    "ds_read_b128 v[112:115], v21 offset:2048\n" \
    "v_mfma_f32_16x16x32_bf16 v[80:83], v[100:103], a[0:3], 0\n" \
    "ds_read_b128 v[116:119], v21 offset:22528\n" \
    "v_mfma_f32_16x16x32_bf16 v[68:71], v[96:99], a[4:7], 0\n" \
    "s_add_u32 m0, s43, 0\n" \
    "v_mfma_f32_16x16x32_bf16 v[84:87], v[100:103], a[4:7], 0\n" \
    "buffer_load_dwordx4 v16, s[36:39], s40 offen offset:0 sc1 lds\n" \
    "v_mfma_f32_16x16x32_bf16 v[72:75], v[96:99], a[8:11], 0\n" \
    "v_mfma_f32_16x16x32_bf16 v[88:91], v[100:103], a[8:11], 0\n" \
    "v_mfma_f32_16x16x32_bf16 v[76:79], v[96:99], a[12:15], 0\n" \
    "v_mfma_f32_16x16x32_bf16 v[92:95], v[100:103], a[12:15], 0\n" \
    "s_waitcnt lgkmcnt(2)\n" \
    "v_mfma_f32_16x16x32_bf16 v[64:67], v[104:107], a[16:19], v[64:67]\n" \
    "ds_read_b128 v[120:123], v21 offset:3072\n" \
    "v_mfma_f32_16x16x32_bf16 v[80:83], v[108:111], a[16:19], v[80:83]\n" \
    "ds_read_b128 v[124:127], v21 offset:23552\n" \
    "v_mfma_f32_16x16x32_bf16 v[68:71], v[104:107], a[20:23], v[68:71]\n" \
    "s_add_u32 m0, s43, 0\n" \
    "v_mfma_f32_16x16x32_bf16 v[84:87], v[108:111], a[20:23], v[84:87]\n" \
    "buffer_load_dwordx4 v16, s[36:39], s40 offen offset:1024 sc1 lds\n" \
    "v_mfma_f32_16x16x32_bf16 v[72:75], v[104:107], a[24:27], v[72:75]\n" \
    "v_mfma_f32_16x16x32_bf16 v[88:91], v[108:111], a[24:27], v[88:91]\n" \
    "v_mfma_f32_16x16x32_bf16 v[76:79], v[104:107], a[28:31], v[76:79]\n" \
    "v_mfma_f32_16x16x32_bf16 v[92:95], v[108:111], a[28:31], v[92:95]\n" \
    "s_waitcnt lgkmcnt(2)\n" \
    "v_mfma_f32_16x16x32_bf16 v[64:67], v[112:115], a[32:35], v[64:67]\n" \
    "ds_read_b128 v[96:99], v21 offset:4096\n" \
    "v_mfma_f32_16x16x32_bf16 v[80:83], v[116:119], a[32:35], v[80:83]\n" \
    "ds_read_b128 v[100:103], v21 offset:24576\n" \
    "v_mfma_f32_16x16x32_bf16 v[68:71], v[112:115], a[36:39], v[68:71]\n" \
    "s_add_u32 m0, s43, 0\n" \
    "v_mfma_f32_16x16x32_bf16 v[84:87], v[116:119], a[36:39], v[84:87]\n" \
    "buffer_load_dwordx4 v16, s[36:39], s40 offen offset:2048 sc1 lds\n" \
    "v_mfma_f32_16x16x32_bf16 v[72:75], v[112:115], a[40:43], v[72:75]\n" \
    "v_mfma_f32_16x16x32_bf16 v[88:91], v[116:119], a[40:43], v[88:91]\n" \
    "v_mfma_f32_16x16x32_bf16 v[76:79], v[112:115], a[44:47], v[76:79]\n" \
    "v_mfma_f32_16x16x32_bf16 v[92:95], v[116:119], a[44:47], v[92:95]\n" \
    "s_waitcnt lgkmcnt(2)\n" \
    "v_mfma_f32_16x16x32_bf16 v[64:67], v[120:123], a[48:51], v[64:67]\n" \
    "ds_read_b128 v[104:107], v21 offset:5120\n" \
    "v_mfma_f32_16x16x32_bf16 v[80:83], v[124:127], a[48:51], v[80:83]\n" \
    "ds_read_b128 v[108:111], v21 offset:25600\n" \
    "v_mfma_f32_16x16x32_bf16 v[68:71], v[120:123], a[52:55], v[68:71]\n" \
    "s_add_u32 m0, s43, 0\n" \
    "v_mfma_f32_16x16x32_bf16 v[84:87], v[124:127], a[52:55], v[84:87]\n" \
    "buffer_load_dwordx4 v16, s[36:39], s40 offen offset:3072 sc1 lds\n" \
    "v_mfma_f32_16x16x32_bf16 v[72:75], v[120:123], a[56:59], v[72:75]\n" \
    "v_mfma_f32_16x16x32_bf16 v[88:91], v[124:127], a[56:59], v[88:91]\n" \
    "v_mfma_f32_16x16x32_bf16 v[76:79], v[120:123], a[60:63], v[76:79]\n" \
    "v_mfma_f32_16x16x32_bf16 v[92:95], v[124:127], a[60:63], v[92:95]\n" \
    "s_waitcnt lgkmcnt(2)\n" \
    "v_mfma_f32_16x16x32_bf16 v[64:67], v[96:99], a[64:67], v[64:67]\n" \
    "ds_read_b128 v[112:115], v21 offset:6144\n" \
    "v_mfma_f32_16x16x32_bf16 v[80:83], v[100:103], a[64:67], v[80:83]\n" \
    "ds_read_b128 v[116:119], v21 offset:26624\n" \
    "v_mfma_f32_16x16x32_bf16 v[68:71], v[96:99], a[68:71], v[68:71]\n" \
    "s_add_u32 m0, s43, 4096\n" \
    "v_mfma_f32_16x16x32_bf16 v[84:87], v[100:103], a[68:71], v[84:87]\n" \
    "buffer_load_dwordx4 v16, s[36:39], s41 offen offset:0 sc1 lds\n" \
    "v_mfma_f32_16x16x32_bf16 v[72:75], v[96:99], a[72:75], v[72:75]\n" \
    "v_mfma_f32_16x16x32_bf16 v[88:91], v[100:103], a[72:75], v[88:91]\n" \
    "v_mfma_f32_16x16x32_bf16 v[76:79], v[96:99], a[76:79], v[76:79]\n" \
    "v_mfma_f32_16x16x32_bf16 v[92:95], v[100:103], a[76:79], v[92:95]\n" \
    "s_waitcnt lgkmcnt(2)\n" \
    "v_mfma_f32_16x16x32_bf16 v[64:67], v[104:107], a[80:83], v[64:67]\n" \
    "ds_read_b128 v[120:123], v21 offset:7168\n" \
    "v_mfma_f32_16x16x32_bf16 v[80:83], v[108:111], a[80:83], v[80:83]\n" \
    "ds_read_b128 v[124:127], v21 offset:27648\n" \
    "v_mfma_f32_16x16x32_bf16 v[68:71], v[104:107], a[84:87], v[68:71]\n" \
    "s_add_u32 m0, s43, 4096\n" \
    "v_mfma_f32_16x16x32_bf16 v[84:87], v[108:111], a[84:87], v[84:87]\n" \
    "buffer_load_dwordx4 v16, s[36:39], s41 offen offset:1024 sc1 lds\n" \
    "v_mfma_f32_16x16x32_bf16 v[72:75], v[104:107], a[88:91], v[72:75]\n" \
    "v_mfma_f32_16x16x32_bf16 v[88:91], v[108:111], a[88:91], v[88:91]\n" \
    "v_mfma_f32_16x16x32_bf16 v[76:79], v[104:107], a[92:95], v[76:79]\n" \
    "v_mfma_f32_16x16x32_bf16 v[92:95], v[108:111], a[92:95], v[92:95]\n" \
    "s_waitcnt lgkmcnt(2)\n" \
    "v_mfma_f32_16x16x32_bf16 v[64:67], v[112:115], a[96:99], v[64:67]\n" \
    "ds_read_b128 v[96:99], v21 offset:8192\n" \
    "v_mfma_f32_16x16x32_bf16 v[80:83], v[116:119], a[96:99], v[80:83]\n" \
    "ds_read_b128 v[100:103], v21 offset:28672\n" \
    "v_mfma_f32_16x16x32_bf16 v[68:71], v[112:115], a[100:103], v[68:71]\n" \
    "s_add_u32 m0, s43, 4096\n" \
    "v_mfma_f32_16x16x32_bf16 v[84:87], v[116:119], a[100:103], v[84:87]\n" \
    "buffer_load_dwordx4 v16, s[36:39], s41 offen offset:2048 sc1 lds\n" \
    "v_mfma_f32_16x16x32_bf16 v[72:75], v[112:115], a[104:107], v[72:75]\n" \
    "v_mfma_f32_16x16x32_bf16 v[88:91], v[116:119], a[104:107], v[88:91]\n" \
    "v_mfma_f32_16x16x32_bf16 v[76:79], v[112:115], a[108:111], v[76:79]\n" \
    "v_mfma_f32_16x16x32_bf16 v[92:95], v[116:119], a[108:111], v[92:95]\n" \
    "s_waitcnt lgkmcnt(2)\n" \
    "v_mfma_f32_16x16x32_bf16 v[64:67], v[120:123], a[112:115], v[64:67]\n" \
    "ds_read_b128 v[104:107], v21 offset:9216\n" \
    "v_mfma_f32_16x16x32_bf16 v[80:83], v[124:127], a[112:115], v[80:83]\n" \
    "ds_read_b128 v[108:111], v21 offset:29696\n" \
    "v_mfma_f32_16x16x32_bf16 v[68:71], v[120:123], a[116:119], v[68:71]\n" \
    "s_add_u32 m0, s43, 4096\n" \
    "v_mfma_f32_16x16x32_bf16 v[84:87], v[124:127], a[116:119], v[84:87]\n" \
    "buffer_load_dwordx4 v16, s[36:39], s41 offen offset:3072 sc1 lds\n" \
    "v_mfma_f32_16x16x32_bf16 v[72:75], v[120:123], a[120:123], v[72:75]\n" \
    "v_mfma_f32_16x16x32_bf16 v[88:91], v[124:127], a[120:123], v[88:91]\n" \
    "v_mfma_f32_16x16x32_bf16 v[76:79], v[120:123], a[124:127], v[76:79]\n" \
    "v_mfma_f32_16x16x32_bf16 v[92:95], v[124:127], a[124:127], v[92:95]\n" \
    "s_waitcnt lgkmcnt(2)\n" \
    "v_mfma_f32_16x16x32_bf16 v[64:67], v[96:99], a[128:131], v[64:67]\n" \
    "ds_read_b128 v[112:115], v21 offset:10240\n" \
    "v_mfma_f32_16x16x32_bf16 v[80:83], v[100:103], a[128:131], v[80:83]\n" \
    "ds_read_b128 v[116:119], v21 offset:30720\n" \
    "v_mfma_f32_16x16x32_bf16 v[68:71], v[96:99], a[132:135], v[68:71]\n" \
    "s_add_u32 m0, s43, 8192\n" \
    "v_mfma_f32_16x16x32_bf16 v[84:87], v[100:103], a[132:135], v[84:87]\n" \
    "buffer_load_dwordx4 v16, s[36:39], s42 offen offset:0 sc1 lds\n" \
    "v_mfma_f32_16x16x32_bf16 v[72:75], v[96:99], a[136:139], v[72:75]\n" \
    "v_mfma_f32_16x16x32_bf16 v[88:91], v[100:103], a[136:139], v[88:91]\n" \
    "v_mfma_f32_16x16x32_bf16 v[76:79], v[96:99], a[140:143], v[76:79]\n" \
    "v_mfma_f32_16x16x32_bf16 v[92:95], v[100:103], a[140:143], v[92:95]\n" \
    "s_waitcnt lgkmcnt(2)\n" \
    "v_mfma_f32_16x16x32_bf16 v[64:67], v[104:107], a[144:147], v[64:67]\n" \
    "ds_read_b128 v[120:123], v21 offset:11264\n" \
    "v_mfma_f32_16x16x32_bf16 v[80:83], v[108:111], a[144:147], v[80:83]\n" \
    "ds_read_b128 v[124:127], v21 offset:31744\n" \
    "v_mfma_f32_16x16x32_bf16 v[68:71], v[104:107], a[148:151], v[68:71]\n" \
    "s_add_u32 m0, s43, 8192\n" \
    "v_mfma_f32_16x16x32_bf16 v[84:87], v[108:111], a[148:151], v[84:87]\n" \
    "buffer_load_dwordx4 v16, s[36:39], s42 offen offset:1024 sc1 lds\n" \
    "v_mfma_f32_16x16x32_bf16 v[72:75], v[104:107], a[152:155], v[72:75]\n" \
    "v_mfma_f32_16x16x32_bf16 v[88:91], v[108:111], a[152:155], v[88:91]\n" \
    "v_mfma_f32_16x16x32_bf16 v[76:79], v[104:107], a[156:159], v[76:79]\n" \
    "v_mfma_f32_16x16x32_bf16 v[92:95], v[108:111], a[156:159], v[92:95]\n" \
    "s_waitcnt lgkmcnt(2)\n" \
    "v_mfma_f32_16x16x32_bf16 v[64:67], v[112:115], a[160:163], v[64:67]\n" \
    "ds_read_b128 v[96:99], v21 offset:12288\n" \
    "v_mfma_f32_16x16x32_bf16 v[80:83], v[116:119], a[160:163], v[80:83]\n" \
    "ds_read_b128 v[100:103], v21 offset:32768\n" \
    "v_mfma_f32_16x16x32_bf16 v[68:71], v[112:115], a[164:167], v[68:71]\n" \
    "v_mfma_f32_16x16x32_bf16 v[84:87], v[116:119], a[164:167], v[84:87]\n" \
    "v_mfma_f32_16x16x32_bf16 v[72:75], v[112:115], a[168:171], v[72:75]\n" \
    "v_mfma_f32_16x16x32_bf16 v[88:91], v[116:119], a[168:171], v[88:91]\n" \
    "v_mfma_f32_16x16x32_bf16 v[76:79], v[112:115], a[172:175], v[76:79]\n" \
    "v_mfma_f32_16x16x32_bf16 v[92:95], v[116:119], a[172:175], v[92:95]\n" \
    "s_waitcnt lgkmcnt(2)\n" \
    "v_mfma_f32_16x16x32_bf16 v[64:67], v[120:123], a[176:179], v[64:67]\n" \
    "ds_read_b128 v[104:107], v21 offset:13312\n" \
    "v_mfma_f32_16x16x32_bf16 v[80:83], v[124:127], a[176:179], v[80:83]\n" \
    "ds_read_b128 v[108:111], v21 offset:33792\n" \
    "v_mfma_f32_16x16x32_bf16 v[68:71], v[120:123], a[180:183], v[68:71]\n" \
    "v_mfma_f32_16x16x32_bf16 v[84:87], v[124:127], a[180:183], v[84:87]\n" \
    "v_mfma_f32_16x16x32_bf16 v[72:75], v[120:123], a[184:187], v[72:75]\n" \
    "v_mfma_f32_16x16x32_bf16 v[88:91], v[124:127], a[184:187], v[88:91]\n" \
    "v_mfma_f32_16x16x32_bf16 v[76:79], v[120:123], a[188:191], v[76:79]\n" \
    "v_mfma_f32_16x16x32_bf16 v[92:95], v[124:127], a[188:191], v[92:95]\n" \
    "s_waitcnt lgkmcnt(2)\n" \
    "v_mfma_f32_16x16x32_bf16 v[64:67], v[96:99], a[192:195], v[64:67]\n" \
    "ds_read_b128 v[112:115], v21 offset:14336\n" \
    "v_mfma_f32_16x16x32_bf16 v[80:83], v[100:103], a[192:195], v[80:83]\n" \
    "ds_read_b128 v[116:119], v21 offset:34816\n" \
    "v_mfma_f32_16x16x32_bf16 v[68:71], v[96:99], a[196:199], v[68:71]\n" \
    "v_mfma_f32_16x16x32_bf16 v[84:87], v[100:103], a[196:199], v[84:87]\n" \
    "v_mfma_f32_16x16x32_bf16 v[72:75], v[96:99], a[200:203], v[72:75]\n" \
    "v_mfma_f32_16x16x32_bf16 v[88:91], v[100:103], a[200:203], v[88:91]\n" \
    "v_mfma_f32_16x16x32_bf16 v[76:79], v[96:99], a[204:207], v[76:79]\n" \
    "v_mfma_f32_16x16x32_bf16 v[92:95], v[100:103], a[204:207], v[92:95]\n" \
    "s_waitcnt lgkmcnt(2)\n" \
    "v_mfma_f32_16x16x32_bf16 v[64:67], v[104:107], a[208:211], v[64:67]\n" \
    "ds_read_b128 v[120:123], v21 offset:15360\n" \
    "v_mfma_f32_16x16x32_bf16 v[80:83], v[108:111], a[208:211], v[80:83]\n" \
    "ds_read_b128 v[124:127], v21 offset:35840\n" \
    "v_mfma_f32_16x16x32_bf16 v[68:71], v[104:107], a[212:215], v[68:71]\n" \
    "v_mfma_f32_16x16x32_bf16 v[84:87], v[108:111], a[212:215], v[84:87]\n" \
    "v_mfma_f32_16x16x32_bf16 v[72:75], v[104:107], a[216:219], v[72:75]\n" \
    "v_mfma_f32_16x16x32_bf16 v[88:91], v[108:111], a[216:219], v[88:91]\n" \
    "v_mfma_f32_16x16x32_bf16 v[76:79], v[104:107], a[220:223], v[76:79]\n" \
    "v_mfma_f32_16x16x32_bf16 v[92:95], v[108:111], a[220:223], v[92:95]\n" \
    "s_waitcnt lgkmcnt(2)\n" \
    "v_mfma_f32_16x16x32_bf16 v[64:67], v[112:115], a[224:227], v[64:67]\n" \
    "ds_read_b128 v[96:99], v21 offset:16384\n" \
    "v_mfma_f32_16x16x32_bf16 v[80:83], v[116:119], a[224:227], v[80:83]\n" \
    "ds_read_b128 v[100:103], v21 offset:36864\n" \
    "v_mfma_f32_16x16x32_bf16 v[68:71], v[112:115], a[228:231], v[68:71]\n" \
    "v_mfma_f32_16x16x32_bf16 v[84:87], v[116:119], a[228:231], v[84:87]\n" \
    "v_mfma_f32_16x16x32_bf16 v[72:75], v[112:115], a[232:235], v[72:75]\n" \
    "v_mfma_f32_16x16x32_bf16 v[88:91], v[116:119], a[232:235], v[88:91]\n" \
    "v_mfma_f32_16x16x32_bf16 v[76:79], v[112:115], a[236:239], v[76:79]\n" \
    "v_mfma_f32_16x16x32_bf16 v[92:95], v[116:119], a[236:239], v[92:95]\n" \
    "s_waitcnt lgkmcnt(2)\n" \
    "v_mfma_f32_16x16x32_bf16 v[64:67], v[120:123], a[240:243], v[64:67]\n" \
    "ds_read_b128 v[104:107], v21 offset:17408\n" \
    "v_mfma_f32_16x16x32_bf16 v[80:83], v[124:127], a[240:243], v[80:83]\n" \
    "ds_read_b128 v[108:111], v21 offset:37888\n" \
    "v_mfma_f32_16x16x32_bf16 v[68:71], v[120:123], a[244:247], v[68:71]\n" \
    "v_mfma_f32_16x16x32_bf16 v[84:87], v[124:127], a[244:247], v[84:87]\n" \
    "v_mfma_f32_16x16x32_bf16 v[72:75], v[120:123], a[248:251], v[72:75]\n" \
    "v_mfma_f32_16x16x32_bf16 v[88:91], v[124:127], a[248:251], v[88:91]\n" \
    "v_mfma_f32_16x16x32_bf16 v[76:79], v[120:123], a[252:255], v[76:79]\n" \
    "v_mfma_f32_16x16x32_bf16 v[92:95], v[124:127], a[252:255], v[92:95]\n" \
    "s_waitcnt lgkmcnt(2)\n" \
    "v_mfma_f32_16x16x32_bf16 v[64:67], v[96:99], v[192:195], v[64:67]\n" \
    "ds_read_b128 v[112:115], v21 offset:18432\n" \
    "v_mfma_f32_16x16x32_bf16 v[80:83], v[100:103], v[192:195], v[80:83]\n" \
    "ds_read_b128 v[116:119], v21 offset:38912\n" \
    "v_mfma_f32_16x16x32_bf16 v[68:71], v[96:99], v[196:199], v[68:71]\n" \
    "v_mfma_f32_16x16x32_bf16 v[84:87], v[100:103], v[196:199], v[84:87]\n" \
    "v_mfma_f32_16x16x32_bf16 v[72:75], v[96:99], v[200:203], v[72:75]\n" \
    "v_mfma_f32_16x16x32_bf16 v[88:91], v[100:103], v[200:203], v[88:91]\n" \
    "v_mfma_f32_16x16x32_bf16 v[76:79], v[96:99], v[204:207], v[76:79]\n" \
    "v_mfma_f32_16x16x32_bf16 v[92:95], v[100:103], v[204:207], v[92:95]\n" \
    "s_waitcnt lgkmcnt(2)\n" \
    "v_mfma_f32_16x16x32_bf16 v[64:67], v[104:107], v[208:211], v[64:67]\n" \
    "ds_read_b128 v[120:123], v21 offset:19456\n" \
    "v_mfma_f32_16x16x32_bf16 v[80:83], v[108:111], v[208:211], v[80:83]\n" \
    "ds_read_b128 v[124:127], v21 offset:39936\n" \
    "v_mfma_f32_16x16x32_bf16 v[68:71], v[104:107], v[212:215], v[68:71]\n" \
    "v_mfma_f32_16x16x32_bf16 v[84:87], v[108:111], v[212:215], v[84:87]\n" \
    "v_mfma_f32_16x16x32_bf16 v[72:75], v[104:107], v[216:219], v[72:75]\n" \
    "v_mfma_f32_16x16x32_bf16 v[88:91], v[108:111], v[216:219], v[88:91]\n" \
    "v_mfma_f32_16x16x32_bf16 v[76:79], v[104:107], v[220:223], v[76:79]\n" \
    "v_mfma_f32_16x16x32_bf16 v[92:95], v[108:111], v[220:223], v[92:95]\n" \
    "s_add_u32 s75, s72, s54\n" \
    "s_add_u32 s72, s72, 40960\n" \
    "s_cmp_eq_u32 s72, 81920\n" \
    "s_cselect_b32 s72, 114688, s72\n" \
    "s_cmp_eq_u32 s72, 155648\n" \
    "s_cselect_b32 s72, 0, s72\n" \
    "v_add_u32 v21, s72, v16\n" \
    "s_waitcnt lgkmcnt(0) vmcnt(10)\n" \
    "s_barrier\n" \
    "v_mfma_f32_16x16x32_bf16 v[64:67], v[112:115], v[224:227], v[64:67]\n" \
    "ds_read_b128 v[96:99], v21 offset:0\n" \
    "v_mfma_f32_16x16x32_bf16 v[80:83], v[116:119], v[224:227], v[80:83]\n" \
    "ds_read_b128 v[100:103], v21 offset:20480\n" \
    "v_mfma_f32_16x16x32_bf16 v[68:71], v[112:115], v[228:231], v[68:71]\n" \
    "v_mfma_f32_16x16x32_bf16 v[84:87], v[116:119], v[228:231], v[84:87]\n" \
    "v_mfma_f32_16x16x32_bf16 v[72:75], v[112:115], v[232:235], v[72:75]\n" \
    "v_mfma_f32_16x16x32_bf16 v[88:91], v[116:119], v[232:235], v[88:91]\n" \
    "v_mfma_f32_16x16x32_bf16 v[76:79], v[112:115], v[236:239], v[76:79]\n" \
    "v_mfma_f32_16x16x32_bf16 v[92:95], v[116:119], v[236:239], v[92:95]\n" \
    "v_mfma_f32_16x16x32_bf16 v[64:67], v[120:123], v[240:243], v[64:67]\n" \
    "ds_read_b128 v[104:107], v21 offset:1024\n" \
    "v_mfma_f32_16x16x32_bf16 v[80:83], v[124:127], v[240:243], v[80:83]\n" \
    "ds_read_b128 v[108:111], v21 offset:21504\n" \
    "v_mfma_f32_16x16x32_bf16 v[68:71], v[120:123], v[244:247], v[68:71]\n" \
    "v_mfma_f32_16x16x32_bf16 v[84:87], v[124:127], v[244:247], v[84:87]\n" \
    "v_mfma_f32_16x16x32_bf16 v[72:75], v[120:123], v[248:251], v[72:75]\n" \
    "v_mfma_f32_16x16x32_bf16 v[88:91], v[124:127], v[248:251], v[88:91]\n" \
    "v_mfma_f32_16x16x32_bf16 v[76:79], v[120:123], v[252:255], v[76:79]\n" \
    "v_mfma_f32_16x16x32_bf16 v[92:95], v[124:127], v[252:255], v[92:95]\n" \
    "s_mov_b32 s74, s53\n" \
    "s_nop 7\n" \
    "v_cvt_pk_bf16_f32 v160, v64, v65\n" \
    "v_cvt_pk_bf16_f32 v161, v66, v67\n" \
    "v_cvt_pk_bf16_f32 v162, v80, v81\n" \
    "v_cvt_pk_bf16_f32 v163, v82, v83\n" \
    "buffer_store_dwordx4 v[160:163], v18, s[36:39], s74 offen offset:0 sc1\n" \
    "v_cvt_pk_bf16_f32 v164, v68, v69\n" \
    "v_cvt_pk_bf16_f32 v165, v70, v71\n" \
    "v_cvt_pk_bf16_f32 v166, v84, v85\n" \
    "v_cvt_pk_bf16_f32 v167, v86, v87\n" \
    "buffer_store_dwordx4 v[164:167], v18, s[36:39], s74 offen offset:1024 sc1\n" \
    "v_cvt_pk_bf16_f32 v168, v72, v73\n" \
    "v_cvt_pk_bf16_f32 v169, v74, v75\n" \
    "v_cvt_pk_bf16_f32 v170, v88, v89\n" \
    "v_cvt_pk_bf16_f32 v171, v90, v91\n" \
    "buffer_store_dwordx4 v[168:171], v18, s[36:39], s74 offen offset:2048 sc1\n" \
    "v_cvt_pk_bf16_f32 v172, v76, v77\n" \
    "v_cvt_pk_bf16_f32 v173, v78, v79\n" \
    "v_cvt_pk_bf16_f32 v174, v92, v93\n" \
    "v_cvt_pk_bf16_f32 v175, v94, v95\n" \
    "buffer_store_dwordx4 v[172:175], v18, s[36:39], s74 offen offset:3072 sc1\n" \
    "v_mov_b32 v22, s63\n" \
    "v_mov_b32 v23, 1\n" \
    "s_waitcnt vmcnt(0)\n" \
    "buffer_store_dwordx2 v[22:23], v17, s[36:39], 0 offen sc1\n" \
    "P1_la_%=:\n" \
    "buffer_load_dwordx2 v[22:23], v19, s[36:39], 0 offen sc1\n" \
    "s_waitcnt vmcnt(0)\n" \
    "v_cmp_eq_u32 vcc, s63, v22\n" \
    "v_cmp_eq_u32 s[76:77], s63, v23\n" \
    "s_and_b64 vcc, vcc, s[76:77]\n" \
    "s_and_b64 vcc, vcc, exec\n" \
    "s_cmp_eq_u64 vcc, exec\n" \
    "s_cbranch_scc0 P1_la_no_%=\n" \
    "P1_loop_%=:\n" \
    "s_mul_i32 s68, s70, 24576\n" \
    "s_add_u32 s74, s68, s53\n" \
    "s_add_u32 s70, s70, 1\n" \
    "s_add_u32 s68, s70, 2\n" \
    "s_min_u32 s68, s68, 63\n" \
    "s_mul_i32 s68, s68, 40960\n" \
    "s_add_u32 s40, s68, s54\n" \
    "s_add_u32 s40, s40, 2097152\n" \
    "s_add_u32 s41, s40, 4096\n" \
    "s_add_u32 s42, s40, 8192\n" \
    "s_mov_b32 s43, s75\n" \
    "s_waitcnt lgkmcnt(2)\n" \
    "v_mfma_f32_16x16x32_bf16 v[128:131], v[96:99], a[0:3], 0\n" \
    "ds_read_b128 v[112:115], v21 offset:2048\n" \
    "v_mfma_f32_16x16x32_bf16 v[144:147], v[100:103], a[0:3], 0\n" \
    "ds_read_b128 v[116:119], v21 offset:22528\n" \
    "v_mfma_f32_16x16x32_bf16 v[132:135], v[96:99], a[4:7], 0\n" \
    "s_add_u32 m0, s43, 0\n" \
    "v_mfma_f32_16x16x32_bf16 v[148:151], v[100:103], a[4:7], 0\n" \
    "buffer_load_dwordx4 v16, s[36:39], s40 offen offset:0 sc1 lds\n" \
    "v_mfma_f32_16x16x32_bf16 v[136:139], v[96:99], a[8:11], 0\n" \
    "v_mfma_f32_16x16x32_bf16 v[152:155], v[100:103], a[8:11], 0\n" \
    "v_mfma_f32_16x16x32_bf16 v[140:143], v[96:99], a[12:15], 0\n" \
    "v_mfma_f32_16x16x32_bf16 v[156:159], v[100:103], a[12:15], 0\n" \
    "s_waitcnt lgkmcnt(2)\n" \
    "v_mfma_f32_16x16x32_bf16 v[128:131], v[104:107], a[16:19], v[128:131]\n" \
    "ds_read_b128 v[120:123], v21 offset:3072\n" \
    "v_mfma_f32_16x16x32_bf16 v[144:147], v[108:111], a[16:19], v[144:147]\n" \
    "ds_read_b128 v[124:127], v21 offset:23552\n" \
    "v_mfma_f32_16x16x32_bf16 v[132:135], v[104:107], a[20:23], v[132:135]\n" \
    "s_add_u32 m0, s43, 0\n" \
    "v_mfma_f32_16x16x32_bf16 v[148:151], v[108:111], a[20:23], v[148:151]\n" \
    "buffer_load_dwordx4 v16, s[36:39], s40 offen offset:1024 sc1 lds\n" \
    "v_mfma_f32_16x16x32_bf16 v[136:139], v[104:107], a[24:27], v[136:139]\n" \
    "v_cvt_pk_bf16_f32 v160, v64, v65\n" \
    "v_cvt_pk_bf16_f32 v161, v66, v67\n" \
    "v_mfma_f32_16x16x32_bf16 v[152:155], v[108:111], a[24:27], v[152:155]\n" \
    "v_cvt_pk_bf16_f32 v162, v80, v81\n" \
    "v_mfma_f32_16x16x32_bf16 v[140:143], v[104:107], a[28:31], v[140:143]\n" \
    "v_cvt_pk_bf16_f32 v163, v82, v83\n" \
    "buffer_store_dwordx4 v[160:163], v18, s[36:39], s74 offen offset:0 sc1\n" \
    "v_mfma_f32_16x16x32_bf16 v[156:159], v[108:111], a[28:31], v[156:159]\n" \
    "v_cvt_pk_bf16_f32 v164, v68, v69\n" \
    "s_waitcnt lgkmcnt(2)\n" \
    "v_mfma_f32_16x16x32_bf16 v[128:131], v[112:115], a[32:35], v[128:131]\n" \
    "ds_read_b128 v[96:99], v21 offset:4096\n" \
    "v_mfma_f32_16x16x32_bf16 v[144:147], v[116:119], a[32:35], v[144:147]\n" \
    "ds_read_b128 v[100:103], v21 offset:24576\n" \
    "v_mfma_f32_16x16x32_bf16 v[132:135], v[112:115], a[36:39], v[132:135]\n" \
    "s_add_u32 m0, s43, 0\n" \
    "v_mfma_f32_16x16x32_bf16 v[148:151], v[116:119], a[36:39], v[148:151]\n" \
    "buffer_load_dwordx4 v16, s[36:39], s40 offen offset:2048 sc1 lds\n" \
    "v_mfma_f32_16x16x32_bf16 v[136:139], v[112:115], a[40:43], v[136:139]\n" \
    "v_cvt_pk_bf16_f32 v165, v70, v71\n" \
    "v_cvt_pk_bf16_f32 v166, v84, v85\n" \
    "v_mfma_f32_16x16x32_bf16 v[152:155], v[116:119], a[40:43], v[152:155]\n" \
    "v_cvt_pk_bf16_f32 v167, v86, v87\n" \
    "v_mfma_f32_16x16x32_bf16 v[140:143], v[112:115], a[44:47], v[140:143]\n" \
    "buffer_store_dwordx4 v[164:167], v18, s[36:39], s74 offen offset:1024 sc1\n" \
    "v_cvt_pk_bf16_f32 v168, v72, v73\n" \
    "v_mfma_f32_16x16x32_bf16 v[156:159], v[116:119], a[44:47], v[156:159]\n" \
    "v_cvt_pk_bf16_f32 v169, v74, v75\n" \
    "s_waitcnt lgkmcnt(2)\n" \
    "v_mfma_f32_16x16x32_bf16 v[128:131], v[120:123], a[48:51], v[128:131]\n" \
    "ds_read_b128 v[104:107], v21 offset:5120\n" \
    "v_mfma_f32_16x16x32_bf16 v[144:147], v[124:127], a[48:51], v[144:147]\n" \
    "ds_read_b128 v[108:111], v21 offset:25600\n" \
    "v_mfma_f32_16x16x32_bf16 v[132:135], v[120:123], a[52:55], v[132:135]\n" \
    "s_add_u32 m0, s43, 0\n" \
    "v_mfma_f32_16x16x32_bf16 v[148:151], v[124:127], a[52:55], v[148:151]\n" \
    "buffer_load_dwordx4 v16, s[36:39], s40 offen offset:3072 sc1 lds\n" \
    "v_mfma_f32_16x16x32_bf16 v[136:139], v[120:123], a[56:59], v[136:139]\n" \
    "v_cvt_pk_bf16_f32 v170, v88, v89\n" \
    "v_cvt_pk_bf16_f32 v171, v90, v91\n" \
    "v_mfma_f32_16x16x32_bf16 v[152:155], v[124:127], a[56:59], v[152:155]\n" \
    "buffer_store_dwordx4 v[168:171], v18, s[36:39], s74 offen offset:2048 sc1\n" \
    "v_mfma_f32_16x16x32_bf16 v[140:143], v[120:123], a[60:63], v[140:143]\n" \
    "v_cvt_pk_bf16_f32 v172, v76, v77\n" \
    "v_cvt_pk_bf16_f32 v173, v78, v79\n" \
    "v_mfma_f32_16x16x32_bf16 v[156:159], v[124:127], a[60:63], v[156:159]\n" \
    "v_cvt_pk_bf16_f32 v174, v92, v93\n" \
    "s_waitcnt lgkmcnt(2)\n" \
    "v_mfma_f32_16x16x32_bf16 v[128:131], v[96:99], a[64:67], v[128:131]\n" \
    "ds_read_b128 v[112:115], v21 offset:6144\n" \
    "v_mfma_f32_16x16x32_bf16 v[144:147], v[100:103], a[64:67], v[144:147]\n" \
    "ds_read_b128 v[116:119], v21 offset:26624\n" \
    "v_mfma_f32_16x16x32_bf16 v[132:135], v[96:99], a[68:71], v[132:135]\n" \
    "s_add_u32 m0, s43, 4096\n" \
    "v_mfma_f32_16x16x32_bf16 v[148:151], v[100:103], a[68:71], v[148:151]\n" \
    "buffer_load_dwordx4 v16, s[36:39], s41 offen offset:0 sc1 lds\n" \
    "v_mfma_f32_16x16x32_bf16 v[136:139], v[96:99], a[72:75], v[136:139]\n" \
    "v_cvt_pk_bf16_f32 v175, v94, v95\n" \
    "v_mfma_f32_16x16x32_bf16 v[152:155], v[100:103], a[72:75], v[152:155]\n" \
    "v_mfma_f32_16x16x32_bf16 v[140:143], v[96:99], a[76:79], v[140:143]\n" \
    "buffer_store_dwordx4 v[172:175], v18, s[36:39], s74 offen offset:3072 sc1\n" \
    "v_mfma_f32_16x16x32_bf16 v[156:159], v[100:103], a[76:79], v[156:159]\n" \
    "s_waitcnt lgkmcnt(2)\n" \
    "v_mfma_f32_16x16x32_bf16 v[128:131], v[104:107], a[80:83], v[128:131]\n" \
    "ds_read_b128 v[120:123], v21 offset:7168\n" \
    "v_mfma_f32_16x16x32_bf16 v[144:147], v[108:111], a[80:83], v[144:147]\n" \
    "ds_read_b128 v[124:127], v21 offset:27648\n" \
    "v_mfma_f32_16x16x32_bf16 v[132:135], v[104:107], a[84:87], v[132:135]\n" \
    "s_add_u32 m0, s43, 4096\n" \
    "v_mfma_f32_16x16x32_bf16 v[148:151], v[108:111], a[84:87], v[148:151]\n" \
    "buffer_load_dwordx4 v16, s[36:39], s41 offen offset:1024 sc1 lds\n" \
    "v_mfma_f32_16x16x32_bf16 v[136:139], v[104:107], a[88:91], v[136:139]\n" \
    "v_mfma_f32_16x16x32_bf16 v[152:155], v[108:111], a[88:91], v[152:155]\n" \
    "v_mfma_f32_16x16x32_bf16 v[140:143], v[104:107], a[92:95], v[140:143]\n" \
    "v_mfma_f32_16x16x32_bf16 v[156:159], v[108:111], a[92:95], v[156:159]\n" \
    "s_waitcnt lgkmcnt(2)\n" \
    "v_mfma_f32_16x16x32_bf16 v[128:131], v[112:115], a[96:99], v[128:131]\n" \
    "ds_read_b128 v[96:99], v21 offset:8192\n" \
    "v_mfma_f32_16x16x32_bf16 v[144:147], v[116:119], a[96:99], v[144:147]\n" \
    "ds_read_b128 v[100:103], v21 offset:28672\n" \
    "v_mfma_f32_16x16x32_bf16 v[132:135], v[112:115], a[100:103], v[132:135]\n" \
    "s_add_u32 m0, s43, 4096\n" \
    "v_mfma_f32_16x16x32_bf16 v[148:151], v[116:119], a[100:103], v[148:151]\n" \
    "buffer_load_dwordx4 v16, s[36:39], s41 offen offset:2048 sc1 lds\n" \
    "v_mfma_f32_16x16x32_bf16 v[136:139], v[112:115], a[104:107], v[136:139]\n" \
    "v_mfma_f32_16x16x32_bf16 v[152:155], v[116:119], a[104:107], v[152:155]\n" \
    "v_mfma_f32_16x16x32_bf16 v[140:143], v[112:115], a[108:111], v[140:143]\n" \
    "v_mfma_f32_16x16x32_bf16 v[156:159], v[116:119], a[108:111], v[156:159]\n" \
    "s_waitcnt lgkmcnt(2)\n" \
    "v_mfma_f32_16x16x32_bf16 v[128:131], v[120:123], a[112:115], v[128:131]\n" \
    "ds_read_b128 v[104:107], v21 offset:9216\n" \
    "v_mfma_f32_16x16x32_bf16 v[144:147], v[124:127], a[112:115], v[144:147]\n" \
    "ds_read_b128 v[108:111], v21 offset:29696\n" \
    "v_mfma_f32_16x16x32_bf16 v[132:135], v[120:123], a[116:119], v[132:135]\n" \
    "s_add_u32 m0, s43, 4096\n" \
    "v_mfma_f32_16x16x32_bf16 v[148:151], v[124:127], a[116:119], v[148:151]\n" \
    "buffer_load_dwordx4 v16, s[36:39], s41 offen offset:3072 sc1 lds\n" \
    "v_mfma_f32_16x16x32_bf16 v[136:139], v[120:123], a[120:123], v[136:139]\n" \
    "v_mfma_f32_16x16x32_bf16 v[152:155], v[124:127], a[120:123], v[152:155]\n" \
    "v_mfma_f32_16x16x32_bf16 v[140:143], v[120:123], a[124:127], v[140:143]\n" \
    "v_mfma_f32_16x16x32_bf16 v[156:159], v[124:127], a[124:127], v[156:159]\n" \
    "s_waitcnt lgkmcnt(2)\n" \
    "v_mfma_f32_16x16x32_bf16 v[128:131], v[96:99], a[128:131], v[128:131]\n" \
    "ds_read_b128 v[112:115], v21 offset:10240\n" \
    "v_mfma_f32_16x16x32_bf16 v[144:147], v[100:103], a[128:131], v[144:147]\n" \
    "ds_read_b128 v[116:119], v21 offset:30720\n" \
    "v_mfma_f32_16x16x32_bf16 v[132:135], v[96:99], a[132:135], v[132:135]\n" \
    "s_add_u32 m0, s43, 8192\n" \
    "v_mfma_f32_16x16x32_bf16 v[148:151], v[100:103], a[132:135], v[148:151]\n" \
    "buffer_load_dwordx4 v16, s[36:39], s42 offen offset:0 sc1 lds\n" \
    "v_mfma_f32_16x16x32_bf16 v[136:139], v[96:99], a[136:139], v[136:139]\n" \
    "v_mfma_f32_16x16x32_bf16 v[152:155], v[100:103], a[136:139], v[152:155]\n" \
    "v_mfma_f32_16x16x32_bf16 v[140:143], v[96:99], a[140:143], v[140:143]\n" \
    "v_mfma_f32_16x16x32_bf16 v[156:159], v[100:103], a[140:143], v[156:159]\n" \
    "s_waitcnt lgkmcnt(2)\n" \
    "v_mfma_f32_16x16x32_bf16 v[128:131], v[104:107], a[144:147], v[128:131]\n" \
    "ds_read_b128 v[120:123], v21 offset:11264\n" \
    "v_mfma_f32_16x16x32_bf16 v[144:147], v[108:111], a[144:147], v[144:147]\n" \
    "ds_read_b128 v[124:127], v21 offset:31744\n" \
    "v_mfma_f32_16x16x32_bf16 v[132:135], v[104:107], a[148:151], v[132:135]\n" \
    "s_add_u32 m0, s43, 8192\n" \
    "v_mfma_f32_16x16x32_bf16 v[148:151], v[108:111], a[148:151], v[148:151]\n" \
    "buffer_load_dwordx4 v16, s[36:39], s42 offen offset:1024 sc1 lds\n" \
    "v_mfma_f32_16x16x32_bf16 v[136:139], v[104:107], a[152:155], v[136:139]\n" \
    "v_mfma_f32_16x16x32_bf16 v[152:155], v[108:111], a[152:155], v[152:155]\n" \
    "v_mfma_f32_16x16x32_bf16 v[140:143], v[104:107], a[156:159], v[140:143]\n" \
    "v_mfma_f32_16x16x32_bf16 v[156:159], v[108:111], a[156:159], v[156:159]\n" \
    "s_waitcnt lgkmcnt(2)\n" \
    "v_mfma_f32_16x16x32_bf16 v[128:131], v[112:115], a[160:163], v[128:131]\n" \
    "ds_read_b128 v[96:99], v21 offset:12288\n" \
    "v_mfma_f32_16x16x32_bf16 v[144:147], v[116:119], a[160:163], v[144:147]\n" \
    "ds_read_b128 v[100:103], v21 offset:32768\n" \
    "v_mfma_f32_16x16x32_bf16 v[132:135], v[112:115], a[164:167], v[132:135]\n" \
    "v_mfma_f32_16x16x32_bf16 v[148:151], v[116:119], a[164:167], v[148:151]\n" \
    "v_mfma_f32_16x16x32_bf16 v[136:139], v[112:115], a[168:171], v[136:139]\n" \
    "v_mov_b32 v22, s63\n" \
    "v_mov_b32 v23, s70\n" \
    "v_mfma_f32_16x16x32_bf16 v[152:155], v[116:119], a[168:171], v[152:155]\n" \
    "s_waitcnt vmcnt(5)\n" \
    "v_mfma_f32_16x16x32_bf16 v[140:143], v[112:115], a[172:175], v[140:143]\n" \
    "buffer_store_dwordx2 v[22:23], v17, s[36:39], 0 offen sc1\n" \
    "v_mfma_f32_16x16x32_bf16 v[156:159], v[116:119], a[172:175], v[156:159]\n" \
    "s_waitcnt lgkmcnt(2)\n" \
    "v_mfma_f32_16x16x32_bf16 v[128:131], v[120:123], a[176:179], v[128:131]\n" \
    "ds_read_b128 v[104:107], v21 offset:13312\n" \
    "v_mfma_f32_16x16x32_bf16 v[144:147], v[124:127], a[176:179], v[144:147]\n" \
    "ds_read_b128 v[108:111], v21 offset:33792\n" \
    "v_mfma_f32_16x16x32_bf16 v[132:135], v[120:123], a[180:183], v[132:135]\n" \
    "v_mfma_f32_16x16x32_bf16 v[148:151], v[124:127], a[180:183], v[148:151]\n" \
    "v_mfma_f32_16x16x32_bf16 v[136:139], v[120:123], a[184:187], v[136:139]\n" \
    "v_mfma_f32_16x16x32_bf16 v[152:155], v[124:127], a[184:187], v[152:155]\n" \
    "v_mfma_f32_16x16x32_bf16 v[140:143], v[120:123], a[188:191], v[140:143]\n" \
    "v_mfma_f32_16x16x32_bf16 v[156:159], v[124:127], a[188:191], v[156:159]\n" \
    "s_waitcnt lgkmcnt(2)\n" \
    "v_mfma_f32_16x16x32_bf16 v[128:131], v[96:99], a[192:195], v[128:131]\n" \
    "ds_read_b128 v[112:115], v21 offset:14336\n" \
    "v_mfma_f32_16x16x32_bf16 v[144:147], v[100:103], a[192:195], v[144:147]\n" \
    "ds_read_b128 v[116:119], v21 offset:34816\n" \
    "v_mfma_f32_16x16x32_bf16 v[132:135], v[96:99], a[196:199], v[132:135]\n" \
    "v_mfma_f32_16x16x32_bf16 v[148:151], v[100:103], a[196:199], v[148:151]\n" \
    "v_mfma_f32_16x16x32_bf16 v[136:139], v[96:99], a[200:203], v[136:139]\n" \
    "v_mfma_f32_16x16x32_bf16 v[152:155], v[100:103], a[200:203], v[152:155]\n" \
    "v_mfma_f32_16x16x32_bf16 v[140:143], v[96:99], a[204:207], v[140:143]\n" \
    "v_mfma_f32_16x16x32_bf16 v[156:159], v[100:103], a[204:207], v[156:159]\n" \
    "s_waitcnt lgkmcnt(2)\n" \
    "v_mfma_f32_16x16x32_bf16 v[128:131], v[104:107], a[208:211], v[128:131]\n" \
    "ds_read_b128 v[120:123], v21 offset:15360\n" \
    "v_mfma_f32_16x16x32_bf16 v[144:147], v[108:111], a[208:211], v[144:147]\n" \
    "ds_read_b128 v[124:127], v21 offset:35840\n" \
    "v_mfma_f32_16x16x32_bf16 v[132:135], v[104:107], a[212:215], v[132:135]\n" \
    "v_mfma_f32_16x16x32_bf16 v[148:151], v[108:111], a[212:215], v[148:151]\n" \
    "v_mfma_f32_16x16x32_bf16 v[136:139], v[104:107], a[216:219], v[136:139]\n" \
    "v_mfma_f32_16x16x32_bf16 v[152:155], v[108:111], a[216:219], v[152:155]\n" \
    "v_mfma_f32_16x16x32_bf16 v[140:143], v[104:107], a[220:223], v[140:143]\n" \
    "v_mfma_f32_16x16x32_bf16 v[156:159], v[108:111], a[220:223], v[156:159]\n" \
    "s_waitcnt lgkmcnt(2)\n" \
    "v_mfma_f32_16x16x32_bf16 v[128:131], v[112:115], a[224:227], v[128:131]\n" \
    "ds_read_b128 v[96:99], v21 offset:16384\n" \
    "v_mfma_f32_16x16x32_bf16 v[144:147], v[116:119], a[224:227], v[144:147]\n" \
    "ds_read_b128 v[100:103], v21 offset:36864\n" \
    "v_mfma_f32_16x16x32_bf16 v[132:135], v[112:115], a[228:231], v[132:135]\n" \
    "v_mfma_f32_16x16x32_bf16 v[148:151], v[116:119], a[228:231], v[148:151]\n" \
    "v_mfma_f32_16x16x32_bf16 v[136:139], v[112:115], a[232:235], v[136:139]\n" \
    "v_mfma_f32_16x16x32_bf16 v[152:155], v[116:119], a[232:235], v[152:155]\n" \
    "v_mfma_f32_16x16x32_bf16 v[140:143], v[112:115], a[236:239], v[140:143]\n" \
    "v_mfma_f32_16x16x32_bf16 v[156:159], v[116:119], a[236:239], v[156:159]\n" \
    "s_waitcnt lgkmcnt(2)\n" \
    "v_mfma_f32_16x16x32_bf16 v[128:131], v[120:123], a[240:243], v[128:131]\n" \
    "ds_read_b128 v[104:107], v21 offset:17408\n" \
    "v_mfma_f32_16x16x32_bf16 v[144:147], v[124:127], a[240:243], v[144:147]\n" \
    "ds_read_b128 v[108:111], v21 offset:37888\n" \
    "v_mfma_f32_16x16x32_bf16 v[132:135], v[120:123], a[244:247], v[132:135]\n" \
    "v_mfma_f32_16x16x32_bf16 v[148:151], v[124:127], a[244:247], v[148:151]\n" \
    "v_mfma_f32_16x16x32_bf16 v[136:139], v[120:123], a[248:251], v[136:139]\n" \
    "v_mfma_f32_16x16x32_bf16 v[152:155], v[124:127], a[248:251], v[152:155]\n" \
    "v_mfma_f32_16x16x32_bf16 v[140:143], v[120:123], a[252:255], v[140:143]\n" \
    "v_mfma_f32_16x16x32_bf16 v[156:159], v[124:127], a[252:255], v[156:159]\n" \
    "s_waitcnt lgkmcnt(2)\n" \
    "v_mfma_f32_16x16x32_bf16 v[128:131], v[96:99], v[192:195], v[128:131]\n" \
    "ds_read_b128 v[112:115], v21 offset:18432\n" \
    "v_mfma_f32_16x16x32_bf16 v[144:147], v[100:103], v[192:195], v[144:147]\n" \
    "ds_read_b128 v[116:119], v21 offset:38912\n" \
    "v_mfma_f32_16x16x32_bf16 v[132:135], v[96:99], v[196:199], v[132:135]\n" \
    "v_mfma_f32_16x16x32_bf16 v[148:151], v[100:103], v[196:199], v[148:151]\n" \
    "v_mfma_f32_16x16x32_bf16 v[136:139], v[96:99], v[200:203], v[136:139]\n" \
    "v_mfma_f32_16x16x32_bf16 v[152:155], v[100:103], v[200:203], v[152:155]\n" \
    "v_mfma_f32_16x16x32_bf16 v[140:143], v[96:99], v[204:207], v[140:143]\n" \
    "v_mfma_f32_16x16x32_bf16 v[156:159], v[100:103], v[204:207], v[156:159]\n" \
    "s_waitcnt lgkmcnt(2)\n" \
    "v_mfma_f32_16x16x32_bf16 v[128:131], v[104:107], v[208:211], v[128:131]\n" \
    "ds_read_b128 v[120:123], v21 offset:19456\n" \
    "v_mfma_f32_16x16x32_bf16 v[144:147], v[108:111], v[208:211], v[144:147]\n" \
    "ds_read_b128 v[124:127], v21 offset:39936\n" \
    "v_mfma_f32_16x16x32_bf16 v[132:135], v[104:107], v[212:215], v[132:135]\n" \
    "v_mfma_f32_16x16x32_bf16 v[148:151], v[108:111], v[212:215], v[148:151]\n" \
    "v_mfma_f32_16x16x32_bf16 v[136:139], v[104:107], v[216:219], v[136:139]\n" \
    "v_mfma_f32_16x16x32_bf16 v[152:155], v[108:111], v[216:219], v[152:155]\n" \
    "v_mfma_f32_16x16x32_bf16 v[140:143], v[104:107], v[220:223], v[140:143]\n" \
    "v_mfma_f32_16x16x32_bf16 v[156:159], v[108:111], v[220:223], v[156:159]\n" \
    "s_add_u32 s75, s72, s54\n" \
    "s_add_u32 s72, s72, 40960\n" \
    "s_cmp_eq_u32 s72, 81920\n" \
    "s_cselect_b32 s72, 114688, s72\n" \
    "s_cmp_eq_u32 s72, 155648\n" \
    "s_cselect_b32 s72, 0, s72\n" \
    "v_add_u32 v21, s72, v16\n" \
    "s_waitcnt lgkmcnt(0)\n" \
    "s_barrier\n" \
    "v_mfma_f32_16x16x32_bf16 v[128:131], v[112:115], v[224:227], v[128:131]\n" \
    "ds_read_b128 v[96:99], v21 offset:0\n" \
    "v_mfma_f32_16x16x32_bf16 v[144:147], v[116:119], v[224:227], v[144:147]\n" \
    "ds_read_b128 v[100:103], v21 offset:20480\n" \
    "v_mfma_f32_16x16x32_bf16 v[132:135], v[112:115], v[228:231], v[132:135]\n" \
    "v_mfma_f32_16x16x32_bf16 v[148:151], v[116:119], v[228:231], v[148:151]\n" \
    "v_mfma_f32_16x16x32_bf16 v[136:139], v[112:115], v[232:235], v[136:139]\n" \
    "v_mfma_f32_16x16x32_bf16 v[152:155], v[116:119], v[232:235], v[152:155]\n" \
    "v_mfma_f32_16x16x32_bf16 v[140:143], v[112:115], v[236:239], v[140:143]\n" \
    "v_mfma_f32_16x16x32_bf16 v[156:159], v[116:119], v[236:239], v[156:159]\n" \
    "v_mfma_f32_16x16x32_bf16 v[128:131], v[120:123], v[240:243], v[128:131]\n" \
    "ds_read_b128 v[104:107], v21 offset:1024\n" \
    "v_mfma_f32_16x16x32_bf16 v[144:147], v[124:127], v[240:243], v[144:147]\n" \
    "ds_read_b128 v[108:111], v21 offset:21504\n" \
    "v_mfma_f32_16x16x32_bf16 v[132:135], v[120:123], v[244:247], v[132:135]\n" \
    "v_mfma_f32_16x16x32_bf16 v[148:151], v[124:127], v[244:247], v[148:151]\n" \
    "v_mfma_f32_16x16x32_bf16 v[136:139], v[120:123], v[248:251], v[136:139]\n" \
    "v_mfma_f32_16x16x32_bf16 v[152:155], v[124:127], v[248:251], v[152:155]\n" \
    "v_mfma_f32_16x16x32_bf16 v[140:143], v[120:123], v[252:255], v[140:143]\n" \
    "v_mfma_f32_16x16x32_bf16 v[156:159], v[124:127], v[252:255], v[156:159]\n" \
    "s_cmp_eq_u32 s70, 63\n" \
    "s_cbranch_scc1 P1_fin_%=\n" \
    "s_mul_i32 s68, s70, 24576\n" \
    "s_add_u32 s74, s68, s53\n" \
    "s_add_u32 s70, s70, 1\n" \
    "s_add_u32 s68, s70, 2\n" \
    "s_min_u32 s68, s68, 63\n" \
    "s_mul_i32 s68, s68, 40960\n" \
    "s_add_u32 s40, s68, s54\n" \
    "s_add_u32 s40, s40, 2097152\n" \
    "s_add_u32 s41, s40, 4096\n" \
    "s_add_u32 s42, s40, 8192\n" \
    "s_mov_b32 s43, s75\n" \
    "s_waitcnt lgkmcnt(2)\n" \
    "v_mfma_f32_16x16x32_bf16 v[64:67], v[96:99], a[0:3], 0\n" \
    "ds_read_b128 v[112:115], v21 offset:2048\n" \
    "v_mfma_f32_16x16x32_bf16 v[80:83], v[100:103], a[0:3], 0\n" \
    "ds_read_b128 v[116:119], v21 offset:22528\n" \
    "v_mfma_f32_16x16x32_bf16 v[68:71], v[96:99], a[4:7], 0\n" \
    "s_add_u32 m0, s43, 0\n" \
    "v_mfma_f32_16x16x32_bf16 v[84:87], v[100:103], a[4:7], 0\n" \
    "buffer_load_dwordx4 v16, s[36:39], s40 offen offset:0 sc1 lds\n" \
    "v_mfma_f32_16x16x32_bf16 v[72:75], v[96:99], a[8:11], 0\n" \
    "v_mfma_f32_16x16x32_bf16 v[88:91], v[100:103], a[8:11], 0\n" \
    "v_mfma_f32_16x16x32_bf16 v[76:79], v[96:99], a[12:15], 0\n" \
    "v_mfma_f32_16x16x32_bf16 v[92:95], v[100:103], a[12:15], 0\n" \
    "s_waitcnt lgkmcnt(2)\n" \
    "v_mfma_f32_16x16x32_bf16 v[64:67], v[104:107], a[16:19], v[64:67]\n" \
    "ds_read_b128 v[120:123], v21 offset:3072\n" \
    "v_mfma_f32_16x16x32_bf16 v[80:83], v[108:111], a[16:19], v[80:83]\n" \
    "ds_read_b128 v[124:127], v21 offset:23552\n" \
    "v_mfma_f32_16x16x32_bf16 v[68:71], v[104:107], a[20:23], v[68:71]\n" \
    "s_add_u32 m0, s43, 0\n" \
    "v_mfma_f32_16x16x32_bf16 v[84:87], v[108:111], a[20:23], v[84:87]\n" \
    "buffer_load_dwordx4 v16, s[36:39], s40 offen offset:1024 sc1 lds\n" \
    "v_mfma_f32_16x16x32_bf16 v[72:75], v[104:107], a[24:27], v[72:75]\n" \
    "v_cvt_pk_bf16_f32 v160, v128, v129\n" \
    "v_cvt_pk_bf16_f32 v161, v130, v131\n" \
    "v_mfma_f32_16x16x32_bf16 v[88:91], v[108:111], a[24:27], v[88:91]\n" \
    "v_cvt_pk_bf16_f32 v162, v144, v145\n" \
    "v_mfma_f32_16x16x32_bf16 v[76:79], v[104:107], a[28:31], v[76:79]\n" \
    "v_cvt_pk_bf16_f32 v163, v146, v147\n" \
    "buffer_store_dwordx4 v[160:163], v18, s[36:39], s74 offen offset:0 sc1\n" \
    "v_mfma_f32_16x16x32_bf16 v[92:95], v[108:111], a[28:31], v[92:95]\n" \
    "v_cvt_pk_bf16_f32 v164, v132, v133\n" \
    "s_waitcnt lgkmcnt(2)\n" \
    "v_mfma_f32_16x16x32_bf16 v[64:67], v[112:115], a[32:35], v[64:67]\n" \
    "ds_read_b128 v[96:99], v21 offset:4096\n" \
    "v_mfma_f32_16x16x32_bf16 v[80:83], v[116:119], a[32:35], v[80:83]\n" \
    "ds_read_b128 v[100:103], v21 offset:24576\n" \
    "v_mfma_f32_16x16x32_bf16 v[68:71], v[112:115], a[36:39], v[68:71]\n" \
    "s_add_u32 m0, s43, 0\n" \
    "v_mfma_f32_16x16x32_bf16 v[84:87], v[116:119], a[36:39], v[84:87]\n" \
    "buffer_load_dwordx4 v16, s[36:39], s40 offen offset:2048 sc1 lds\n" \
    "v_mfma_f32_16x16x32_bf16 v[72:75], v[112:115], a[40:43], v[72:75]\n" \
    "v_cvt_pk_bf16_f32 v165, v134, v135\n" \
    "v_cvt_pk_bf16_f32 v166, v148, v149\n" \
    "v_mfma_f32_16x16x32_bf16 v[88:91], v[116:119], a[40:43], v[88:91]\n" \
    "v_cvt_pk_bf16_f32 v167, v150, v151\n" \
    "v_mfma_f32_16x16x32_bf16 v[76:79], v[112:115], a[44:47], v[76:79]\n" \
    "buffer_store_dwordx4 v[164:167], v18, s[36:39], s74 offen offset:1024 sc1\n" \
    "v_cvt_pk_bf16_f32 v168, v136, v137\n" \
    "v_mfma_f32_16x16x32_bf16 v[92:95], v[116:119], a[44:47], v[92:95]\n" \
    "v_cvt_pk_bf16_f32 v169, v138, v139\n" \
    "s_waitcnt lgkmcnt(2)\n" \
    "v_mfma_f32_16x16x32_bf16 v[64:67], v[120:123], a[48:51], v[64:67]\n" \
    "ds_read_b128 v[104:107], v21 offset:5120\n" \
    "v_mfma_f32_16x16x32_bf16 v[80:83], v[124:127], a[48:51], v[80:83]\n" \
    "ds_read_b128 v[108:111], v21 offset:25600\n" \
    "v_mfma_f32_16x16x32_bf16 v[68:71], v[120:123], a[52:55], v[68:71]\n" \
    "s_add_u32 m0, s43, 0\n" \
    "v_mfma_f32_16x16x32_bf16 v[84:87], v[124:127], a[52:55], v[84:87]\n" \
    "buffer_load_dwordx4 v16, s[36:39], s40 offen offset:3072 sc1 lds\n" \
    "v_mfma_f32_16x16x32_bf16 v[72:75], v[120:123], a[56:59], v[72:75]\n" \
    "v_cvt_pk_bf16_f32 v170, v152, v153\n" \
    "v_cvt_pk_bf16_f32 v171, v154, v155\n" \
    "v_mfma_f32_16x16x32_bf16 v[88:91], v[124:127], a[56:59], v[88:91]\n" \
    "buffer_store_dwordx4 v[168:171], v18, s[36:39], s74 offen offset:2048 sc1\n" \
    "v_mfma_f32_16x16x32_bf16 v[76:79], v[120:123], a[60:63], v[76:79]\n" \
    "v_cvt_pk_bf16_f32 v172, v140, v141\n" \
    "v_cvt_pk_bf16_f32 v173, v142, v143\n" \
    "v_mfma_f32_16x16x32_bf16 v[92:95], v[124:127], a[60:63], v[92:95]\n" \
    "v_cvt_pk_bf16_f32 v174, v156, v157\n" \
    "s_waitcnt lgkmcnt(2)\n" \
    "v_mfma_f32_16x16x32_bf16 v[64:67], v[96:99], a[64:67], v[64:67]\n" \
    "ds_read_b128 v[112:115], v21 offset:6144\n" \
    "v_mfma_f32_16x16x32_bf16 v[80:83], v[100:103], a[64:67], v[80:83]\n" \
    "ds_read_b128 v[116:119], v21 offset:26624\n" \
    "v_mfma_f32_16x16x32_bf16 v[68:71], v[96:99], a[68:71], v[68:71]\n" \
    "s_add_u32 m0, s43, 4096\n" \
    "v_mfma_f32_16x16x32_bf16 v[84:87], v[100:103], a[68:71], v[84:87]\n" \
    "buffer_load_dwordx4 v16, s[36:39], s41 offen offset:0 sc1 lds\n" \
    "v_mfma_f32_16x16x32_bf16 v[72:75], v[96:99], a[72:75], v[72:75]\n" \
    "v_cvt_pk_bf16_f32 v175, v158, v159\n" \
    "v_mfma_f32_16x16x32_bf16 v[88:91], v[100:103], a[72:75], v[88:91]\n" \
    "v_mfma_f32_16x16x32_bf16 v[76:79], v[96:99], a[76:79], v[76:79]\n" \
    "buffer_store_dwordx4 v[172:175], v18, s[36:39], s74 offen offset:3072 sc1\n" \
    "v_mfma_f32_16x16x32_bf16 v[92:95], v[100:103], a[76:79], v[92:95]\n" \
    "s_waitcnt lgkmcnt(2)\n" \
    "v_mfma_f32_16x16x32_bf16 v[64:67], v[104:107], a[80:83], v[64:67]\n" \
    "ds_read_b128 v[120:123], v21 offset:7168\n" \
    "v_mfma_f32_16x16x32_bf16 v[80:83], v[108:111], a[80:83], v[80:83]\n" \
    "ds_read_b128 v[124:127], v21 offset:27648\n" \
    "v_mfma_f32_16x16x32_bf16 v[68:71], v[104:107], a[84:87], v[68:71]\n" \
    "s_add_u32 m0, s43, 4096\n" \
    "v_mfma_f32_16x16x32_bf16 v[84:87], v[108:111], a[84:87], v[84:87]\n" \
    "buffer_load_dwordx4 v16, s[36:39], s41 offen offset:1024 sc1 lds\n" \
    "v_mfma_f32_16x16x32_bf16 v[72:75], v[104:107], a[88:91], v[72:75]\n" \
    "v_mfma_f32_16x16x32_bf16 v[88:91], v[108:111], a[88:91], v[88:91]\n" \
    "v_mfma_f32_16x16x32_bf16 v[76:79], v[104:107], a[92:95], v[76:79]\n" \
    "v_mfma_f32_16x16x32_bf16 v[92:95], v[108:111], a[92:95], v[92:95]\n" \
    "s_waitcnt lgkmcnt(2)\n" \
    "v_mfma_f32_16x16x32_bf16 v[64:67], v[112:115], a[96:99], v[64:67]\n" \
    "ds_read_b128 v[96:99], v21 offset:8192\n" \
    "v_mfma_f32_16x16x32_bf16 v[80:83], v[116:119], a[96:99], v[80:83]\n" \
    "ds_read_b128 v[100:103], v21 offset:28672\n" \
    "v_mfma_f32_16x16x32_bf16 v[68:71], v[112:115], a[100:103], v[68:71]\n" \
    "s_add_u32 m0, s43, 4096\n" \
    "v_mfma_f32_16x16x32_bf16 v[84:87], v[116:119], a[100:103], v[84:87]\n" \
    "buffer_load_dwordx4 v16, s[36:39], s41 offen offset:2048 sc1 lds\n" \
    "v_mfma_f32_16x16x32_bf16 v[72:75], v[112:115], a[104:107], v[72:75]\n" \
    "v_mfma_f32_16x16x32_bf16 v[88:91], v[116:119], a[104:107], v[88:91]\n" \
    "v_mfma_f32_16x16x32_bf16 v[76:79], v[112:115], a[108:111], v[76:79]\n" \
    "v_mfma_f32_16x16x32_bf16 v[92:95], v[116:119], a[108:111], v[92:95]\n" \
    "s_waitcnt lgkmcnt(2)\n" \
    "v_mfma_f32_16x16x32_bf16 v[64:67], v[120:123], a[112:115], v[64:67]\n" \
    "ds_read_b128 v[104:107], v21 offset:9216\n" \
    "v_mfma_f32_16x16x32_bf16 v[80:83], v[124:127], a[112:115], v[80:83]\n" \
    "ds_read_b128 v[108:111], v21 offset:29696\n" \
    "v_mfma_f32_16x16x32_bf16 v[68:71], v[120:123], a[116:119], v[68:71]\n" \
    "s_add_u32 m0, s43, 4096\n" \
    "v_mfma_f32_16x16x32_bf16 v[84:87], v[124:127], a[116:119], v[84:87]\n" \
    "buffer_load_dwordx4 v16, s[36:39], s41 offen offset:3072 sc1 lds\n" \
    "v_mfma_f32_16x16x32_bf16 v[72:75], v[120:123], a[120:123], v[72:75]\n" \
    "v_mfma_f32_16x16x32_bf16 v[88:91], v[124:127], a[120:123], v[88:91]\n" \
    "v_mfma_f32_16x16x32_bf16 v[76:79], v[120:123], a[124:127], v[76:79]\n" \
    "v_mfma_f32_16x16x32_bf16 v[92:95], v[124:127], a[124:127], v[92:95]\n" \
    "s_waitcnt lgkmcnt(2)\n" \
    "v_mfma_f32_16x16x32_bf16 v[64:67], v[96:99], a[128:131], v[64:67]\n" \
    "ds_read_b128 v[112:115], v21 offset:10240\n" \
    "v_mfma_f32_16x16x32_bf16 v[80:83], v[100:103], a[128:131], v[80:83]\n" \
    "ds_read_b128 v[116:119], v21 offset:30720\n" \
    "v_mfma_f32_16x16x32_bf16 v[68:71], v[96:99], a[132:135], v[68:71]\n" \
    "s_add_u32 m0, s43, 8192\n" \
    "v_mfma_f32_16x16x32_bf16 v[84:87], v[100:103], a[132:135], v[84:87]\n" \
    "buffer_load_dwordx4 v16, s[36:39], s42 offen offset:0 sc1 lds\n" \
    "v_mfma_f32_16x16x32_bf16 v[72:75], v[96:99], a[136:139], v[72:75]\n" \
    "v_mfma_f32_16x16x32_bf16 v[88:91], v[100:103], a[136:139], v[88:91]\n" \
    "v_mfma_f32_16x16x32_bf16 v[76:79], v[96:99], a[140:143], v[76:79]\n" \
    "v_mfma_f32_16x16x32_bf16 v[92:95], v[100:103], a[140:143], v[92:95]\n" \
    "s_waitcnt lgkmcnt(2)\n" \
    "v_mfma_f32_16x16x32_bf16 v[64:67], v[104:107], a[144:147], v[64:67]\n" \
    "ds_read_b128 v[120:123], v21 offset:11264\n" \
    "v_mfma_f32_16x16x32_bf16 v[80:83], v[108:111], a[144:147], v[80:83]\n" \
    "ds_read_b128 v[124:127], v21 offset:31744\n" \
    "v_mfma_f32_16x16x32_bf16 v[68:71], v[104:107], a[148:151], v[68:71]\n" \
    "s_add_u32 m0, s43, 8192\n" \
    "v_mfma_f32_16x16x32_bf16 v[84:87], v[108:111], a[148:151], v[84:87]\n" \
    "buffer_load_dwordx4 v16, s[36:39], s42 offen offset:1024 sc1 lds\n" \
    "v_mfma_f32_16x16x32_bf16 v[72:75], v[104:107], a[152:155], v[72:75]\n" \
    "v_mfma_f32_16x16x32_bf16 v[88:91], v[108:111], a[152:155], v[88:91]\n" \
    "v_mfma_f32_16x16x32_bf16 v[76:79], v[104:107], a[156:159], v[76:79]\n" \
    "v_mfma_f32_16x16x32_bf16 v[92:95], v[108:111], a[156:159], v[92:95]\n" \
    "s_waitcnt lgkmcnt(2)\n" \
    "v_mfma_f32_16x16x32_bf16 v[64:67], v[112:115], a[160:163], v[64:67]\n" \
    "ds_read_b128 v[96:99], v21 offset:12288\n" \
    "v_mfma_f32_16x16x32_bf16 v[80:83], v[116:119], a[160:163], v[80:83]\n" \
    "ds_read_b128 v[100:103], v21 offset:32768\n" \
    "v_mfma_f32_16x16x32_bf16 v[68:71], v[112:115], a[164:167], v[68:71]\n" \
    "v_mfma_f32_16x16x32_bf16 v[84:87], v[116:119], a[164:167], v[84:87]\n" \
    "v_mfma_f32_16x16x32_bf16 v[72:75], v[112:115], a[168:171], v[72:75]\n" \
    "v_mov_b32 v22, s63\n" \
    "v_mov_b32 v23, s70\n" \
    "v_mfma_f32_16x16x32_bf16 v[88:91], v[116:119], a[168:171], v[88:91]\n" \
    "s_waitcnt vmcnt(5)\n" \
    "v_mfma_f32_16x16x32_bf16 v[76:79], v[112:115], a[172:175], v[76:79]\n" \
    "buffer_store_dwordx2 v[22:23], v17, s[36:39], 0 offen sc1\n" \
    "v_mfma_f32_16x16x32_bf16 v[92:95], v[116:119], a[172:175], v[92:95]\n" \
    "s_waitcnt lgkmcnt(2)\n" \
    "v_mfma_f32_16x16x32_bf16 v[64:67], v[120:123], a[176:179], v[64:67]\n" \
    "ds_read_b128 v[104:107], v21 offset:13312\n" \
    "v_mfma_f32_16x16x32_bf16 v[80:83], v[124:127], a[176:179], v[80:83]\n" \
    "ds_read_b128 v[108:111], v21 offset:33792\n" \
    "v_mfma_f32_16x16x32_bf16 v[68:71], v[120:123], a[180:183], v[68:71]\n" \
    "v_mfma_f32_16x16x32_bf16 v[84:87], v[124:127], a[180:183], v[84:87]\n" \
    "v_mfma_f32_16x16x32_bf16 v[72:75], v[120:123], a[184:187], v[72:75]\n" \
    "v_mfma_f32_16x16x32_bf16 v[88:91], v[124:127], a[184:187], v[88:91]\n" \
    "v_mfma_f32_16x16x32_bf16 v[76:79], v[120:123], a[188:191], v[76:79]\n" \
    "v_mfma_f32_16x16x32_bf16 v[92:95], v[124:127], a[188:191], v[92:95]\n" \
    "s_waitcnt lgkmcnt(2)\n" \
    "v_mfma_f32_16x16x32_bf16 v[64:67], v[96:99], a[192:195], v[64:67]\n" \
    "ds_read_b128 v[112:115], v21 offset:14336\n" \
    "v_mfma_f32_16x16x32_bf16 v[80:83], v[100:103], a[192:195], v[80:83]\n" \
    "ds_read_b128 v[116:119], v21 offset:34816\n" \
    "v_mfma_f32_16x16x32_bf16 v[68:71], v[96:99], a[196:199], v[68:71]\n" \
    "v_mfma_f32_16x16x32_bf16 v[84:87], v[100:103], a[196:199], v[84:87]\n" \
    "v_mfma_f32_16x16x32_bf16 v[72:75], v[96:99], a[200:203], v[72:75]\n" \
    "v_mfma_f32_16x16x32_bf16 v[88:91], v[100:103], a[200:203], v[88:91]\n" \
    "v_mfma_f32_16x16x32_bf16 v[76:79], v[96:99], a[204:207], v[76:79]\n" \
    "v_mfma_f32_16x16x32_bf16 v[92:95], v[100:103], a[204:207], v[92:95]\n" \
    "s_waitcnt lgkmcnt(2)\n" \
    "v_mfma_f32_16x16x32_bf16 v[64:67], v[104:107], a[208:211], v[64:67]\n" \
    "ds_read_b128 v[120:123], v21 offset:15360\n" \
    "v_mfma_f32_16x16x32_bf16 v[80:83], v[108:111], a[208:211], v[80:83]\n" \
    "ds_read_b128 v[124:127], v21 offset:35840\n" \
    "v_mfma_f32_16x16x32_bf16 v[68:71], v[104:107], a[212:215], v[68:71]\n" \
    "v_mfma_f32_16x16x32_bf16 v[84:87], v[108:111], a[212:215], v[84:87]\n" \
    "v_mfma_f32_16x16x32_bf16 v[72:75], v[104:107], a[216:219], v[72:75]\n" \
    "v_mfma_f32_16x16x32_bf16 v[88:91], v[108:111], a[216:219], v[88:91]\n" \
    "v_mfma_f32_16x16x32_bf16 v[76:79], v[104:107], a[220:223], v[76:79]\n" \
    "v_mfma_f32_16x16x32_bf16 v[92:95], v[108:111], a[220:223], v[92:95]\n" \
    "s_waitcnt lgkmcnt(2)\n" \
    "v_mfma_f32_16x16x32_bf16 v[64:67], v[112:115], a[224:227], v[64:67]\n" \
    "ds_read_b128 v[96:99], v21 offset:16384\n" \
    "v_mfma_f32_16x16x32_bf16 v[80:83], v[116:119], a[224:227], v[80:83]\n" \
    "ds_read_b128 v[100:103], v21 offset:36864\n" \
    "v_mfma_f32_16x16x32_bf16 v[68:71], v[112:115], a[228:231], v[68:71]\n" \
    "v_mfma_f32_16x16x32_bf16 v[84:87], v[116:119], a[228:231], v[84:87]\n" \
    "v_mfma_f32_16x16x32_bf16 v[72:75], v[112:115], a[232:235], v[72:75]\n" \
    "v_mfma_f32_16x16x32_bf16 v[88:91], v[116:119], a[232:235], v[88:91]\n" \
    "v_mfma_f32_16x16x32_bf16 v[76:79], v[112:115], a[236:239], v[76:79]\n" \
    "v_mfma_f32_16x16x32_bf16 v[92:95], v[116:119], a[236:239], v[92:95]\n" \
    "s_waitcnt lgkmcnt(2)\n" \
    "v_mfma_f32_16x16x32_bf16 v[64:67], v[120:123], a[240:243], v[64:67]\n" \
    "ds_read_b128 v[104:107], v21 offset:17408\n" \
    "v_mfma_f32_16x16x32_bf16 v[80:83], v[124:127], a[240:243], v[80:83]\n" \
    "ds_read_b128 v[108:111], v21 offset:37888\n" \
    "v_mfma_f32_16x16x32_bf16 v[68:71], v[120:123], a[244:247], v[68:71]\n" \
    "v_mfma_f32_16x16x32_bf16 v[84:87], v[124:127], a[244:247], v[84:87]\n" \
    "v_mfma_f32_16x16x32_bf16 v[72:75], v[120:123], a[248:251], v[72:75]\n" \
    "v_mfma_f32_16x16x32_bf16 v[88:91], v[124:127], a[248:251], v[88:91]\n" \
    "v_mfma_f32_16x16x32_bf16 v[76:79], v[120:123], a[252:255], v[76:79]\n" \
    "v_mfma_f32_16x16x32_bf16 v[92:95], v[124:127], a[252:255], v[92:95]\n" \
    "s_waitcnt lgkmcnt(2)\n" \
    "v_mfma_f32_16x16x32_bf16 v[64:67], v[96:99], v[192:195], v[64:67]\n" \
    "ds_read_b128 v[112:115], v21 offset:18432\n" \
    "v_mfma_f32_16x16x32_bf16 v[80:83], v[100:103], v[192:195], v[80:83]\n" \
    "ds_read_b128 v[116:119], v21 offset:38912\n" \
    "v_mfma_f32_16x16x32_bf16 v[68:71], v[96:99], v[196:199], v[68:71]\n" \
    "v_mfma_f32_16x16x32_bf16 v[84:87], v[100:103], v[196:199], v[84:87]\n" \
    "v_mfma_f32_16x16x32_bf16 v[72:75], v[96:99], v[200:203], v[72:75]\n" \
    "v_mfma_f32_16x16x32_bf16 v[88:91], v[100:103], v[200:203], v[88:91]\n" \
    "v_mfma_f32_16x16x32_bf16 v[76:79], v[96:99], v[204:207], v[76:79]\n" \
    "v_mfma_f32_16x16x32_bf16 v[92:95], v[100:103], v[204:207], v[92:95]\n" \
    "s_waitcnt lgkmcnt(2)\n" \
    "v_mfma_f32_16x16x32_bf16 v[64:67], v[104:107], v[208:211], v[64:67]\n" \
    "ds_read_b128 v[120:123], v21 offset:19456\n" \
    "v_mfma_f32_16x16x32_bf16 v[80:83], v[108:111], v[208:211], v[80:83]\n" \
    "ds_read_b128 v[124:127], v21 offset:39936\n" \
    "v_mfma_f32_16x16x32_bf16 v[68:71], v[104:107], v[212:215], v[68:71]\n" \
    "v_mfma_f32_16x16x32_bf16 v[84:87], v[108:111], v[212:215], v[84:87]\n" \
    "v_mfma_f32_16x16x32_bf16 v[72:75], v[104:107], v[216:219], v[72:75]\n" \
    "v_mfma_f32_16x16x32_bf16 v[88:91], v[108:111], v[216:219], v[88:91]\n" \
    "v_mfma_f32_16x16x32_bf16 v[76:79], v[104:107], v[220:223], v[76:79]\n" \
    "v_mfma_f32_16x16x32_bf16 v[92:95], v[108:111], v[220:223], v[92:95]\n" \
    "s_add_u32 s75, s72, s54\n" \
    "s_add_u32 s72, s72, 40960\n" \
    "s_cmp_eq_u32 s72, 81920\n" \
    "s_cselect_b32 s72, 114688, s72\n" \
    "s_cmp_eq_u32 s72, 155648\n" \
    "s_cselect_b32 s72, 0, s72\n" \
    "v_add_u32 v21, s72, v16\n" \
    "s_waitcnt lgkmcnt(0)\n" \
    "s_barrier\n" \
    "v_mfma_f32_16x16x32_bf16 v[64:67], v[112:115], v[224:227], v[64:67]\n" \
    "ds_read_b128 v[96:99], v21 offset:0\n" \
    "v_mfma_f32_16x16x32_bf16 v[80:83], v[116:119], v[224:227], v[80:83]\n" \
    "ds_read_b128 v[100:103], v21 offset:20480\n" \
    "v_mfma_f32_16x16x32_bf16 v[68:71], v[112:115], v[228:231], v[68:71]\n" \
    "v_mfma_f32_16x16x32_bf16 v[84:87], v[116:119], v[228:231], v[84:87]\n" \
    "v_mfma_f32_16x16x32_bf16 v[72:75], v[112:115], v[232:235], v[72:75]\n" \
    "v_mfma_f32_16x16x32_bf16 v[88:91], v[116:119], v[232:235], v[88:91]\n" \
    "v_mfma_f32_16x16x32_bf16 v[76:79], v[112:115], v[236:239], v[76:79]\n" \
    "v_mfma_f32_16x16x32_bf16 v[92:95], v[116:119], v[236:239], v[92:95]\n" \
    "v_mfma_f32_16x16x32_bf16 v[64:67], v[120:123], v[240:243], v[64:67]\n" \
    "ds_read_b128 v[104:107], v21 offset:1024\n" \
    "v_mfma_f32_16x16x32_bf16 v[80:83], v[124:127], v[240:243], v[80:83]\n" \
    "ds_read_b128 v[108:111], v21 offset:21504\n" \
    "v_mfma_f32_16x16x32_bf16 v[68:71], v[120:123], v[244:247], v[68:71]\n" \
    "v_mfma_f32_16x16x32_bf16 v[84:87], v[124:127], v[244:247], v[84:87]\n" \
    "v_mfma_f32_16x16x32_bf16 v[72:75], v[120:123], v[248:251], v[72:75]\n" \
    "v_mfma_f32_16x16x32_bf16 v[88:91], v[124:127], v[248:251], v[88:91]\n" \
    "v_mfma_f32_16x16x32_bf16 v[76:79], v[120:123], v[252:255], v[76:79]\n" \
    "v_mfma_f32_16x16x32_bf16 v[92:95], v[124:127], v[252:255], v[92:95]\n" \
    "s_branch P1_loop_%=\n" \
    "P1_fin_%=:\n" \
    "s_mul_i32 s68, s70, 24576\n" \
    "s_add_u32 s74, s68, s53\n" \
    "s_add_u32 s70, s70, 1\n" \
    "s_add_u32 s68, s70, 2\n" \
    "s_min_u32 s68, s68, 63\n" \
    "s_mul_i32 s68, s68, 40960\n" \
    "s_add_u32 s40, s68, s54\n" \
    "s_add_u32 s40, s40, 2097152\n" \
    "s_add_u32 s41, s40, 4096\n" \
    "s_add_u32 s42, s40, 8192\n" \
    "s_mov_b32 s43, s75\n" \
    "s_nop 7\n" \
    "v_cvt_pk_bf16_f32 v160, v128, v129\n" \
    "v_cvt_pk_bf16_f32 v161, v130, v131\n" \
    "v_cvt_pk_bf16_f32 v162, v144, v145\n" \
    "v_cvt_pk_bf16_f32 v163, v146, v147\n" \
    "buffer_store_dwordx4 v[160:163], v18, s[36:39], s74 offen offset:0 sc1\n" \
    "v_cvt_pk_bf16_f32 v164, v132, v133\n" \
    "v_cvt_pk_bf16_f32 v165, v134, v135\n" \
    "v_cvt_pk_bf16_f32 v166, v148, v149\n" \
    "v_cvt_pk_bf16_f32 v167, v150, v151\n" \
    "buffer_store_dwordx4 v[164:167], v18, s[36:39], s74 offen offset:1024 sc1\n" \
    "v_cvt_pk_bf16_f32 v168, v136, v137\n" \
    "v_cvt_pk_bf16_f32 v169, v138, v139\n" \
    "v_cvt_pk_bf16_f32 v170, v152, v153\n" \
    "v_cvt_pk_bf16_f32 v171, v154, v155\n" \
    "buffer_store_dwordx4 v[168:171], v18, s[36:39], s74 offen offset:2048 sc1\n" \
    "v_cvt_pk_bf16_f32 v172, v140, v141\n" \
    "v_cvt_pk_bf16_f32 v173, v142, v143\n" \
    "v_cvt_pk_bf16_f32 v174, v156, v157\n" \
    "v_cvt_pk_bf16_f32 v175, v158, v159\n" \
    "buffer_store_dwordx4 v[172:175], v18, s[36:39], s74 offen offset:3072 sc1\n" \
    "v_mov_b32 v22, s63\n" \
    "v_mov_b32 v23, 64\n" \
    "s_waitcnt vmcnt(0)\n" \
    "buffer_store_dwordx2 v[22:23], v17, s[36:39], 0 offen sc1\n" \
    "s_endpgm\n" \
    "P1_ln_no_%=:\n" \
    "s_add_u32 s82, s82, 1\n" \
    "s_and_b32 s85, s82, 63\n" \
    "s_cmp_lg_u32 s85, 0\n" \
    "s_cbranch_scc1 P1_g0_ok_%=\n" \
    "s_mov_b32 s85, 1048832\n" \
    "buffer_load_dword v22, off, s[36:39], s85 sc1\n" \
    "s_waitcnt vmcnt(0)\n" \
    "v_readfirstlane_b32 s85, v22\n" \
    "s_cmp_eq_u32 s85, s63\n" \
    "s_cbranch_scc1 P1_end_%=\n" \
    "s_memrealtime s[80:81]\n" \
    "s_waitcnt lgkmcnt(0)\n" \
    "s_sub_u32 s85, s80, s83\n" \
    "s_cmp_gt_u32 s85, 3000000\n" \
    "s_cbranch_scc1 P1_giveup_%=\n" \
    "P1_g0_ok_%=:\n" \
    "s_sleep 1\n" \
    "s_branch P1_ln_%=\n" \
    "P1_la_no_%=:\n" \
    "s_add_u32 s82, s82, 1\n" \
    "s_and_b32 s85, s82, 63\n" \
    "s_cmp_lg_u32 s85, 0\n" \
    "s_cbranch_scc1 P1_g1_ok_%=\n" \
    "s_mov_b32 s85, 1048832\n" \
    "buffer_load_dword v22, off, s[36:39], s85 sc1\n" \
    "s_waitcnt vmcnt(0)\n" \
    "v_readfirstlane_b32 s85, v22\n" \
    "s_cmp_eq_u32 s85, s63\n" \
    "s_cbranch_scc1 P1_end_%=\n" \
    "s_memrealtime s[80:81]\n" \
    "s_waitcnt lgkmcnt(0)\n" \
    "s_sub_u32 s85, s80, s83\n" \
    "s_cmp_gt_u32 s85, 3000000\n" \
    "s_cbranch_scc1 P1_giveup_%=\n" \
    "P1_g1_ok_%=:\n" \
    "s_sleep 1\n" \
    "s_branch P1_la_%=\n" \
    "P1_giveup_%=:\n" \
    "s_mov_b32 s85, 1048832\n" \
    "v_mov_b32 v22, s63\n" \
    "buffer_store_dword v22, off, s[36:39], s85 sc1\n" \
    "P1_end_%=:\n" \
    "s_endpgm\n"

#define PROJ2_ASM \
    "s_nop 4\n" \
    "s_mov_b32 s68, 0\n" \
    "s_add_u32 m0, s52, 0\n" \
    "s_nop 0\n" \
    "buffer_load_dwordx4 v0, s[40:43], s68 offen lds\n" \
    "s_add_u32 m0, s52, 1024\n" \
    "s_nop 0\n" \
    "buffer_load_dwordx4 v1, s[40:43], s68 offen lds\n" \
    "s_add_u32 m0, s52, 2048\n" \
    "s_nop 0\n" \
    "buffer_load_dwordx4 v0, s[44:47], s68 offen lds\n" \
    "s_add_u32 m0, s52, 3072\n" \
    "s_nop 0\n" \
    "buffer_load_dwordx4 v1, s[44:47], s68 offen lds\n" \
    "s_mov_b32 s68, 110592\n" \
    "s_add_u32 m0, s52, 4096\n" \
    "s_nop 0\n" \
    "buffer_load_dwordx4 v0, s[40:43], s68 offen lds\n" \
    "s_add_u32 m0, s52, 5120\n" \
    "s_nop 0\n" \
    "buffer_load_dwordx4 v1, s[40:43], s68 offen lds\n" \
    "s_add_u32 m0, s52, 6144\n" \
    "s_nop 0\n" \
    "buffer_load_dwordx4 v0, s[44:47], s68 offen lds\n" \
    "s_add_u32 m0, s52, 7168\n" \
    "s_nop 0\n" \
    "buffer_load_dwordx4 v1, s[44:47], s68 offen lds\n" \
    "s_mov_b32 s68, 221184\n" \
    "s_add_u32 m0, s52, 8192\n" \
    "s_nop 0\n" \
    "buffer_load_dwordx4 v0, s[40:43], s68 offen lds\n" \
    "s_add_u32 m0, s52, 9216\n" \
    "s_nop 0\n" \
    "buffer_load_dwordx4 v1, s[40:43], s68 offen lds\n" \
    "s_add_u32 m0, s52, 10240\n" \
    "s_nop 0\n" \
    "buffer_load_dwordx4 v0, s[44:47], s68 offen lds\n" \
    "s_add_u32 m0, s52, 11264\n" \
    "s_nop 0\n" \
    "buffer_load_dwordx4 v1, s[44:47], s68 offen lds\n" \
    "s_mov_b32 s68, 331776\n" \
    "s_add_u32 m0, s52, 12288\n" \
    "s_nop 0\n" \
    "buffer_load_dwordx4 v0, s[40:43], s68 offen lds\n" \
    "s_add_u32 m0, s52, 13312\n" \
    "s_nop 0\n" \
    "buffer_load_dwordx4 v1, s[40:43], s68 offen lds\n" \
    "s_add_u32 m0, s52, 14336\n" \
    "s_nop 0\n" \
    "buffer_load_dwordx4 v0, s[44:47], s68 offen lds\n" \
    "s_add_u32 m0, s52, 15360\n" \
    "s_nop 0\n" \
    "buffer_load_dwordx4 v1, s[44:47], s68 offen lds\n" \
    "s_mov_b32 s68, 442368\n" \
    "s_add_u32 m0, s52, 16384\n" \
    "s_nop 0\n" \
    "buffer_load_dwordx4 v0, s[40:43], s68 offen lds\n" \
    "s_add_u32 m0, s52, 17408\n" \
    "s_nop 0\n" \
    "buffer_load_dwordx4 v1, s[40:43], s68 offen lds\n" \
    "s_add_u32 m0, s52, 18432\n" \
    "s_nop 0\n" \
    "buffer_load_dwordx4 v0, s[44:47], s68 offen lds\n" \
    "s_add_u32 m0, s52, 19456\n" \
    "s_nop 0\n" \
    "buffer_load_dwordx4 v1, s[44:47], s68 offen lds\n" \
    "s_mov_b32 s68, 552960\n" \
    "s_add_u32 m0, s52, 20480\n" \
    "s_nop 0\n" \
    "buffer_load_dwordx4 v0, s[40:43], s68 offen lds\n" \
    "s_add_u32 m0, s52, 21504\n" \
    "s_nop 0\n" \
    "buffer_load_dwordx4 v1, s[40:43], s68 offen lds\n" \
    "s_add_u32 m0, s52, 22528\n" \
    "s_nop 0\n" \
    "buffer_load_dwordx4 v0, s[44:47], s68 offen lds\n" \
    "s_add_u32 m0, s52, 23552\n" \
    "s_nop 0\n" \
    "buffer_load_dwordx4 v1, s[44:47], s68 offen lds\n" \
    "s_mov_b32 s68, 663552\n" \
    "s_add_u32 m0, s52, 24576\n" \
    "s_nop 0\n" \
    "buffer_load_dwordx4 v0, s[40:43], s68 offen lds\n" \
    "s_add_u32 m0, s52, 25600\n" \
    "s_nop 0\n" \
    "buffer_load_dwordx4 v1, s[40:43], s68 offen lds\n" \
    "s_add_u32 m0, s52, 26624\n" \
    "s_nop 0\n" \
    "buffer_load_dwordx4 v0, s[44:47], s68 offen lds\n" \
    "s_add_u32 m0, s52, 27648\n" \
    "s_nop 0\n" \
    "buffer_load_dwordx4 v1, s[44:47], s68 offen lds\n" \
    "s_waitcnt vmcnt(24)\n" \
    "ds_read_b64_tr_b16 a[0:1], v4 offset:0\n" \
    "ds_read_b64_tr_b16 a[2:3], v5 offset:0\n" \
    "ds_read_b64_tr_b16 a[4:5], v6 offset:0\n" \
    "ds_read_b64_tr_b16 a[6:7], v7 offset:0\n" \
    "ds_read_b64_tr_b16 a[8:9], v4 offset:2048\n" \
    "ds_read_b64_tr_b16 a[10:11], v5 offset:2048\n" \
    "ds_read_b64_tr_b16 a[12:13], v6 offset:2048\n" \
    "ds_read_b64_tr_b16 a[14:15], v7 offset:2048\n" \
    "s_waitcnt lgkmcnt(0)\n" \
    "s_mov_b32 s68, 774144\n" \
    "s_add_u32 m0, s52, 0\n" \
    "s_nop 0\n" \
    "buffer_load_dwordx4 v0, s[40:43], s68 offen lds\n" \
    "s_add_u32 m0, s52, 1024\n" \
    "s_nop 0\n" \
    "buffer_load_dwordx4 v1, s[40:43], s68 offen lds\n" \
    "s_add_u32 m0, s52, 2048\n" \
    "s_nop 0\n" \
    "buffer_load_dwordx4 v0, s[44:47], s68 offen lds\n" \
    "s_add_u32 m0, s52, 3072\n" \
    "s_nop 0\n" \
    "buffer_load_dwordx4 v1, s[44:47], s68 offen lds\n" \
    "s_waitcnt vmcnt(24)\n" \
    "ds_read_b64_tr_b16 a[16:17], v4 offset:4096\n" \
    "ds_read_b64_tr_b16 a[18:19], v5 offset:4096\n" \
    "ds_read_b64_tr_b16 a[20:21], v6 offset:4096\n" \
    "ds_read_b64_tr_b16 a[22:23], v7 offset:4096\n" \
    "ds_read_b64_tr_b16 a[24:25], v4 offset:6144\n" \
    "ds_read_b64_tr_b16 a[26:27], v5 offset:6144\n" \
    "ds_read_b64_tr_b16 a[28:29], v6 offset:6144\n" \
    "ds_read_b64_tr_b16 a[30:31], v7 offset:6144\n" \
    "s_waitcnt lgkmcnt(0)\n" \
    "s_mov_b32 s68, 884736\n" \
    "s_add_u32 m0, s52, 4096\n" \
    "s_nop 0\n" \
    "buffer_load_dwordx4 v0, s[40:43], s68 offen lds\n" \
    "s_add_u32 m0, s52, 5120\n" \
    "s_nop 0\n" \
    "buffer_load_dwordx4 v1, s[40:43], s68 offen lds\n" \
    "s_add_u32 m0, s52, 6144\n" \
    "s_nop 0\n" \
    "buffer_load_dwordx4 v0, s[44:47], s68 offen lds\n" \
    "s_add_u32 m0, s52, 7168\n" \
    "s_nop 0\n" \
    "buffer_load_dwordx4 v1, s[44:47], s68 offen lds\n" \
    "s_waitcnt vmcnt(24)\n" \
    "ds_read_b64_tr_b16 a[32:33], v4 offset:8192\n" \
    "ds_read_b64_tr_b16 a[34:35], v5 offset:8192\n" \
    "ds_read_b64_tr_b16 a[36:37], v6 offset:8192\n" \
    "ds_read_b64_tr_b16 a[38:39], v7 offset:8192\n" \
    "ds_read_b64_tr_b16 a[40:41], v4 offset:10240\n" \
    "ds_read_b64_tr_b16 a[42:43], v5 offset:10240\n" \
    "ds_read_b64_tr_b16 a[44:45], v6 offset:10240\n" \
    "ds_read_b64_tr_b16 a[46:47], v7 offset:10240\n" \
    "s_waitcnt lgkmcnt(0)\n" \
    "s_mov_b32 s68, 995328\n" \
    "s_add_u32 m0, s52, 8192\n" \
    "s_nop 0\n" \
    "buffer_load_dwordx4 v0, s[40:43], s68 offen lds\n" \
    "s_add_u32 m0, s52, 9216\n" \
    "s_nop 0\n" \
    "buffer_load_dwordx4 v1, s[40:43], s68 offen lds\n" \
    "s_add_u32 m0, s52, 10240\n" \
    "s_nop 0\n" \
    "buffer_load_dwordx4 v0, s[44:47], s68 offen lds\n" \
    "s_add_u32 m0, s52, 11264\n" \
    "s_nop 0\n" \
    "buffer_load_dwordx4 v1, s[44:47], s68 offen lds\n" \
    "s_waitcnt vmcnt(24)\n" \
    "ds_read_b64_tr_b16 a[48:49], v4 offset:12288\n" \
    "ds_read_b64_tr_b16 a[50:51], v5 offset:12288\n" \
    "ds_read_b64_tr_b16 a[52:53], v6 offset:12288\n" \
    "ds_read_b64_tr_b16 a[54:55], v7 offset:12288\n" \
    "ds_read_b64_tr_b16 a[56:57], v4 offset:14336\n" \
    "ds_read_b64_tr_b16 a[58:59], v5 offset:14336\n" \
    "ds_read_b64_tr_b16 a[60:61], v6 offset:14336\n" \
    "ds_read_b64_tr_b16 a[62:63], v7 offset:14336\n" \
    "s_waitcnt lgkmcnt(0)\n" \
    "s_mov_b32 s68, 1105920\n" \
    "s_add_u32 m0, s52, 12288\n" \
    "s_nop 0\n" \
    "buffer_load_dwordx4 v0, s[40:43], s68 offen lds\n" \
    "s_add_u32 m0, s52, 13312\n" \
    "s_nop 0\n" \
    "buffer_load_dwordx4 v1, s[40:43], s68 offen lds\n" \
    "s_add_u32 m0, s52, 14336\n" \
    "s_nop 0\n" \
    "buffer_load_dwordx4 v0, s[44:47], s68 offen lds\n" \
    "s_add_u32 m0, s52, 15360\n" \
    "s_nop 0\n" \
    "buffer_load_dwordx4 v1, s[44:47], s68 offen lds\n" \
    "s_waitcnt vmcnt(24)\n" \
    "ds_read_b64_tr_b16 a[64:65], v4 offset:16384\n" \
    "ds_read_b64_tr_b16 a[66:67], v5 offset:16384\n" \
    "ds_read_b64_tr_b16 a[68:69], v6 offset:16384\n" \
    "ds_read_b64_tr_b16 a[70:71], v7 offset:16384\n" \
    "ds_read_b64_tr_b16 a[72:73], v4 offset:18432\n" \
    "ds_read_b64_tr_b16 a[74:75], v5 offset:18432\n" \
    "ds_read_b64_tr_b16 a[76:77], v6 offset:18432\n" \
    "ds_read_b64_tr_b16 a[78:79], v7 offset:18432\n" \
    "s_waitcnt lgkmcnt(0)\n" \
    "s_mov_b32 s68, 1216512\n" \
    "s_add_u32 m0, s52, 16384\n" \
    "s_nop 0\n" \
    "buffer_load_dwordx4 v0, s[40:43], s68 offen lds\n" \
    "s_add_u32 m0, s52, 17408\n" \
    "s_nop 0\n" \
    "buffer_load_dwordx4 v1, s[40:43], s68 offen lds\n" \
    "s_add_u32 m0, s52, 18432\n" \
    "s_nop 0\n" \
    "buffer_load_dwordx4 v0, s[44:47], s68 offen lds\n" \
    "s_add_u32 m0, s52, 19456\n" \
    "s_nop 0\n" \
    "buffer_load_dwordx4 v1, s[44:47], s68 offen lds\n" \
    "s_waitcnt vmcnt(24)\n" \
    "ds_read_b64_tr_b16 a[80:81], v4 offset:20480\n" \
    "ds_read_b64_tr_b16 a[82:83], v5 offset:20480\n" \
    "ds_read_b64_tr_b16 a[84:85], v6 offset:20480\n" \
    "ds_read_b64_tr_b16 a[86:87], v7 offset:20480\n" \
    "ds_read_b64_tr_b16 a[88:89], v4 offset:22528\n" \
    "ds_read_b64_tr_b16 a[90:91], v5 offset:22528\n" \
    "ds_read_b64_tr_b16 a[92:93], v6 offset:22528\n" \
    "ds_read_b64_tr_b16 a[94:95], v7 offset:22528\n" \
    "s_waitcnt lgkmcnt(0)\n" \
    "s_mov_b32 s68, 1327104\n" \
    "s_add_u32 m0, s52, 20480\n" \
    "s_nop 0\n" \
    "buffer_load_dwordx4 v0, s[40:43], s68 offen lds\n" \
    "s_add_u32 m0, s52, 21504\n" \
    "s_nop 0\n" \
    "buffer_load_dwordx4 v1, s[40:43], s68 offen lds\n" \
    "s_add_u32 m0, s52, 22528\n" \
    "s_nop 0\n" \
    "buffer_load_dwordx4 v0, s[44:47], s68 offen lds\n" \
    "s_add_u32 m0, s52, 23552\n" \
    "s_nop 0\n" \
    "buffer_load_dwordx4 v1, s[44:47], s68 offen lds\n" \
    "s_waitcnt vmcnt(24)\n" \
    "ds_read_b64_tr_b16 a[96:97], v4 offset:24576\n" \
    "ds_read_b64_tr_b16 a[98:99], v5 offset:24576\n" \
    "ds_read_b64_tr_b16 a[100:101], v6 offset:24576\n" \
    "ds_read_b64_tr_b16 a[102:103], v7 offset:24576\n" \
    "ds_read_b64_tr_b16 a[104:105], v4 offset:26624\n" \
    "ds_read_b64_tr_b16 a[106:107], v5 offset:26624\n" \
    "ds_read_b64_tr_b16 a[108:109], v6 offset:26624\n" \
    "ds_read_b64_tr_b16 a[110:111], v7 offset:26624\n" \
    "s_waitcnt lgkmcnt(0)\n" \
    "s_mov_b32 s68, 1437696\n" \
    "s_add_u32 m0, s52, 24576\n" \
    "s_nop 0\n" \
    "buffer_load_dwordx4 v0, s[40:43], s68 offen lds\n" \
    "s_add_u32 m0, s52, 25600\n" \
    "s_nop 0\n" \
    "buffer_load_dwordx4 v1, s[40:43], s68 offen lds\n" \
    "s_add_u32 m0, s52, 26624\n" \
    "s_nop 0\n" \
    "buffer_load_dwordx4 v0, s[44:47], s68 offen lds\n" \
    "s_add_u32 m0, s52, 27648\n" \
    "s_nop 0\n" \
    "buffer_load_dwordx4 v1, s[44:47], s68 offen lds\n" \
    "s_waitcnt vmcnt(24)\n" \
    "ds_read_b64_tr_b16 a[112:113], v4 offset:0\n" \
    "ds_read_b64_tr_b16 a[114:115], v5 offset:0\n" \
    "ds_read_b64_tr_b16 a[116:117], v6 offset:0\n" \
    "ds_read_b64_tr_b16 a[118:119], v7 offset:0\n" \
    "ds_read_b64_tr_b16 a[120:121], v4 offset:2048\n" \
    "ds_read_b64_tr_b16 a[122:123], v5 offset:2048\n" \
    "ds_read_b64_tr_b16 a[124:125], v6 offset:2048\n" \
    "ds_read_b64_tr_b16 a[126:127], v7 offset:2048\n" \
    "s_waitcnt lgkmcnt(0)\n" \
    "s_mov_b32 s68, 1548288\n" \
    "s_add_u32 m0, s52, 0\n" \
    "s_nop 0\n" \
    "buffer_load_dwordx4 v0, s[40:43], s68 offen lds\n" \
    "s_add_u32 m0, s52, 1024\n" \
    "s_nop 0\n" \
    "buffer_load_dwordx4 v1, s[40:43], s68 offen lds\n" \
    "s_add_u32 m0, s52, 2048\n" \
    "s_nop 0\n" \
    "buffer_load_dwordx4 v0, s[44:47], s68 offen lds\n" \
    "s_add_u32 m0, s52, 3072\n" \
    "s_nop 0\n" \
    "buffer_load_dwordx4 v1, s[44:47], s68 offen lds\n" \
    "s_waitcnt vmcnt(24)\n" \
    "ds_read_b64_tr_b16 a[128:129], v4 offset:4096\n" \
    "ds_read_b64_tr_b16 a[130:131], v5 offset:4096\n" \
    "ds_read_b64_tr_b16 a[132:133], v6 offset:4096\n" \
    "ds_read_b64_tr_b16 a[134:135], v7 offset:4096\n" \
    "ds_read_b64_tr_b16 a[136:137], v4 offset:6144\n" \
    "ds_read_b64_tr_b16 a[138:139], v5 offset:6144\n" \
    "ds_read_b64_tr_b16 a[140:141], v6 offset:6144\n" \
    "ds_read_b64_tr_b16 a[142:143], v7 offset:6144\n" \
    "s_waitcnt lgkmcnt(0)\n" \
    "s_mov_b32 s68, 1658880\n" \
    "s_add_u32 m0, s52, 4096\n" \
    "s_nop 0\n" \
    "buffer_load_dwordx4 v0, s[40:43], s68 offen lds\n" \
    "s_add_u32 m0, s52, 5120\n" \
    "s_nop 0\n" \
    "buffer_load_dwordx4 v1, s[40:43], s68 offen lds\n" \
    "s_add_u32 m0, s52, 6144\n" \
    "s_nop 0\n" \
    "buffer_load_dwordx4 v0, s[44:47], s68 offen lds\n" \
    "s_add_u32 m0, s52, 7168\n" \
    "s_nop 0\n" \
    "buffer_load_dwordx4 v1, s[44:47], s68 offen lds\n" \
    "s_waitcnt vmcnt(24)\n" \
    "ds_read_b64_tr_b16 a[144:145], v4 offset:8192\n" \
    "ds_read_b64_tr_b16 a[146:147], v5 offset:8192\n" \
    "ds_read_b64_tr_b16 a[148:149], v6 offset:8192\n" \
    "ds_read_b64_tr_b16 a[150:151], v7 offset:8192\n" \
    "ds_read_b64_tr_b16 a[152:153], v4 offset:10240\n" \
    "ds_read_b64_tr_b16 a[154:155], v5 offset:10240\n" \
    "ds_read_b64_tr_b16 a[156:157], v6 offset:10240\n" \
    "ds_read_b64_tr_b16 a[158:159], v7 offset:10240\n" \
    "s_waitcnt lgkmcnt(0)\n" \
    "s_mov_b32 s68, 1769472\n" \
    "s_add_u32 m0, s52, 8192\n" \
    "s_nop 0\n" \
    "buffer_load_dwordx4 v0, s[40:43], s68 offen lds\n" \
    "s_add_u32 m0, s52, 9216\n" \
    "s_nop 0\n" \
    "buffer_load_dwordx4 v1, s[40:43], s68 offen lds\n" \
    "s_add_u32 m0, s52, 10240\n" \
    "s_nop 0\n" \
    "buffer_load_dwordx4 v0, s[44:47], s68 offen lds\n" \
    "s_add_u32 m0, s52, 11264\n" \
    "s_nop 0\n" \
    "buffer_load_dwordx4 v1, s[44:47], s68 offen lds\n" \
    "s_waitcnt vmcnt(24)\n" \
    "ds_read_b64_tr_b16 a[160:161], v4 offset:12288\n" \
    "ds_read_b64_tr_b16 a[162:163], v5 offset:12288\n" \
    "ds_read_b64_tr_b16 a[164:165], v6 offset:12288\n" \
    "ds_read_b64_tr_b16 a[166:167], v7 offset:12288\n" \
    "ds_read_b64_tr_b16 a[168:169], v4 offset:14336\n" \
    "ds_read_b64_tr_b16 a[170:171], v5 offset:14336\n" \
    "ds_read_b64_tr_b16 a[172:173], v6 offset:14336\n" \
    "ds_read_b64_tr_b16 a[174:175], v7 offset:14336\n" \
    "s_waitcnt lgkmcnt(0)\n" \
    "s_mov_b32 s68, 1880064\n" \
    "s_add_u32 m0, s52, 12288\n" \
    "s_nop 0\n" \
    "buffer_load_dwordx4 v0, s[40:43], s68 offen lds\n" \
    "s_add_u32 m0, s52, 13312\n" \
    "s_nop 0\n" \
    "buffer_load_dwordx4 v1, s[40:43], s68 offen lds\n" \
    "s_add_u32 m0, s52, 14336\n" \
    "s_nop 0\n" \
    "buffer_load_dwordx4 v0, s[44:47], s68 offen lds\n" \
    "s_add_u32 m0, s52, 15360\n" \
    "s_nop 0\n" \
    "buffer_load_dwordx4 v1, s[44:47], s68 offen lds\n" \
    "s_waitcnt vmcnt(24)\n" \
    "ds_read_b64_tr_b16 a[176:177], v4 offset:16384\n" \
    "ds_read_b64_tr_b16 a[178:179], v5 offset:16384\n" \
    "ds_read_b64_tr_b16 a[180:181], v6 offset:16384\n" \
    "ds_read_b64_tr_b16 a[182:183], v7 offset:16384\n" \
    "ds_read_b64_tr_b16 a[184:185], v4 offset:18432\n" \
    "ds_read_b64_tr_b16 a[186:187], v5 offset:18432\n" \
    "ds_read_b64_tr_b16 a[188:189], v6 offset:18432\n" \
    "ds_read_b64_tr_b16 a[190:191], v7 offset:18432\n" \
    "s_waitcnt lgkmcnt(0)\n" \
    "s_mov_b32 s68, 1990656\n" \
    "s_add_u32 m0, s52, 16384\n" \
    "s_nop 0\n" \
    "buffer_load_dwordx4 v0, s[40:43], s68 offen lds\n" \
    "s_add_u32 m0, s52, 17408\n" \
    "s_nop 0\n" \
    "buffer_load_dwordx4 v1, s[40:43], s68 offen lds\n" \
    "s_add_u32 m0, s52, 18432\n" \
    "s_nop 0\n" \
    "buffer_load_dwordx4 v0, s[44:47], s68 offen lds\n" \
    "s_add_u32 m0, s52, 19456\n" \
    "s_nop 0\n" \
    "buffer_load_dwordx4 v1, s[44:47], s68 offen lds\n" \
    "s_waitcnt vmcnt(24)\n" \
    "ds_read_b64_tr_b16 a[192:193], v4 offset:20480\n" \
    "ds_read_b64_tr_b16 a[194:195], v5 offset:20480\n" \
    "ds_read_b64_tr_b16 a[196:197], v6 offset:20480\n" \
    "ds_read_b64_tr_b16 a[198:199], v7 offset:20480\n" \
    "ds_read_b64_tr_b16 a[200:201], v4 offset:22528\n" \
    "ds_read_b64_tr_b16 a[202:203], v5 offset:22528\n" \
    "ds_read_b64_tr_b16 a[204:205], v6 offset:22528\n" \
    "ds_read_b64_tr_b16 a[206:207], v7 offset:22528\n" \
    "s_waitcnt lgkmcnt(0)\n" \
    "s_mov_b32 s68, 2101248\n" \
    "s_add_u32 m0, s52, 20480\n" \
    "s_nop 0\n" \
    "buffer_load_dwordx4 v0, s[40:43], s68 offen lds\n" \
    "s_add_u32 m0, s52, 21504\n" \
    "s_nop 0\n" \
    "buffer_load_dwordx4 v1, s[40:43], s68 offen lds\n" \
    "s_add_u32 m0, s52, 22528\n" \
    "s_nop 0\n" \
    "buffer_load_dwordx4 v0, s[44:47], s68 offen lds\n" \
    "s_add_u32 m0, s52, 23552\n" \
    "s_nop 0\n" \
    "buffer_load_dwordx4 v1, s[44:47], s68 offen lds\n" \
    "s_memrealtime s[80:81]\n" \
    "s_waitcnt lgkmcnt(0)\n" \
    "s_mov_b32 s83, s80\n" \
    "s_mov_b32 s82, 0\n" \
    "P2_ln_%=:\n" \
    "buffer_load_dwordx2 v[22:23], v19, s[36:39], 0 offen sc1\n" \
    "s_waitcnt vmcnt(0)\n" \
    "v_cmp_eq_u32 vcc, s63, v22\n" \
    "v_cmp_eq_u32 s[76:77], s63, v23\n" \
    "s_and_b64 vcc, vcc, s[76:77]\n" \
    "s_and_b64 vcc, vcc, 7\n" \
    "s_cmp_eq_u64 vcc, 7\n" \
    "s_cbranch_scc0 P2_ln_no_%=\n" \
    "s_add_u32 s40, s54, 2097152\n" \
    "s_add_u32 s41, s40, 4096\n" \
    "s_add_u32 s42, s40, 8192\n" \
    "s_add_u32 s43, s54, 114688\n" \
    "s_add_u32 m0, s43, 0\n" \
    "s_nop 0\n" \
    "buffer_load_dwordx4 v16, s[36:39], s40 offen offset:0 sc1 lds\n" \
    "s_add_u32 m0, s43, 0\n" \
    "s_nop 0\n" \
    "buffer_load_dwordx4 v16, s[36:39], s40 offen offset:1024 sc1 lds\n" \
    "s_add_u32 m0, s43, 0\n" \
    "s_nop 0\n" \
    "buffer_load_dwordx4 v16, s[36:39], s40 offen offset:2048 sc1 lds\n" \
    "s_add_u32 m0, s43, 0\n" \
    "s_nop 0\n" \
    "buffer_load_dwordx4 v16, s[36:39], s40 offen offset:3072 sc1 lds\n" \
    "s_add_u32 m0, s43, 4096\n" \
    "s_nop 0\n" \
    "buffer_load_dwordx4 v16, s[36:39], s41 offen offset:0 sc1 lds\n" \
    "s_add_u32 m0, s43, 4096\n" \
    "s_nop 0\n" \
    "buffer_load_dwordx4 v16, s[36:39], s41 offen offset:1024 sc1 lds\n" \
    "s_add_u32 m0, s43, 4096\n" \
    "s_nop 0\n" \
    "buffer_load_dwordx4 v16, s[36:39], s41 offen offset:2048 sc1 lds\n" \
    "s_add_u32 m0, s43, 4096\n" \
    "s_nop 0\n" \
    "buffer_load_dwordx4 v16, s[36:39], s41 offen offset:3072 sc1 lds\n" \
    "s_add_u32 m0, s43, 8192\n" \
    "s_nop 0\n" \
    "buffer_load_dwordx4 v16, s[36:39], s42 offen offset:0 sc1 lds\n" \
    "s_add_u32 m0, s43, 8192\n" \
    "s_nop 0\n" \
    "buffer_load_dwordx4 v16, s[36:39], s42 offen offset:1024 sc1 lds\n" \
    "v_add_u32 v21, 114688, v16\n" \
    "ds_read_b64_tr_b16 a[208:209], v4 offset:24576\n" \
    "ds_read_b64_tr_b16 a[210:211], v5 offset:24576\n" \
    "ds_read_b64_tr_b16 a[212:213], v6 offset:24576\n" \
    "ds_read_b64_tr_b16 a[214:215], v7 offset:24576\n" \
    "ds_read_b64_tr_b16 a[216:217], v4 offset:26624\n" \
    "ds_read_b64_tr_b16 a[218:219], v5 offset:26624\n" \
    "ds_read_b64_tr_b16 a[220:221], v6 offset:26624\n" \
    "ds_read_b64_tr_b16 a[222:223], v7 offset:26624\n" \
    "s_waitcnt lgkmcnt(0)\n" \
    "ds_read_b64_tr_b16 a[224:225], v4 offset:0\n" \
    "ds_read_b64_tr_b16 a[226:227], v5 offset:0\n" \
    "ds_read_b64_tr_b16 a[228:229], v6 offset:0\n" \
    "ds_read_b64_tr_b16 a[230:231], v7 offset:0\n" \
    "ds_read_b64_tr_b16 a[232:233], v4 offset:2048\n" \
    "ds_read_b64_tr_b16 a[234:235], v5 offset:2048\n" \
    "ds_read_b64_tr_b16 a[236:237], v6 offset:2048\n" \
    "ds_read_b64_tr_b16 a[238:239], v7 offset:2048\n" \
    "s_waitcnt lgkmcnt(0)\n" \
    "ds_read_b64_tr_b16 a[240:241], v4 offset:4096\n" \
    "ds_read_b64_tr_b16 a[242:243], v5 offset:4096\n" \
    "ds_read_b64_tr_b16 a[244:245], v6 offset:4096\n" \
    "ds_read_b64_tr_b16 a[246:247], v7 offset:4096\n" \
    "ds_read_b64_tr_b16 a[248:249], v4 offset:6144\n" \
    "ds_read_b64_tr_b16 a[250:251], v5 offset:6144\n" \
    "ds_read_b64_tr_b16 a[252:253], v6 offset:6144\n" \
    "ds_read_b64_tr_b16 a[254:255], v7 offset:6144\n" \
    "s_waitcnt lgkmcnt(0)\n" \
    "ds_read_b64_tr_b16 v[192:193], v4 offset:8192\n" \
    "ds_read_b64_tr_b16 v[194:195], v5 offset:8192\n" \
    "ds_read_b64_tr_b16 v[196:197], v6 offset:8192\n" \
    "ds_read_b64_tr_b16 v[198:199], v7 offset:8192\n" \
    "ds_read_b64_tr_b16 v[200:201], v4 offset:10240\n" \
    "ds_read_b64_tr_b16 v[202:203], v5 offset:10240\n" \
    "ds_read_b64_tr_b16 v[204:205], v6 offset:10240\n" \
    "ds_read_b64_tr_b16 v[206:207], v7 offset:10240\n" \
    "s_waitcnt lgkmcnt(0)\n" \
    "ds_read_b64_tr_b16 v[208:209], v4 offset:12288\n" \
    "ds_read_b64_tr_b16 v[210:211], v5 offset:12288\n" \
    "ds_read_b64_tr_b16 v[212:213], v6 offset:12288\n" \
    "ds_read_b64_tr_b16 v[214:215], v7 offset:12288\n" \
    "ds_read_b64_tr_b16 v[216:217], v4 offset:14336\n" \
    "ds_read_b64_tr_b16 v[218:219], v5 offset:14336\n" \
    "ds_read_b64_tr_b16 v[220:221], v6 offset:14336\n" \
    "ds_read_b64_tr_b16 v[222:223], v7 offset:14336\n" \
    "s_waitcnt lgkmcnt(0)\n" \
    "ds_read_b64_tr_b16 v[224:225], v4 offset:16384\n" \
    "ds_read_b64_tr_b16 v[226:227], v5 offset:16384\n" \
    "ds_read_b64_tr_b16 v[228:229], v6 offset:16384\n" \
    "ds_read_b64_tr_b16 v[230:231], v7 offset:16384\n" \
    "ds_read_b64_tr_b16 v[232:233], v4 offset:18432\n" \
    "ds_read_b64_tr_b16 v[234:235], v5 offset:18432\n" \
    "ds_read_b64_tr_b16 v[236:237], v6 offset:18432\n" \
    "ds_read_b64_tr_b16 v[238:239], v7 offset:18432\n" \
    "s_waitcnt lgkmcnt(0)\n" \
    "ds_read_b64_tr_b16 v[240:241], v4 offset:20480\n" \
    "ds_read_b64_tr_b16 v[242:243], v5 offset:20480\n" \
    "ds_read_b64_tr_b16 v[244:245], v6 offset:20480\n" \
    "ds_read_b64_tr_b16 v[246:247], v7 offset:20480\n" \
    "ds_read_b64_tr_b16 v[248:249], v4 offset:22528\n" \
    "ds_read_b64_tr_b16 v[250:251], v5 offset:22528\n" \
    "ds_read_b64_tr_b16 v[252:253], v6 offset:22528\n" \
    "ds_read_b64_tr_b16 v[254:255], v7 offset:22528\n" \
    "s_waitcnt lgkmcnt(0)\n" \
    "s_mov_b32 s70, 0\n" \
    "s_mov_b32 s72, 114688\n" \
    "s_waitcnt vmcnt(0)\n" \
    "s_barrier\n" \
    "ds_read_b128 v[96:99], v21 offset:0\n" \
    "ds_read_b128 v[100:103], v21 offset:20480\n" \
    "ds_read_b128 v[104:107], v21 offset:1024\n" \
    "ds_read_b128 v[108:111], v21 offset:21504\n" \
    "s_add_u32 s40, s54, 2138112\n" \
    "s_add_u32 s41, s40, 4096\n" \
    "s_add_u32 s42, s40, 8192\n" \
    "s_add_u32 s43, s54, 0\n" \
    "s_add_u32 m0, s43, 0\n" \
    "s_nop 0\n" \
    "buffer_load_dwordx4 v16, s[36:39], s40 offen offset:0 sc1 lds\n" \
    "s_add_u32 m0, s43, 0\n" \
    "s_nop 0\n" \
    "buffer_load_dwordx4 v16, s[36:39], s40 offen offset:1024 sc1 lds\n" \
    "s_add_u32 m0, s43, 0\n" \
    "s_nop 0\n" \
    "buffer_load_dwordx4 v16, s[36:39], s40 offen offset:2048 sc1 lds\n" \
    "s_add_u32 m0, s43, 0\n" \
    "s_nop 0\n" \
    "buffer_load_dwordx4 v16, s[36:39], s40 offen offset:3072 sc1 lds\n" \
    "s_add_u32 m0, s43, 4096\n" \
    "s_nop 0\n" \
    "buffer_load_dwordx4 v16, s[36:39], s41 offen offset:0 sc1 lds\n" \
    "s_add_u32 m0, s43, 4096\n" \
    "s_nop 0\n" \
    "buffer_load_dwordx4 v16, s[36:39], s41 offen offset:1024 sc1 lds\n" \
    "s_add_u32 m0, s43, 4096\n" \
    "s_nop 0\n" \
    "buffer_load_dwordx4 v16, s[36:39], s41 offen offset:2048 sc1 lds\n" \
    "s_add_u32 m0, s43, 4096\n" \
    "s_nop 0\n" \
    "buffer_load_dwordx4 v16, s[36:39], s41 offen offset:3072 sc1 lds\n" \
    "s_add_u32 m0, s43, 8192\n" \
    "s_nop 0\n" \
    "buffer_load_dwordx4 v16, s[36:39], s42 offen offset:0 sc1 lds\n" \
    "s_add_u32 m0, s43, 8192\n" \
    "s_nop 0\n" \
    "buffer_load_dwordx4 v16, s[36:39], s42 offen offset:1024 sc1 lds\n" \
    "s_nop 0\n" \
    "s_add_u32 s40, s54, 2179072\n" \
    "s_add_u32 s41, s40, 4096\n" \
    "s_add_u32 s42, s40, 8192\n" \
    "s_add_u32 s43, s54, 40960\n" \
    "s_waitcnt lgkmcnt(2)\n" \
    "v_mfma_f32_16x16x32_bf16 v[64:67], a[0:3], v[96:99], 0\n" \
    "ds_read_b128 v[112:115], v21 offset:2048\n" \
    "v_mfma_f32_16x16x32_bf16 v[80:83], a[0:3], v[100:103], 0\n" \
    "ds_read_b128 v[116:119], v21 offset:22528\n" \
    "v_mfma_f32_16x16x32_bf16 v[68:71], a[4:7], v[96:99], 0\n" \
    "s_add_u32 m0, s43, 0\n" \
    "v_mfma_f32_16x16x32_bf16 v[84:87], a[4:7], v[100:103], 0\n" \
    "buffer_load_dwordx4 v16, s[36:39], s40 offen offset:0 sc1 lds\n" \
    "v_mfma_f32_16x16x32_bf16 v[72:75], a[8:11], v[96:99], 0\n" \
    "v_mfma_f32_16x16x32_bf16 v[88:91], a[8:11], v[100:103], 0\n" \
    "v_mfma_f32_16x16x32_bf16 v[76:79], a[12:15], v[96:99], 0\n" \
    "v_mfma_f32_16x16x32_bf16 v[92:95], a[12:15], v[100:103], 0\n" \
    "s_waitcnt lgkmcnt(2)\n" \
    "v_mfma_f32_16x16x32_bf16 v[64:67], a[16:19], v[104:107], v[64:67]\n" \
    "ds_read_b128 v[120:123], v21 offset:3072\n" \
    "v_mfma_f32_16x16x32_bf16 v[80:83], a[16:19], v[108:111], v[80:83]\n" \
    "ds_read_b128 v[124:127], v21 offset:23552\n" \
    "v_mfma_f32_16x16x32_bf16 v[68:71], a[20:23], v[104:107], v[68:71]\n" \
    "s_add_u32 m0, s43, 0\n" \
    "v_mfma_f32_16x16x32_bf16 v[84:87], a[20:23], v[108:111], v[84:87]\n" \
    "buffer_load_dwordx4 v16, s[36:39], s40 offen offset:1024 sc1 lds\n" \
    "v_mfma_f32_16x16x32_bf16 v[72:75], a[24:27], v[104:107], v[72:75]\n" \
    "v_mfma_f32_16x16x32_bf16 v[88:91], a[24:27], v[108:111], v[88:91]\n" \
    "v_mfma_f32_16x16x32_bf16 v[76:79], a[28:31], v[104:107], v[76:79]\n" \
    "v_mfma_f32_16x16x32_bf16 v[92:95], a[28:31], v[108:111], v[92:95]\n" \
    "s_waitcnt lgkmcnt(2)\n" \
    "v_mfma_f32_16x16x32_bf16 v[64:67], a[32:35], v[112:115], v[64:67]\n" \
    "ds_read_b128 v[96:99], v21 offset:4096\n" \
    "v_mfma_f32_16x16x32_bf16 v[80:83], a[32:35], v[116:119], v[80:83]\n" \
    "ds_read_b128 v[100:103], v21 offset:24576\n" \
    "v_mfma_f32_16x16x32_bf16 v[68:71], a[36:39], v[112:115], v[68:71]\n" \
    "s_add_u32 m0, s43, 0\n" \
    "v_mfma_f32_16x16x32_bf16 v[84:87], a[36:39], v[116:119], v[84:87]\n" \
    "buffer_load_dwordx4 v16, s[36:39], s40 offen offset:2048 sc1 lds\n" \
    "v_mfma_f32_16x16x32_bf16 v[72:75], a[40:43], v[112:115], v[72:75]\n" \
    "v_mfma_f32_16x16x32_bf16 v[88:91], a[40:43], v[116:119], v[88:91]\n" \
    "v_mfma_f32_16x16x32_bf16 v[76:79], a[44:47], v[112:115], v[76:79]\n" \
    "v_mfma_f32_16x16x32_bf16 v[92:95], a[44:47], v[116:119], v[92:95]\n" \
    "s_waitcnt lgkmcnt(2)\n" \
    "v_mfma_f32_16x16x32_bf16 v[64:67], a[48:51], v[120:123], v[64:67]\n" \
    "ds_read_b128 v[104:107], v21 offset:5120\n" \
    "v_mfma_f32_16x16x32_bf16 v[80:83], a[48:51], v[124:127], v[80:83]\n" \
    "ds_read_b128 v[108:111], v21 offset:25600\n" \
    "v_mfma_f32_16x16x32_bf16 v[68:71], a[52:55], v[120:123], v[68:71]\n" \
    "s_add_u32 m0, s43, 0\n" \
    "v_mfma_f32_16x16x32_bf16 v[84:87], a[52:55], v[124:127], v[84:87]\n" \
    "buffer_load_dwordx4 v16, s[36:39], s40 offen offset:3072 sc1 lds\n" \
    "v_mfma_f32_16x16x32_bf16 v[72:75], a[56:59], v[120:123], v[72:75]\n" \
    "v_mfma_f32_16x16x32_bf16 v[88:91], a[56:59], v[124:127], v[88:91]\n" \
    "v_mfma_f32_16x16x32_bf16 v[76:79], a[60:63], v[120:123], v[76:79]\n" \
    "v_mfma_f32_16x16x32_bf16 v[92:95], a[60:63], v[124:127], v[92:95]\n" \
    "s_waitcnt lgkmcnt(2)\n" \
    "v_mfma_f32_16x16x32_bf16 v[64:67], a[64:67], v[96:99], v[64:67]\n" \
    "ds_read_b128 v[112:115], v21 offset:6144\n" \
    "v_mfma_f32_16x16x32_bf16 v[80:83], a[64:67], v[100:103], v[80:83]\n" \
    "ds_read_b128 v[116:119], v21 offset:26624\n" \
    "v_mfma_f32_16x16x32_bf16 v[68:71], a[68:71], v[96:99], v[68:71]\n" \
    "s_add_u32 m0, s43, 4096\n" \
    "v_mfma_f32_16x16x32_bf16 v[84:87], a[68:71], v[100:103], v[84:87]\n" \
    "buffer_load_dwordx4 v16, s[36:39], s41 offen offset:0 sc1 lds\n" \
    "v_mfma_f32_16x16x32_bf16 v[72:75], a[72:75], v[96:99], v[72:75]\n" \
    "v_mfma_f32_16x16x32_bf16 v[88:91], a[72:75], v[100:103], v[88:91]\n" \
    "v_mfma_f32_16x16x32_bf16 v[76:79], a[76:79], v[96:99], v[76:79]\n" \
    "v_mfma_f32_16x16x32_bf16 v[92:95], a[76:79], v[100:103], v[92:95]\n" \
    "s_waitcnt lgkmcnt(2)\n" \
    "v_mfma_f32_16x16x32_bf16 v[64:67], a[80:83], v[104:107], v[64:67]\n" \
    "ds_read_b128 v[120:123], v21 offset:7168\n" \
    "v_mfma_f32_16x16x32_bf16 v[80:83], a[80:83], v[108:111], v[80:83]\n" \
    "ds_read_b128 v[124:127], v21 offset:27648\n" \
    "v_mfma_f32_16x16x32_bf16 v[68:71], a[84:87], v[104:107], v[68:71]\n" \
    "s_add_u32 m0, s43, 4096\n" \
    "v_mfma_f32_16x16x32_bf16 v[84:87], a[84:87], v[108:111], v[84:87]\n" \
    "buffer_load_dwordx4 v16, s[36:39], s41 offen offset:1024 sc1 lds\n" \
    "v_mfma_f32_16x16x32_bf16 v[72:75], a[88:91], v[104:107], v[72:75]\n" \
    "v_mfma_f32_16x16x32_bf16 v[88:91], a[88:91], v[108:111], v[88:91]\n" \
    "v_mfma_f32_16x16x32_bf16 v[76:79], a[92:95], v[104:107], v[76:79]\n" \
    "v_mfma_f32_16x16x32_bf16 v[92:95], a[92:95], v[108:111], v[92:95]\n" \
    "s_waitcnt lgkmcnt(2)\n" \
    "v_mfma_f32_16x16x32_bf16 v[64:67], a[96:99], v[112:115], v[64:67]\n" \
    "ds_read_b128 v[96:99], v21 offset:8192\n" \
    "v_mfma_f32_16x16x32_bf16 v[80:83], a[96:99], v[116:119], v[80:83]\n" \
    "ds_read_b128 v[100:103], v21 offset:28672\n" \
    "v_mfma_f32_16x16x32_bf16 v[68:71], a[100:103], v[112:115], v[68:71]\n" \
    "s_add_u32 m0, s43, 4096\n" \
    "v_mfma_f32_16x16x32_bf16 v[84:87], a[100:103], v[116:119], v[84:87]\n" \
    "buffer_load_dwordx4 v16, s[36:39], s41 offen offset:2048 sc1 lds\n" \
    "v_mfma_f32_16x16x32_bf16 v[72:75], a[104:107], v[112:115], v[72:75]\n" \
    "v_mfma_f32_16x16x32_bf16 v[88:91], a[104:107], v[116:119], v[88:91]\n" \
    "v_mfma_f32_16x16x32_bf16 v[76:79], a[108:111], v[112:115], v[76:79]\n" \
    "v_mfma_f32_16x16x32_bf16 v[92:95], a[108:111], v[116:119], v[92:95]\n" \
    "s_waitcnt lgkmcnt(2)\n" \
    "v_mfma_f32_16x16x32_bf16 v[64:67], a[112:115], v[120:123], v[64:67]\n" \
    "ds_read_b128 v[104:107], v21 offset:9216\n" \
    "v_mfma_f32_16x16x32_bf16 v[80:83], a[112:115], v[124:127], v[80:83]\n" \
    "ds_read_b128 v[108:111], v21 offset:29696\n" \
    "v_mfma_f32_16x16x32_bf16 v[68:71], a[116:119], v[120:123], v[68:71]\n" \
    "s_add_u32 m0, s43, 4096\n" \
    "v_mfma_f32_16x16x32_bf16 v[84:87], a[116:119], v[124:127], v[84:87]\n" \
    "buffer_load_dwordx4 v16, s[36:39], s41 offen offset:3072 sc1 lds\n" \
    "v_mfma_f32_16x16x32_bf16 v[72:75], a[120:123], v[120:123], v[72:75]\n" \
    "v_mfma_f32_16x16x32_bf16 v[88:91], a[120:123], v[124:127], v[88:91]\n" \
    "v_mfma_f32_16x16x32_bf16 v[76:79], a[124:127], v[120:123], v[76:79]\n" \
    "v_mfma_f32_16x16x32_bf16 v[92:95], a[124:127], v[124:127], v[92:95]\n" \
    "s_waitcnt lgkmcnt(2)\n" \
    "v_mfma_f32_16x16x32_bf16 v[64:67], a[128:131], v[96:99], v[64:67]\n" \
    "ds_read_b128 v[112:115], v21 offset:10240\n" \
    "v_mfma_f32_16x16x32_bf16 v[80:83], a[128:131], v[100:103], v[80:83]\n" \
    "ds_read_b128 v[116:119], v21 offset:30720\n" \
    "v_mfma_f32_16x16x32_bf16 v[68:71], a[132:135], v[96:99], v[68:71]\n" \
    "s_add_u32 m0, s43, 8192\n" \
    "v_mfma_f32_16x16x32_bf16 v[84:87], a[132:135], v[100:103], v[84:87]\n" \
    "buffer_load_dwordx4 v16, s[36:39], s42 offen offset:0 sc1 lds\n" \
    "v_mfma_f32_16x16x32_bf16 v[72:75], a[136:139], v[96:99], v[72:75]\n" \
    "v_mfma_f32_16x16x32_bf16 v[88:91], a[136:139], v[100:103], v[88:91]\n" \
    "v_mfma_f32_16x16x32_bf16 v[76:79], a[140:143], v[96:99], v[76:79]\n" \
    "v_mfma_f32_16x16x32_bf16 v[92:95], a[140:143], v[100:103], v[92:95]\n" \
    "s_waitcnt lgkmcnt(2)\n" \
    "v_mfma_f32_16x16x32_bf16 v[64:67], a[144:147], v[104:107], v[64:67]\n" \
    "ds_read_b128 v[120:123], v21 offset:11264\n" \
    "v_mfma_f32_16x16x32_bf16 v[80:83], a[144:147], v[108:111], v[80:83]\n" \
    "ds_read_b128 v[124:127], v21 offset:31744\n" \
    "v_mfma_f32_16x16x32_bf16 v[68:71], a[148:151], v[104:107], v[68:71]\n" \
    "s_add_u32 m0, s43, 8192\n" \
    "v_mfma_f32_16x16x32_bf16 v[84:87], a[148:151], v[108:111], v[84:87]\n" \
    "buffer_load_dwordx4 v16, s[36:39], s42 offen offset:1024 sc1 lds\n" \
    "v_mfma_f32_16x16x32_bf16 v[72:75], a[152:155], v[104:107], v[72:75]\n" \
    "v_mfma_f32_16x16x32_bf16 v[88:91], a[152:155], v[108:111], v[88:91]\n" \
    "v_mfma_f32_16x16x32_bf16 v[76:79], a[156:159], v[104:107], v[76:79]\n" \
    "v_mfma_f32_16x16x32_bf16 v[92:95], a[156:159], v[108:111], v[92:95]\n" \
    "s_waitcnt lgkmcnt(2)\n" \
    "v_mfma_f32_16x16x32_bf16 v[64:67], a[160:163], v[112:115], v[64:67]\n" \
    "ds_read_b128 v[96:99], v21 offset:12288\n" \
    "v_mfma_f32_16x16x32_bf16 v[80:83], a[160:163], v[116:119], v[80:83]\n" \
    "ds_read_b128 v[100:103], v21 offset:32768\n" \
    "v_mfma_f32_16x16x32_bf16 v[68:71], a[164:167], v[112:115], v[68:71]\n" \
    "v_mfma_f32_16x16x32_bf16 v[84:87], a[164:167], v[116:119], v[84:87]\n" \
    "v_mfma_f32_16x16x32_bf16 v[72:75], a[168:171], v[112:115], v[72:75]\n" \
    "v_mfma_f32_16x16x32_bf16 v[88:91], a[168:171], v[116:119], v[88:91]\n" \
    "v_mfma_f32_16x16x32_bf16 v[76:79], a[172:175], v[112:115], v[76:79]\n" \
    "v_mfma_f32_16x16x32_bf16 v[92:95], a[172:175], v[116:119], v[92:95]\n" \
    "s_waitcnt lgkmcnt(2)\n" \
    "v_mfma_f32_16x16x32_bf16 v[64:67], a[176:179], v[120:123], v[64:67]\n" \
    "ds_read_b128 v[104:107], v21 offset:13312\n" \
    "v_mfma_f32_16x16x32_bf16 v[80:83], a[176:179], v[124:127], v[80:83]\n" \
    "ds_read_b128 v[108:111], v21 offset:33792\n" \
    "v_mfma_f32_16x16x32_bf16 v[68:71], a[180:183], v[120:123], v[68:71]\n" \
    "v_mfma_f32_16x16x32_bf16 v[84:87], a[180:183], v[124:127], v[84:87]\n" \
    "v_mfma_f32_16x16x32_bf16 v[72:75], a[184:187], v[120:123], v[72:75]\n" \
    "v_mfma_f32_16x16x32_bf16 v[88:91], a[184:187], v[124:127], v[88:91]\n" \
    "v_mfma_f32_16x16x32_bf16 v[76:79], a[188:191], v[120:123], v[76:79]\n" \
    "v_mfma_f32_16x16x32_bf16 v[92:95], a[188:191], v[124:127], v[92:95]\n" \
    "s_waitcnt lgkmcnt(2)\n" \
    "v_mfma_f32_16x16x32_bf16 v[64:67], a[192:195], v[96:99], v[64:67]\n" \
    "ds_read_b128 v[112:115], v21 offset:14336\n" \
    "v_mfma_f32_16x16x32_bf16 v[80:83], a[192:195], v[100:103], v[80:83]\n" \
    "ds_read_b128 v[116:119], v21 offset:34816\n" \
    "v_mfma_f32_16x16x32_bf16 v[68:71], a[196:199], v[96:99], v[68:71]\n" \
    "v_mfma_f32_16x16x32_bf16 v[84:87], a[196:199], v[100:103], v[84:87]\n" \
    "v_mfma_f32_16x16x32_bf16 v[72:75], a[200:203], v[96:99], v[72:75]\n" \
    "v_mfma_f32_16x16x32_bf16 v[88:91], a[200:203], v[100:103], v[88:91]\n" \
    "v_mfma_f32_16x16x32_bf16 v[76:79], a[204:207], v[96:99], v[76:79]\n" \
    "v_mfma_f32_16x16x32_bf16 v[92:95], a[204:207], v[100:103], v[92:95]\n" \
    "s_waitcnt lgkmcnt(2)\n" \
    "v_mfma_f32_16x16x32_bf16 v[64:67], a[208:211], v[104:107], v[64:67]\n" \
    "ds_read_b128 v[120:123], v21 offset:15360\n" \
    "v_mfma_f32_16x16x32_bf16 v[80:83], a[208:211], v[108:111], v[80:83]\n" \
    "ds_read_b128 v[124:127], v21 offset:35840\n" \
    "v_mfma_f32_16x16x32_bf16 v[68:71], a[212:215], v[104:107], v[68:71]\n" \
    "v_mfma_f32_16x16x32_bf16 v[84:87], a[212:215], v[108:111], v[84:87]\n" \
    "v_mfma_f32_16x16x32_bf16 v[72:75], a[216:219], v[104:107], v[72:75]\n" \
    "v_mfma_f32_16x16x32_bf16 v[88:91], a[216:219], v[108:111], v[88:91]\n" \
    "v_mfma_f32_16x16x32_bf16 v[76:79], a[220:223], v[104:107], v[76:79]\n" \
    "v_mfma_f32_16x16x32_bf16 v[92:95], a[220:223], v[108:111], v[92:95]\n" \
    "s_waitcnt lgkmcnt(2)\n" \
    "v_mfma_f32_16x16x32_bf16 v[64:67], a[224:227], v[112:115], v[64:67]\n" \
    "ds_read_b128 v[96:99], v21 offset:16384\n" \
    "v_mfma_f32_16x16x32_bf16 v[80:83], a[224:227], v[116:119], v[80:83]\n" \
    "ds_read_b128 v[100:103], v21 offset:36864\n" \
    "v_mfma_f32_16x16x32_bf16 v[68:71], a[228:231], v[112:115], v[68:71]\n" \
    "v_mfma_f32_16x16x32_bf16 v[84:87], a[228:231], v[116:119], v[84:87]\n" \
    "v_mfma_f32_16x16x32_bf16 v[72:75], a[232:235], v[112:115], v[72:75]\n" \
    "v_mfma_f32_16x16x32_bf16 v[88:91], a[232:235], v[116:119], v[88:91]\n" \
    "v_mfma_f32_16x16x32_bf16 v[76:79], a[236:239], v[112:115], v[76:79]\n" \
    "v_mfma_f32_16x16x32_bf16 v[92:95], a[236:239], v[116:119], v[92:95]\n" \
    "s_waitcnt lgkmcnt(2)\n" \
    "v_mfma_f32_16x16x32_bf16 v[64:67], a[240:243], v[120:123], v[64:67]\n" \
    "ds_read_b128 v[104:107], v21 offset:17408\n" \
    "v_mfma_f32_16x16x32_bf16 v[80:83], a[240:243], v[124:127], v[80:83]\n" \
    "ds_read_b128 v[108:111], v21 offset:37888\n" \
    "v_mfma_f32_16x16x32_bf16 v[68:71], a[244:247], v[120:123], v[68:71]\n" \
    "v_mfma_f32_16x16x32_bf16 v[84:87], a[244:247], v[124:127], v[84:87]\n" \
    "v_mfma_f32_16x16x32_bf16 v[72:75], a[248:251], v[120:123], v[72:75]\n" \
    "v_mfma_f32_16x16x32_bf16 v[88:91], a[248:251], v[124:127], v[88:91]\n" \
    "v_mfma_f32_16x16x32_bf16 v[76:79], a[252:255], v[120:123], v[76:79]\n" \
    "v_mfma_f32_16x16x32_bf16 v[92:95], a[252:255], v[124:127], v[92:95]\n" \
    "s_waitcnt lgkmcnt(2)\n" \
    "v_mfma_f32_16x16x32_bf16 v[64:67], v[192:195], v[96:99], v[64:67]\n" \
    "ds_read_b128 v[112:115], v21 offset:18432\n" \
    "v_mfma_f32_16x16x32_bf16 v[80:83], v[192:195], v[100:103], v[80:83]\n" \
    "ds_read_b128 v[116:119], v21 offset:38912\n" \
    "v_mfma_f32_16x16x32_bf16 v[68:71], v[196:199], v[96:99], v[68:71]\n" \
    "v_mfma_f32_16x16x32_bf16 v[84:87], v[196:199], v[100:103], v[84:87]\n" \
    "v_mfma_f32_16x16x32_bf16 v[72:75], v[200:203], v[96:99], v[72:75]\n" \
    "v_mfma_f32_16x16x32_bf16 v[88:91], v[200:203], v[100:103], v[88:91]\n" \
    "v_mfma_f32_16x16x32_bf16 v[76:79], v[204:207], v[96:99], v[76:79]\n" \
    "v_mfma_f32_16x16x32_bf16 v[92:95], v[204:207], v[100:103], v[92:95]\n" \
    "s_waitcnt lgkmcnt(2)\n" \
    "v_mfma_f32_16x16x32_bf16 v[64:67], v[208:211], v[104:107], v[64:67]\n" \
    "ds_read_b128 v[120:123], v21 offset:19456\n" \
    "v_mfma_f32_16x16x32_bf16 v[80:83], v[208:211], v[108:111], v[80:83]\n" \
    "ds_read_b128 v[124:127], v21 offset:39936\n" \
    "v_mfma_f32_16x16x32_bf16 v[68:71], v[212:215], v[104:107], v[68:71]\n" \
    "v_mfma_f32_16x16x32_bf16 v[84:87], v[212:215], v[108:111], v[84:87]\n" \
    "v_mfma_f32_16x16x32_bf16 v[72:75], v[216:219], v[104:107], v[72:75]\n" \
    "v_mfma_f32_16x16x32_bf16 v[88:91], v[216:219], v[108:111], v[88:91]\n" \
    "v_mfma_f32_16x16x32_bf16 v[76:79], v[220:223], v[104:107], v[76:79]\n" \
    "v_mfma_f32_16x16x32_bf16 v[92:95], v[220:223], v[108:111], v[92:95]\n" \
    "s_add_u32 s75, s72, s54\n" \
    "s_add_u32 s72, s72, 40960\n" \
    "s_cmp_eq_u32 s72, 81920\n" \
    "s_cselect_b32 s72, 114688, s72\n" \
    "s_cmp_eq_u32 s72, 155648\n" \
    "s_cselect_b32 s72, 0, s72\n" \
    "v_add_u32 v21, s72, v16\n" \
    "s_waitcnt lgkmcnt(0) vmcnt(10)\n" \
    "s_barrier\n" \
    "v_mfma_f32_16x16x32_bf16 v[64:67], v[224:227], v[112:115], v[64:67]\n" \
    "ds_read_b128 v[96:99], v21 offset:0\n" \
    "v_mfma_f32_16x16x32_bf16 v[80:83], v[224:227], v[116:119], v[80:83]\n" \
    "ds_read_b128 v[100:103], v21 offset:20480\n" \
    "v_mfma_f32_16x16x32_bf16 v[68:71], v[228:231], v[112:115], v[68:71]\n" \
    "v_mfma_f32_16x16x32_bf16 v[84:87], v[228:231], v[116:119], v[84:87]\n" \
    "v_mfma_f32_16x16x32_bf16 v[72:75], v[232:235], v[112:115], v[72:75]\n" \
    "v_mfma_f32_16x16x32_bf16 v[88:91], v[232:235], v[116:119], v[88:91]\n" \
    "v_mfma_f32_16x16x32_bf16 v[76:79], v[236:239], v[112:115], v[76:79]\n" \
    "v_mfma_f32_16x16x32_bf16 v[92:95], v[236:239], v[116:119], v[92:95]\n" \
    "v_mfma_f32_16x16x32_bf16 v[64:67], v[240:243], v[120:123], v[64:67]\n" \
    "ds_read_b128 v[104:107], v21 offset:1024\n" \
    "v_mfma_f32_16x16x32_bf16 v[80:83], v[240:243], v[124:127], v[80:83]\n" \
    "ds_read_b128 v[108:111], v21 offset:21504\n" \
    "v_mfma_f32_16x16x32_bf16 v[68:71], v[244:247], v[120:123], v[68:71]\n" \
    "v_mfma_f32_16x16x32_bf16 v[84:87], v[244:247], v[124:127], v[84:87]\n" \
    "v_mfma_f32_16x16x32_bf16 v[72:75], v[248:251], v[120:123], v[72:75]\n" \
    "v_mfma_f32_16x16x32_bf16 v[88:91], v[248:251], v[124:127], v[88:91]\n" \
    "v_mfma_f32_16x16x32_bf16 v[76:79], v[252:255], v[120:123], v[76:79]\n" \
    "v_mfma_f32_16x16x32_bf16 v[92:95], v[252:255], v[124:127], v[92:95]\n" \
    "s_mov_b32 s74, s53\n" \
    "s_nop 7\n" \
    "v_cvt_pk_bf16_f32 v168, v64, v65\n" \
    "v_cvt_pk_bf16_f32 v169, v66, v67\n" \
    "v_cvt_pk_bf16_f32 v170, v68, v69\n" \
    "v_cvt_pk_bf16_f32 v171, v70, v71\n" \
    "v_cvt_pk_bf16_f32 v172, v72, v73\n" \
    "v_cvt_pk_bf16_f32 v173, v74, v75\n" \
    "v_cvt_pk_bf16_f32 v174, v76, v77\n" \
    "v_cvt_pk_bf16_f32 v175, v78, v79\n" \
    "v_pk_max_i16 v168, v168, 0\n" \
    "v_pk_max_i16 v169, v169, 0\n" \
    "v_pk_max_i16 v170, v170, 0\n" \
    "v_pk_max_i16 v171, v171, 0\n" \
    "v_lshlrev_b32 v48, 16, v172\n" \
    "v_and_b32 v49, 0xffff0000, v172\n" \
    "v_lshlrev_b32 v50, 16, v173\n" \
    "v_and_b32 v51, 0xffff0000, v173\n" \
    "v_lshlrev_b32 v52, 16, v174\n" \
    "v_and_b32 v53, 0xffff0000, v174\n" \
    "v_lshlrev_b32 v54, 16, v175\n" \
    "v_and_b32 v55, 0xffff0000, v175\n" \
    "v_lshlrev_b32 v32, 16, v168\n" \
    "v_and_b32 v33, 0xffff0000, v168\n" \
    "v_lshlrev_b32 v34, 16, v169\n" \
    "v_and_b32 v35, 0xffff0000, v169\n" \
    "v_lshlrev_b32 v36, 16, v170\n" \
    "v_and_b32 v37, 0xffff0000, v170\n" \
    "v_lshlrev_b32 v38, 16, v171\n" \
    "v_and_b32 v39, 0xffff0000, v171\n" \
    "v_pk_mul_f32 v[32:33], v[32:33], v[48:49]\n" \
    "v_pk_mul_f32 v[34:35], v[34:35], v[50:51]\n" \
    "v_pk_mul_f32 v[36:37], v[36:37], v[52:53]\n" \
    "v_pk_mul_f32 v[38:39], v[38:39], v[54:55]\n" \
    "v_cvt_pk_bf16_f32 v160, v32, v33\n" \
    "v_cvt_pk_bf16_f32 v161, v34, v35\n" \
    "v_cvt_pk_bf16_f32 v162, v36, v37\n" \
    "v_cvt_pk_bf16_f32 v163, v38, v39\n" \
    "buffer_store_dwordx4 v[160:163], v18, s[36:39], s74 offen sc1\n" \
    "v_cvt_pk_bf16_f32 v168, v80, v81\n" \
    "v_cvt_pk_bf16_f32 v169, v82, v83\n" \
    "v_cvt_pk_bf16_f32 v170, v84, v85\n" \
    "v_cvt_pk_bf16_f32 v171, v86, v87\n" \
    "v_cvt_pk_bf16_f32 v172, v88, v89\n" \
    "v_cvt_pk_bf16_f32 v173, v90, v91\n" \
    "v_cvt_pk_bf16_f32 v174, v92, v93\n" \
    "v_cvt_pk_bf16_f32 v175, v94, v95\n" \
    "v_pk_max_i16 v168, v168, 0\n" \
    "v_pk_max_i16 v169, v169, 0\n" \
    "v_pk_max_i16 v170, v170, 0\n" \
    "v_pk_max_i16 v171, v171, 0\n" \
    "v_lshlrev_b32 v48, 16, v172\n" \
    "v_and_b32 v49, 0xffff0000, v172\n" \
    "v_lshlrev_b32 v50, 16, v173\n" \
    "v_and_b32 v51, 0xffff0000, v173\n" \
    "v_lshlrev_b32 v52, 16, v174\n" \
    "v_and_b32 v53, 0xffff0000, v174\n" \
    "v_lshlrev_b32 v54, 16, v175\n" \
    "v_and_b32 v55, 0xffff0000, v175\n" \
    "v_lshlrev_b32 v32, 16, v168\n" \
    "v_and_b32 v33, 0xffff0000, v168\n" \
    "v_lshlrev_b32 v34, 16, v169\n" \
    "v_and_b32 v35, 0xffff0000, v169\n" \
    "v_lshlrev_b32 v36, 16, v170\n" \
    "v_and_b32 v37, 0xffff0000, v170\n" \
    "v_lshlrev_b32 v38, 16, v171\n" \
    "v_and_b32 v39, 0xffff0000, v171\n" \
    "v_pk_mul_f32 v[32:33], v[32:33], v[48:49]\n" \
    "v_pk_mul_f32 v[34:35], v[34:35], v[50:51]\n" \
    "v_pk_mul_f32 v[36:37], v[36:37], v[52:53]\n" \
    "v_pk_mul_f32 v[38:39], v[38:39], v[54:55]\n" \
    "v_cvt_pk_bf16_f32 v164, v32, v33\n" \
    "v_cvt_pk_bf16_f32 v165, v34, v35\n" \
    "v_cvt_pk_bf16_f32 v166, v36, v37\n" \
    "v_cvt_pk_bf16_f32 v167, v38, v39\n" \
    "s_add_u32 s74, s74, 55296\n" \
    "buffer_store_dwordx4 v[164:167], v18, s[36:39], s74 offen sc1\n" \
    "v_mov_b32 v22, s63\n" \
    "v_mov_b32 v23, 1\n" \
    "s_waitcnt vmcnt(0)\n" \
    "buffer_store_dwordx2 v[22:23], v17, s[36:39], 0 offen sc1\n" \
    "P2_la_%=:\n" \
    "buffer_load_dwordx2 v[22:23], v19, s[36:39], 0 offen sc1\n" \
    "s_waitcnt vmcnt(0)\n" \
    "v_cmp_eq_u32 vcc, s63, v22\n" \
    "v_cmp_eq_u32 s[76:77], s63, v23\n" \
    "s_and_b64 vcc, vcc, s[76:77]\n" \
    "s_and_b64 vcc, vcc, exec\n" \
    "s_cmp_eq_u64 vcc, exec\n" \
    "s_cbranch_scc0 P2_la_no_%=\n" \
    "P2_loop_%=:\n" \
    "s_mul_i32 s68, s70, 110592\n" \
    "s_add_u32 s74, s68, s53\n" \
    "s_add_u32 s70, s70, 1\n" \
    "s_add_u32 s68, s70, 2\n" \
    "s_min_u32 s68, s68, 63\n" \
    "s_mul_i32 s68, s68, 40960\n" \
    "s_add_u32 s40, s68, s54\n" \
    "s_add_u32 s40, s40, 2097152\n" \
    "s_add_u32 s41, s40, 4096\n" \
    "s_add_u32 s42, s40, 8192\n" \
    "s_mov_b32 s43, s75\n" \
    "s_waitcnt lgkmcnt(2)\n" \
    "v_mfma_f32_16x16x32_bf16 v[128:131], a[0:3], v[96:99], 0\n" \
    "ds_read_b128 v[112:115], v21 offset:2048\n" \
    "v_mfma_f32_16x16x32_bf16 v[144:147], a[0:3], v[100:103], 0\n" \
    "ds_read_b128 v[116:119], v21 offset:22528\n" \
    "v_mfma_f32_16x16x32_bf16 v[132:135], a[4:7], v[96:99], 0\n" \
    "s_add_u32 m0, s43, 0\n" \
    "v_mfma_f32_16x16x32_bf16 v[148:151], a[4:7], v[100:103], 0\n" \
    "buffer_load_dwordx4 v16, s[36:39], s40 offen offset:0 sc1 lds\n" \
    "v_mfma_f32_16x16x32_bf16 v[136:139], a[8:11], v[96:99], 0\n" \
    "v_mfma_f32_16x16x32_bf16 v[152:155], a[8:11], v[100:103], 0\n" \
    "v_mfma_f32_16x16x32_bf16 v[140:143], a[12:15], v[96:99], 0\n" \
    "v_mfma_f32_16x16x32_bf16 v[156:159], a[12:15], v[100:103], 0\n" \
    "s_waitcnt lgkmcnt(2)\n" \
    "v_mfma_f32_16x16x32_bf16 v[128:131], a[16:19], v[104:107], v[128:131]\n" \
    "ds_read_b128 v[120:123], v21 offset:3072\n" \
    "v_mfma_f32_16x16x32_bf16 v[144:147], a[16:19], v[108:111], v[144:147]\n" \
    "ds_read_b128 v[124:127], v21 offset:23552\n" \
    "v_mfma_f32_16x16x32_bf16 v[132:135], a[20:23], v[104:107], v[132:135]\n" \
    "s_add_u32 m0, s43, 0\n" \
    "v_mfma_f32_16x16x32_bf16 v[148:151], a[20:23], v[108:111], v[148:151]\n" \
    "buffer_load_dwordx4 v16, s[36:39], s40 offen offset:1024 sc1 lds\n" \
    "v_mfma_f32_16x16x32_bf16 v[136:139], a[24:27], v[104:107], v[136:139]\n" \
    "v_cvt_pk_bf16_f32 v168, v64, v65\n" \
    "v_cvt_pk_bf16_f32 v169, v66, v67\n" \
    "v_mfma_f32_16x16x32_bf16 v[152:155], a[24:27], v[108:111], v[152:155]\n" \
    "v_cvt_pk_bf16_f32 v170, v68, v69\n" \
    "v_cvt_pk_bf16_f32 v171, v70, v71\n" \
    "v_mfma_f32_16x16x32_bf16 v[140:143], a[28:31], v[104:107], v[140:143]\n" \
    "v_cvt_pk_bf16_f32 v172, v72, v73\n" \
    "v_cvt_pk_bf16_f32 v173, v74, v75\n" \
    "v_mfma_f32_16x16x32_bf16 v[156:159], a[28:31], v[108:111], v[156:159]\n" \
    "v_cvt_pk_bf16_f32 v174, v76, v77\n" \
    "v_cvt_pk_bf16_f32 v175, v78, v79\n" \
    "s_waitcnt lgkmcnt(2)\n" \
    "v_mfma_f32_16x16x32_bf16 v[128:131], a[32:35], v[112:115], v[128:131]\n" \
    "ds_read_b128 v[96:99], v21 offset:4096\n" \
    "v_mfma_f32_16x16x32_bf16 v[144:147], a[32:35], v[116:119], v[144:147]\n" \
    "ds_read_b128 v[100:103], v21 offset:24576\n" \
    "v_mfma_f32_16x16x32_bf16 v[132:135], a[36:39], v[112:115], v[132:135]\n" \
    "s_add_u32 m0, s43, 0\n" \
    "v_mfma_f32_16x16x32_bf16 v[148:151], a[36:39], v[116:119], v[148:151]\n" \
    "buffer_load_dwordx4 v16, s[36:39], s40 offen offset:2048 sc1 lds\n" \
    "v_mfma_f32_16x16x32_bf16 v[136:139], a[40:43], v[112:115], v[136:139]\n" \
    "v_pk_max_i16 v168, v168, 0\n" \
    "v_pk_max_i16 v169, v169, 0\n" \
    "v_mfma_f32_16x16x32_bf16 v[152:155], a[40:43], v[116:119], v[152:155]\n" \
    "v_pk_max_i16 v170, v170, 0\n" \
    "v_pk_max_i16 v171, v171, 0\n" \
    "v_mfma_f32_16x16x32_bf16 v[140:143], a[44:47], v[112:115], v[140:143]\n" \
    "v_lshlrev_b32 v48, 16, v172\n" \
    "v_and_b32 v49, 0xffff0000, v172\n" \
    "v_mfma_f32_16x16x32_bf16 v[156:159], a[44:47], v[116:119], v[156:159]\n" \
    "v_lshlrev_b32 v50, 16, v173\n" \
    "v_and_b32 v51, 0xffff0000, v173\n" \
    "s_waitcnt lgkmcnt(2)\n" \
    "v_mfma_f32_16x16x32_bf16 v[128:131], a[48:51], v[120:123], v[128:131]\n" \
    "ds_read_b128 v[104:107], v21 offset:5120\n" \
    "v_mfma_f32_16x16x32_bf16 v[144:147], a[48:51], v[124:127], v[144:147]\n" \
    "ds_read_b128 v[108:111], v21 offset:25600\n" \
    "v_mfma_f32_16x16x32_bf16 v[132:135], a[52:55], v[120:123], v[132:135]\n" \
    "s_add_u32 m0, s43, 0\n" \
    "v_mfma_f32_16x16x32_bf16 v[148:151], a[52:55], v[124:127], v[148:151]\n" \
    "buffer_load_dwordx4 v16, s[36:39], s40 offen offset:3072 sc1 lds\n" \
    "v_mfma_f32_16x16x32_bf16 v[136:139], a[56:59], v[120:123], v[136:139]\n" \
    "v_lshlrev_b32 v52, 16, v174\n" \
    "v_and_b32 v53, 0xffff0000, v174\n" \
    "v_mfma_f32_16x16x32_bf16 v[152:155], a[56:59], v[124:127], v[152:155]\n" \
    "v_lshlrev_b32 v54, 16, v175\n" \
    "v_and_b32 v55, 0xffff0000, v175\n" \
    "v_mfma_f32_16x16x32_bf16 v[140:143], a[60:63], v[120:123], v[140:143]\n" \
    "v_lshlrev_b32 v32, 16, v168\n" \
    "v_and_b32 v33, 0xffff0000, v168\n" \
    "v_mfma_f32_16x16x32_bf16 v[156:159], a[60:63], v[124:127], v[156:159]\n" \
    "v_lshlrev_b32 v34, 16, v169\n" \
    "v_and_b32 v35, 0xffff0000, v169\n" \
    "s_waitcnt lgkmcnt(2)\n" \
    "v_mfma_f32_16x16x32_bf16 v[128:131], a[64:67], v[96:99], v[128:131]\n" \
    "ds_read_b128 v[112:115], v21 offset:6144\n" \
    "v_mfma_f32_16x16x32_bf16 v[144:147], a[64:67], v[100:103], v[144:147]\n" \
    "ds_read_b128 v[116:119], v21 offset:26624\n" \
    "v_mfma_f32_16x16x32_bf16 v[132:135], a[68:71], v[96:99], v[132:135]\n" \
    "s_add_u32 m0, s43, 4096\n" \
    "v_mfma_f32_16x16x32_bf16 v[148:151], a[68:71], v[100:103], v[148:151]\n" \
    "buffer_load_dwordx4 v16, s[36:39], s41 offen offset:0 sc1 lds\n" \
    "v_mfma_f32_16x16x32_bf16 v[136:139], a[72:75], v[96:99], v[136:139]\n" \
    "v_lshlrev_b32 v36, 16, v170\n" \
    "v_and_b32 v37, 0xffff0000, v170\n" \
    "v_mfma_f32_16x16x32_bf16 v[152:155], a[72:75], v[100:103], v[152:155]\n" \
    "v_lshlrev_b32 v38, 16, v171\n" \
    "v_and_b32 v39, 0xffff0000, v171\n" \
    "v_mfma_f32_16x16x32_bf16 v[140:143], a[76:79], v[96:99], v[140:143]\n" \
    "v_pk_mul_f32 v[32:33], v[32:33], v[48:49]\n" \
    "v_pk_mul_f32 v[34:35], v[34:35], v[50:51]\n" \
    "v_mfma_f32_16x16x32_bf16 v[156:159], a[76:79], v[100:103], v[156:159]\n" \
    "v_pk_mul_f32 v[36:37], v[36:37], v[52:53]\n" \
    "v_pk_mul_f32 v[38:39], v[38:39], v[54:55]\n" \
    "s_waitcnt lgkmcnt(2)\n" \
    "v_mfma_f32_16x16x32_bf16 v[128:131], a[80:83], v[104:107], v[128:131]\n" \
    "ds_read_b128 v[120:123], v21 offset:7168\n" \
    "v_mfma_f32_16x16x32_bf16 v[144:147], a[80:83], v[108:111], v[144:147]\n" \
    "ds_read_b128 v[124:127], v21 offset:27648\n" \
    "v_mfma_f32_16x16x32_bf16 v[132:135], a[84:87], v[104:107], v[132:135]\n" \
    "s_add_u32 m0, s43, 4096\n" \
    "v_mfma_f32_16x16x32_bf16 v[148:151], a[84:87], v[108:111], v[148:151]\n" \
    "buffer_load_dwordx4 v16, s[36:39], s41 offen offset:1024 sc1 lds\n" \
    "v_mfma_f32_16x16x32_bf16 v[136:139], a[88:91], v[104:107], v[136:139]\n" \
    "v_cvt_pk_bf16_f32 v160, v32, v33\n" \
    "v_cvt_pk_bf16_f32 v161, v34, v35\n" \
    "v_mfma_f32_16x16x32_bf16 v[152:155], a[88:91], v[108:111], v[152:155]\n" \
    "v_cvt_pk_bf16_f32 v162, v36, v37\n" \
    "v_cvt_pk_bf16_f32 v163, v38, v39\n" \
    "v_mfma_f32_16x16x32_bf16 v[140:143], a[92:95], v[104:107], v[140:143]\n" \
    "buffer_store_dwordx4 v[160:163], v18, s[36:39], s74 offen sc1\n" \
    "v_cvt_pk_bf16_f32 v168, v80, v81\n" \
    "v_mfma_f32_16x16x32_bf16 v[156:159], a[92:95], v[108:111], v[156:159]\n" \
    "v_cvt_pk_bf16_f32 v169, v82, v83\n" \
    "v_cvt_pk_bf16_f32 v170, v84, v85\n" \
    "s_waitcnt lgkmcnt(2)\n" \
    "v_mfma_f32_16x16x32_bf16 v[128:131], a[96:99], v[112:115], v[128:131]\n" \
    "ds_read_b128 v[96:99], v21 offset:8192\n" \
    "v_mfma_f32_16x16x32_bf16 v[144:147], a[96:99], v[116:119], v[144:147]\n" \
    "ds_read_b128 v[100:103], v21 offset:28672\n" \
    "v_mfma_f32_16x16x32_bf16 v[132:135], a[100:103], v[112:115], v[132:135]\n" \
    "s_add_u32 m0, s43, 4096\n" \
    "v_mfma_f32_16x16x32_bf16 v[148:151], a[100:103], v[116:119], v[148:151]\n" \
    "buffer_load_dwordx4 v16, s[36:39], s41 offen offset:2048 sc1 lds\n" \
    "v_mfma_f32_16x16x32_bf16 v[136:139], a[104:107], v[112:115], v[136:139]\n" \
    "v_cvt_pk_bf16_f32 v171, v86, v87\n" \
    "v_cvt_pk_bf16_f32 v172, v88, v89\n" \
    "v_mfma_f32_16x16x32_bf16 v[152:155], a[104:107], v[116:119], v[152:155]\n" \
    "v_cvt_pk_bf16_f32 v173, v90, v91\n" \
    "v_cvt_pk_bf16_f32 v174, v92, v93\n" \
    "v_mfma_f32_16x16x32_bf16 v[140:143], a[108:111], v[112:115], v[140:143]\n" \
    "v_cvt_pk_bf16_f32 v175, v94, v95\n" \
    "v_pk_max_i16 v168, v168, 0\n" \
    "v_mfma_f32_16x16x32_bf16 v[156:159], a[108:111], v[116:119], v[156:159]\n" \
    "v_pk_max_i16 v169, v169, 0\n" \
    "v_pk_max_i16 v170, v170, 0\n" \
    "s_waitcnt lgkmcnt(2)\n" \
    "v_mfma_f32_16x16x32_bf16 v[128:131], a[112:115], v[120:123], v[128:131]\n" \
    "ds_read_b128 v[104:107], v21 offset:9216\n" \
    "v_mfma_f32_16x16x32_bf16 v[144:147], a[112:115], v[124:127], v[144:147]\n" \
    "ds_read_b128 v[108:111], v21 offset:29696\n" \
    "v_mfma_f32_16x16x32_bf16 v[132:135], a[116:119], v[120:123], v[132:135]\n" \
    "s_add_u32 m0, s43, 4096\n" \
    "v_mfma_f32_16x16x32_bf16 v[148:151], a[116:119], v[124:127], v[148:151]\n" \
    "buffer_load_dwordx4 v16, s[36:39], s41 offen offset:3072 sc1 lds\n" \
    "v_mfma_f32_16x16x32_bf16 v[136:139], a[120:123], v[120:123], v[136:139]\n" \
    "v_pk_max_i16 v171, v171, 0\n" \
    "v_lshlrev_b32 v48, 16, v172\n" \
    "v_mfma_f32_16x16x32_bf16 v[152:155], a[120:123], v[124:127], v[152:155]\n" \
    "v_and_b32 v49, 0xffff0000, v172\n" \
    "v_lshlrev_b32 v50, 16, v173\n" \
    "v_mfma_f32_16x16x32_bf16 v[140:143], a[124:127], v[120:123], v[140:143]\n" \
    "v_and_b32 v51, 0xffff0000, v173\n" \
    "v_lshlrev_b32 v52, 16, v174\n" \
    "v_mfma_f32_16x16x32_bf16 v[156:159], a[124:127], v[124:127], v[156:159]\n" \
    "v_and_b32 v53, 0xffff0000, v174\n" \
    "v_lshlrev_b32 v54, 16, v175\n" \
    "s_waitcnt lgkmcnt(2)\n" \
    "v_mfma_f32_16x16x32_bf16 v[128:131], a[128:131], v[96:99], v[128:131]\n" \
    "ds_read_b128 v[112:115], v21 offset:10240\n" \
    "v_mfma_f32_16x16x32_bf16 v[144:147], a[128:131], v[100:103], v[144:147]\n" \
    "ds_read_b128 v[116:119], v21 offset:30720\n" \
    "v_mfma_f32_16x16x32_bf16 v[132:135], a[132:135], v[96:99], v[132:135]\n" \
    "s_add_u32 m0, s43, 8192\n" \
    "v_mfma_f32_16x16x32_bf16 v[148:151], a[132:135], v[100:103], v[148:151]\n" \
    "buffer_load_dwordx4 v16, s[36:39], s42 offen offset:0 sc1 lds\n" \
    "v_mfma_f32_16x16x32_bf16 v[136:139], a[136:139], v[96:99], v[136:139]\n" \
    "v_and_b32 v55, 0xffff0000, v175\n" \
    "v_lshlrev_b32 v32, 16, v168\n" \
    "v_mfma_f32_16x16x32_bf16 v[152:155], a[136:139], v[100:103], v[152:155]\n" \
    "v_and_b32 v33, 0xffff0000, v168\n" \
    "v_lshlrev_b32 v34, 16, v169\n" \
    "v_mfma_f32_16x16x32_bf16 v[140:143], a[140:143], v[96:99], v[140:143]\n" \
    "v_and_b32 v35, 0xffff0000, v169\n" \
    "v_lshlrev_b32 v36, 16, v170\n" \
    "v_mfma_f32_16x16x32_bf16 v[156:159], a[140:143], v[100:103], v[156:159]\n" \
    "v_and_b32 v37, 0xffff0000, v170\n" \
    "v_lshlrev_b32 v38, 16, v171\n" \
    "s_waitcnt lgkmcnt(2)\n" \
    "v_mfma_f32_16x16x32_bf16 v[128:131], a[144:147], v[104:107], v[128:131]\n" \
    "ds_read_b128 v[120:123], v21 offset:11264\n" \
    "v_mfma_f32_16x16x32_bf16 v[144:147], a[144:147], v[108:111], v[144:147]\n" \
    "ds_read_b128 v[124:127], v21 offset:31744\n" \
    "v_mfma_f32_16x16x32_bf16 v[132:135], a[148:151], v[104:107], v[132:135]\n" \
    "s_add_u32 m0, s43, 8192\n" \
    "v_mfma_f32_16x16x32_bf16 v[148:151], a[148:151], v[108:111], v[148:151]\n" \
    "buffer_load_dwordx4 v16, s[36:39], s42 offen offset:1024 sc1 lds\n" \
    "v_mfma_f32_16x16x32_bf16 v[136:139], a[152:155], v[104:107], v[136:139]\n" \
    "v_and_b32 v39, 0xffff0000, v171\n" \
    "v_pk_mul_f32 v[32:33], v[32:33], v[48:49]\n" \
    "v_mfma_f32_16x16x32_bf16 v[152:155], a[152:155], v[108:111], v[152:155]\n" \
    "v_pk_mul_f32 v[34:35], v[34:35], v[50:51]\n" \
    "v_pk_mul_f32 v[36:37], v[36:37], v[52:53]\n" \
    "v_mfma_f32_16x16x32_bf16 v[140:143], a[156:159], v[104:107], v[140:143]\n" \
    "v_pk_mul_f32 v[38:39], v[38:39], v[54:55]\n" \
    "v_cvt_pk_bf16_f32 v164, v32, v33\n" \
    "v_mfma_f32_16x16x32_bf16 v[156:159], a[156:159], v[108:111], v[156:159]\n" \
    "v_cvt_pk_bf16_f32 v165, v34, v35\n" \
    "v_cvt_pk_bf16_f32 v166, v36, v37\n" \
    "s_waitcnt lgkmcnt(2)\n" \
    "v_mfma_f32_16x16x32_bf16 v[128:131], a[160:163], v[112:115], v[128:131]\n" \
    "ds_read_b128 v[96:99], v21 offset:12288\n" \
    "v_mfma_f32_16x16x32_bf16 v[144:147], a[160:163], v[116:119], v[144:147]\n" \
    "ds_read_b128 v[100:103], v21 offset:32768\n" \
    "v_mfma_f32_16x16x32_bf16 v[132:135], a[164:167], v[112:115], v[132:135]\n" \
    "v_mfma_f32_16x16x32_bf16 v[148:151], a[164:167], v[116:119], v[148:151]\n" \
    "v_mfma_f32_16x16x32_bf16 v[136:139], a[168:171], v[112:115], v[136:139]\n" \
    "v_cvt_pk_bf16_f32 v167, v38, v39\n" \
    "v_mfma_f32_16x16x32_bf16 v[152:155], a[168:171], v[116:119], v[152:155]\n" \
    "s_add_u32 s74, s74, 55296\n" \
    "v_mfma_f32_16x16x32_bf16 v[140:143], a[172:175], v[112:115], v[140:143]\n" \
    "buffer_store_dwordx4 v[164:167], v18, s[36:39], s74 offen sc1\n" \
    "v_mfma_f32_16x16x32_bf16 v[156:159], a[172:175], v[116:119], v[156:159]\n" \
    "s_waitcnt lgkmcnt(2)\n" \
    "v_mfma_f32_16x16x32_bf16 v[128:131], a[176:179], v[120:123], v[128:131]\n" \
    "ds_read_b128 v[104:107], v21 offset:13312\n" \
    "v_mfma_f32_16x16x32_bf16 v[144:147], a[176:179], v[124:127], v[144:147]\n" \
    "ds_read_b128 v[108:111], v21 offset:33792\n" \
    "v_mfma_f32_16x16x32_bf16 v[132:135], a[180:183], v[120:123], v[132:135]\n" \
    "v_mfma_f32_16x16x32_bf16 v[148:151], a[180:183], v[124:127], v[148:151]\n" \
    "v_mfma_f32_16x16x32_bf16 v[136:139], a[184:187], v[120:123], v[136:139]\n" \
    "v_mfma_f32_16x16x32_bf16 v[152:155], a[184:187], v[124:127], v[152:155]\n" \
    "v_mfma_f32_16x16x32_bf16 v[140:143], a[188:191], v[120:123], v[140:143]\n" \
    "v_mfma_f32_16x16x32_bf16 v[156:159], a[188:191], v[124:127], v[156:159]\n" \
    "s_waitcnt lgkmcnt(2)\n" \
    "v_mfma_f32_16x16x32_bf16 v[128:131], a[192:195], v[96:99], v[128:131]\n" \
    "ds_read_b128 v[112:115], v21 offset:14336\n" \
    "v_mfma_f32_16x16x32_bf16 v[144:147], a[192:195], v[100:103], v[144:147]\n" \
    "ds_read_b128 v[116:119], v21 offset:34816\n" \
    "v_mfma_f32_16x16x32_bf16 v[132:135], a[196:199], v[96:99], v[132:135]\n" \
    "v_mfma_f32_16x16x32_bf16 v[148:151], a[196:199], v[100:103], v[148:151]\n" \
    "v_mfma_f32_16x16x32_bf16 v[136:139], a[200:203], v[96:99], v[136:139]\n" \
    "v_mfma_f32_16x16x32_bf16 v[152:155], a[200:203], v[100:103], v[152:155]\n" \
    "v_mfma_f32_16x16x32_bf16 v[140:143], a[204:207], v[96:99], v[140:143]\n" \
    "v_mfma_f32_16x16x32_bf16 v[156:159], a[204:207], v[100:103], v[156:159]\n" \
    "s_waitcnt lgkmcnt(2)\n" \
    "v_mfma_f32_16x16x32_bf16 v[128:131], a[208:211], v[104:107], v[128:131]\n" \
    "ds_read_b128 v[120:123], v21 offset:15360\n" \
    "v_mfma_f32_16x16x32_bf16 v[144:147], a[208:211], v[108:111], v[144:147]\n" \
    "ds_read_b128 v[124:127], v21 offset:35840\n" \
    "v_mfma_f32_16x16x32_bf16 v[132:135], a[212:215], v[104:107], v[132:135]\n" \
    "v_mfma_f32_16x16x32_bf16 v[148:151], a[212:215], v[108:111], v[148:151]\n" \
    "v_mfma_f32_16x16x32_bf16 v[136:139], a[216:219], v[104:107], v[136:139]\n" \
    "v_mfma_f32_16x16x32_bf16 v[152:155], a[216:219], v[108:111], v[152:155]\n" \
    "v_mfma_f32_16x16x32_bf16 v[140:143], a[220:223], v[104:107], v[140:143]\n" \
    "v_mfma_f32_16x16x32_bf16 v[156:159], a[220:223], v[108:111], v[156:159]\n" \
    "s_waitcnt lgkmcnt(2)\n" \
    "v_mfma_f32_16x16x32_bf16 v[128:131], a[224:227], v[112:115], v[128:131]\n" \
    "ds_read_b128 v[96:99], v21 offset:16384\n" \
    "v_mfma_f32_16x16x32_bf16 v[144:147], a[224:227], v[116:119], v[144:147]\n" \
    "ds_read_b128 v[100:103], v21 offset:36864\n" \
    "v_mfma_f32_16x16x32_bf16 v[132:135], a[228:231], v[112:115], v[132:135]\n" \
    "v_mfma_f32_16x16x32_bf16 v[148:151], a[228:231], v[116:119], v[148:151]\n" \
    "v_mfma_f32_16x16x32_bf16 v[136:139], a[232:235], v[112:115], v[136:139]\n" \
    "v_mfma_f32_16x16x32_bf16 v[152:155], a[232:235], v[116:119], v[152:155]\n" \
    "v_mfma_f32_16x16x32_bf16 v[140:143], a[236:239], v[112:115], v[140:143]\n" \
    "v_mfma_f32_16x16x32_bf16 v[156:159], a[236:239], v[116:119], v[156:159]\n" \
    "s_waitcnt lgkmcnt(2)\n" \
    "v_mfma_f32_16x16x32_bf16 v[128:131], a[240:243], v[120:123], v[128:131]\n" \
    "ds_read_b128 v[104:107], v21 offset:17408\n" \
    "v_mfma_f32_16x16x32_bf16 v[144:147], a[240:243], v[124:127], v[144:147]\n" \
    "ds_read_b128 v[108:111], v21 offset:37888\n" \
    "v_mfma_f32_16x16x32_bf16 v[132:135], a[244:247], v[120:123], v[132:135]\n" \
    "v_mfma_f32_16x16x32_bf16 v[148:151], a[244:247], v[124:127], v[148:151]\n" \
    "v_mfma_f32_16x16x32_bf16 v[136:139], a[248:251], v[120:123], v[136:139]\n" \
    "v_mfma_f32_16x16x32_bf16 v[152:155], a[248:251], v[124:127], v[152:155]\n" \
    "v_mfma_f32_16x16x32_bf16 v[140:143], a[252:255], v[120:123], v[140:143]\n" \
    "v_mfma_f32_16x16x32_bf16 v[156:159], a[252:255], v[124:127], v[156:159]\n" \
    "s_waitcnt lgkmcnt(2)\n" \
    "v_mfma_f32_16x16x32_bf16 v[128:131], v[192:195], v[96:99], v[128:131]\n" \
    "ds_read_b128 v[112:115], v21 offset:18432\n" \
    "v_mfma_f32_16x16x32_bf16 v[144:147], v[192:195], v[100:103], v[144:147]\n" \
    "ds_read_b128 v[116:119], v21 offset:38912\n" \
    "v_mfma_f32_16x16x32_bf16 v[132:135], v[196:199], v[96:99], v[132:135]\n" \
    "v_mfma_f32_16x16x32_bf16 v[148:151], v[196:199], v[100:103], v[148:151]\n" \
    "v_mfma_f32_16x16x32_bf16 v[136:139], v[200:203], v[96:99], v[136:139]\n" \
    "v_mov_b32 v22, s63\n" \
    "v_mov_b32 v23, s70\n" \
    "v_mfma_f32_16x16x32_bf16 v[152:155], v[200:203], v[100:103], v[152:155]\n" \
    "s_waitcnt vmcnt(0)\n" \
    "v_mfma_f32_16x16x32_bf16 v[140:143], v[204:207], v[96:99], v[140:143]\n" \
    "buffer_store_dwordx2 v[22:23], v17, s[36:39], 0 offen sc1\n" \
    "v_mfma_f32_16x16x32_bf16 v[156:159], v[204:207], v[100:103], v[156:159]\n" \
    "s_waitcnt lgkmcnt(2)\n" \
    "v_mfma_f32_16x16x32_bf16 v[128:131], v[208:211], v[104:107], v[128:131]\n" \
    "ds_read_b128 v[120:123], v21 offset:19456\n" \
    "v_mfma_f32_16x16x32_bf16 v[144:147], v[208:211], v[108:111], v[144:147]\n" \
    "ds_read_b128 v[124:127], v21 offset:39936\n" \
    "v_mfma_f32_16x16x32_bf16 v[132:135], v[212:215], v[104:107], v[132:135]\n" \
    "v_mfma_f32_16x16x32_bf16 v[148:151], v[212:215], v[108:111], v[148:151]\n" \
    "v_mfma_f32_16x16x32_bf16 v[136:139], v[216:219], v[104:107], v[136:139]\n" \
    "v_mfma_f32_16x16x32_bf16 v[152:155], v[216:219], v[108:111], v[152:155]\n" \
    "v_mfma_f32_16x16x32_bf16 v[140:143], v[220:223], v[104:107], v[140:143]\n" \
    "v_mfma_f32_16x16x32_bf16 v[156:159], v[220:223], v[108:111], v[156:159]\n" \
    "s_add_u32 s75, s72, s54\n" \
    "s_add_u32 s72, s72, 40960\n" \
    "s_cmp_eq_u32 s72, 81920\n" \
    "s_cselect_b32 s72, 114688, s72\n" \
    "s_cmp_eq_u32 s72, 155648\n" \
    "s_cselect_b32 s72, 0, s72\n" \
    "v_add_u32 v21, s72, v16\n" \
    "s_waitcnt lgkmcnt(0)\n" \
    "s_barrier\n" \
    "v_mfma_f32_16x16x32_bf16 v[128:131], v[224:227], v[112:115], v[128:131]\n" \
    "ds_read_b128 v[96:99], v21 offset:0\n" \
    "v_mfma_f32_16x16x32_bf16 v[144:147], v[224:227], v[116:119], v[144:147]\n" \
    "ds_read_b128 v[100:103], v21 offset:20480\n" \
    "v_mfma_f32_16x16x32_bf16 v[132:135], v[228:231], v[112:115], v[132:135]\n" \
    "v_mfma_f32_16x16x32_bf16 v[148:151], v[228:231], v[116:119], v[148:151]\n" \
    "v_mfma_f32_16x16x32_bf16 v[136:139], v[232:235], v[112:115], v[136:139]\n" \
    "v_mfma_f32_16x16x32_bf16 v[152:155], v[232:235], v[116:119], v[152:155]\n" \
    "v_mfma_f32_16x16x32_bf16 v[140:143], v[236:239], v[112:115], v[140:143]\n" \
    "v_mfma_f32_16x16x32_bf16 v[156:159], v[236:239], v[116:119], v[156:159]\n" \
    "v_mfma_f32_16x16x32_bf16 v[128:131], v[240:243], v[120:123], v[128:131]\n" \
    "ds_read_b128 v[104:107], v21 offset:1024\n" \
    "v_mfma_f32_16x16x32_bf16 v[144:147], v[240:243], v[124:127], v[144:147]\n" \
    "ds_read_b128 v[108:111], v21 offset:21504\n" \
    "v_mfma_f32_16x16x32_bf16 v[132:135], v[244:247], v[120:123], v[132:135]\n" \
    "v_mfma_f32_16x16x32_bf16 v[148:151], v[244:247], v[124:127], v[148:151]\n" \
    "v_mfma_f32_16x16x32_bf16 v[136:139], v[248:251], v[120:123], v[136:139]\n" \
    "v_mfma_f32_16x16x32_bf16 v[152:155], v[248:251], v[124:127], v[152:155]\n" \
    "v_mfma_f32_16x16x32_bf16 v[140:143], v[252:255], v[120:123], v[140:143]\n" \
    "v_mfma_f32_16x16x32_bf16 v[156:159], v[252:255], v[124:127], v[156:159]\n" \
    "s_cmp_eq_u32 s70, 63\n" \
    "s_cbranch_scc1 P2_fin_%=\n" \
    "s_mul_i32 s68, s70, 110592\n" \
    "s_add_u32 s74, s68, s53\n" \
    "s_add_u32 s70, s70, 1\n" \
    "s_add_u32 s68, s70, 2\n" \
    "s_min_u32 s68, s68, 63\n" \
    "s_mul_i32 s68, s68, 40960\n" \
    "s_add_u32 s40, s68, s54\n" \
    "s_add_u32 s40, s40, 2097152\n" \
    "s_add_u32 s41, s40, 4096\n" \
    "s_add_u32 s42, s40, 8192\n" \
    "s_mov_b32 s43, s75\n" \
    "s_waitcnt lgkmcnt(2)\n" \
    "v_mfma_f32_16x16x32_bf16 v[64:67], a[0:3], v[96:99], 0\n" \
    "ds_read_b128 v[112:115], v21 offset:2048\n" \
    "v_mfma_f32_16x16x32_bf16 v[80:83], a[0:3], v[100:103], 0\n" \
    "ds_read_b128 v[116:119], v21 offset:22528\n" \
    "v_mfma_f32_16x16x32_bf16 v[68:71], a[4:7], v[96:99], 0\n" \
    "s_add_u32 m0, s43, 0\n" \
    "v_mfma_f32_16x16x32_bf16 v[84:87], a[4:7], v[100:103], 0\n" \
    "buffer_load_dwordx4 v16, s[36:39], s40 offen offset:0 sc1 lds\n" \
    "v_mfma_f32_16x16x32_bf16 v[72:75], a[8:11], v[96:99], 0\n" \
    "v_mfma_f32_16x16x32_bf16 v[88:91], a[8:11], v[100:103], 0\n" \
    "v_mfma_f32_16x16x32_bf16 v[76:79], a[12:15], v[96:99], 0\n" \
    "v_mfma_f32_16x16x32_bf16 v[92:95], a[12:15], v[100:103], 0\n" \
    "s_waitcnt lgkmcnt(2)\n" \
    "v_mfma_f32_16x16x32_bf16 v[64:67], a[16:19], v[104:107], v[64:67]\n" \
    "ds_read_b128 v[120:123], v21 offset:3072\n" \
    "v_mfma_f32_16x16x32_bf16 v[80:83], a[16:19], v[108:111], v[80:83]\n" \
    "ds_read_b128 v[124:127], v21 offset:23552\n" \
    "v_mfma_f32_16x16x32_bf16 v[68:71], a[20:23], v[104:107], v[68:71]\n" \
    "s_add_u32 m0, s43, 0\n" \
    "v_mfma_f32_16x16x32_bf16 v[84:87], a[20:23], v[108:111], v[84:87]\n" \
    "buffer_load_dwordx4 v16, s[36:39], s40 offen offset:1024 sc1 lds\n" \
    "v_mfma_f32_16x16x32_bf16 v[72:75], a[24:27], v[104:107], v[72:75]\n" \
    "v_cvt_pk_bf16_f32 v168, v128, v129\n" \
    "v_cvt_pk_bf16_f32 v169, v130, v131\n" \
    "v_mfma_f32_16x16x32_bf16 v[88:91], a[24:27], v[108:111], v[88:91]\n" \
    "v_cvt_pk_bf16_f32 v170, v132, v133\n" \
    "v_cvt_pk_bf16_f32 v171, v134, v135\n" \
    "v_mfma_f32_16x16x32_bf16 v[76:79], a[28:31], v[104:107], v[76:79]\n" \
    "v_cvt_pk_bf16_f32 v172, v136, v137\n" \
    "v_cvt_pk_bf16_f32 v173, v138, v139\n" \
    "v_mfma_f32_16x16x32_bf16 v[92:95], a[28:31], v[108:111], v[92:95]\n" \
    "v_cvt_pk_bf16_f32 v174, v140, v141\n" \
    "v_cvt_pk_bf16_f32 v175, v142, v143\n" \
    "s_waitcnt lgkmcnt(2)\n" \
    "v_mfma_f32_16x16x32_bf16 v[64:67], a[32:35], v[112:115], v[64:67]\n" \
    "ds_read_b128 v[96:99], v21 offset:4096\n" \
    "v_mfma_f32_16x16x32_bf16 v[80:83], a[32:35], v[116:119], v[80:83]\n" \
    "ds_read_b128 v[100:103], v21 offset:24576\n" \
    "v_mfma_f32_16x16x32_bf16 v[68:71], a[36:39], v[112:115], v[68:71]\n" \
    "s_add_u32 m0, s43, 0\n" \
    "v_mfma_f32_16x16x32_bf16 v[84:87], a[36:39], v[116:119], v[84:87]\n" \
    "buffer_load_dwordx4 v16, s[36:39], s40 offen offset:2048 sc1 lds\n" \
    "v_mfma_f32_16x16x32_bf16 v[72:75], a[40:43], v[112:115], v[72:75]\n" \
    "v_pk_max_i16 v168, v168, 0\n" \
    "v_pk_max_i16 v169, v169, 0\n" \
    "v_mfma_f32_16x16x32_bf16 v[88:91], a[40:43], v[116:119], v[88:91]\n" \
    "v_pk_max_i16 v170, v170, 0\n" \
    "v_pk_max_i16 v171, v171, 0\n" \
    "v_mfma_f32_16x16x32_bf16 v[76:79], a[44:47], v[112:115], v[76:79]\n" \
    "v_lshlrev_b32 v48, 16, v172\n" \
    "v_and_b32 v49, 0xffff0000, v172\n" \
    "v_mfma_f32_16x16x32_bf16 v[92:95], a[44:47], v[116:119], v[92:95]\n" \
    "v_lshlrev_b32 v50, 16, v173\n" \
    "v_and_b32 v51, 0xffff0000, v173\n" \
    "s_waitcnt lgkmcnt(2)\n" \
    "v_mfma_f32_16x16x32_bf16 v[64:67], a[48:51], v[120:123], v[64:67]\n" \
    "ds_read_b128 v[104:107], v21 offset:5120\n" \
    "v_mfma_f32_16x16x32_bf16 v[80:83], a[48:51], v[124:127], v[80:83]\n" \
    "ds_read_b128 v[108:111], v21 offset:25600\n" \
    "v_mfma_f32_16x16x32_bf16 v[68:71], a[52:55], v[120:123], v[68:71]\n" \
    "s_add_u32 m0, s43, 0\n" \
    "v_mfma_f32_16x16x32_bf16 v[84:87], a[52:55], v[124:127], v[84:87]\n" \
    "buffer_load_dwordx4 v16, s[36:39], s40 offen offset:3072 sc1 lds\n" \
    "v_mfma_f32_16x16x32_bf16 v[72:75], a[56:59], v[120:123], v[72:75]\n" \
    "v_lshlrev_b32 v52, 16, v174\n" \
    "v_and_b32 v53, 0xffff0000, v174\n" \
    "v_mfma_f32_16x16x32_bf16 v[88:91], a[56:59], v[124:127], v[88:91]\n" \
    "v_lshlrev_b32 v54, 16, v175\n" \
    "v_and_b32 v55, 0xffff0000, v175\n" \
    "v_mfma_f32_16x16x32_bf16 v[76:79], a[60:63], v[120:123], v[76:79]\n" \
    "v_lshlrev_b32 v32, 16, v168\n" \
    "v_and_b32 v33, 0xffff0000, v168\n" \
    "v_mfma_f32_16x16x32_bf16 v[92:95], a[60:63], v[124:127], v[92:95]\n" \
    "v_lshlrev_b32 v34, 16, v169\n" \
    "v_and_b32 v35, 0xffff0000, v169\n" \
    "s_waitcnt lgkmcnt(2)\n" \
    "v_mfma_f32_16x16x32_bf16 v[64:67], a[64:67], v[96:99], v[64:67]\n" \
    "ds_read_b128 v[112:115], v21 offset:6144\n" \
    "v_mfma_f32_16x16x32_bf16 v[80:83], a[64:67], v[100:103], v[80:83]\n" \
    "ds_read_b128 v[116:119], v21 offset:26624\n" \
    "v_mfma_f32_16x16x32_bf16 v[68:71], a[68:71], v[96:99], v[68:71]\n" \
    "s_add_u32 m0, s43, 4096\n" \
    "v_mfma_f32_16x16x32_bf16 v[84:87], a[68:71], v[100:103], v[84:87]\n" \
    "buffer_load_dwordx4 v16, s[36:39], s41 offen offset:0 sc1 lds\n" \
    "v_mfma_f32_16x16x32_bf16 v[72:75], a[72:75], v[96:99], v[72:75]\n" \
    "v_lshlrev_b32 v36, 16, v170\n" \
    "v_and_b32 v37, 0xffff0000, v170\n" \
    "v_mfma_f32_16x16x32_bf16 v[88:91], a[72:75], v[100:103], v[88:91]\n" \
    "v_lshlrev_b32 v38, 16, v171\n" \
    "v_and_b32 v39, 0xffff0000, v171\n" \
    "v_mfma_f32_16x16x32_bf16 v[76:79], a[76:79], v[96:99], v[76:79]\n" \
    "v_pk_mul_f32 v[32:33], v[32:33], v[48:49]\n" \
    "v_pk_mul_f32 v[34:35], v[34:35], v[50:51]\n" \
    "v_mfma_f32_16x16x32_bf16 v[92:95], a[76:79], v[100:103], v[92:95]\n" \
    "v_pk_mul_f32 v[36:37], v[36:37], v[52:53]\n" \
    "v_pk_mul_f32 v[38:39], v[38:39], v[54:55]\n" \
    "s_waitcnt lgkmcnt(2)\n" \
    "v_mfma_f32_16x16x32_bf16 v[64:67], a[80:83], v[104:107], v[64:67]\n" \
    "ds_read_b128 v[120:123], v21 offset:7168\n" \
    "v_mfma_f32_16x16x32_bf16 v[80:83], a[80:83], v[108:111], v[80:83]\n" \
    "ds_read_b128 v[124:127], v21 offset:27648\n" \
    "v_mfma_f32_16x16x32_bf16 v[68:71], a[84:87], v[104:107], v[68:71]\n" \
    "s_add_u32 m0, s43, 4096\n" \
    "v_mfma_f32_16x16x32_bf16 v[84:87], a[84:87], v[108:111], v[84:87]\n" \
    "buffer_load_dwordx4 v16, s[36:39], s41 offen offset:1024 sc1 lds\n" \
    "v_mfma_f32_16x16x32_bf16 v[72:75], a[88:91], v[104:107], v[72:75]\n" \
    "v_cvt_pk_bf16_f32 v160, v32, v33\n" \
    "v_cvt_pk_bf16_f32 v161, v34, v35\n" \
    "v_mfma_f32_16x16x32_bf16 v[88:91], a[88:91], v[108:111], v[88:91]\n" \
    "v_cvt_pk_bf16_f32 v162, v36, v37\n" \
    "v_cvt_pk_bf16_f32 v163, v38, v39\n" \
    "v_mfma_f32_16x16x32_bf16 v[76:79], a[92:95], v[104:107], v[76:79]\n" \
    "buffer_store_dwordx4 v[160:163], v18, s[36:39], s74 offen sc1\n" \
    "v_cvt_pk_bf16_f32 v168, v144, v145\n" \
    "v_mfma_f32_16x16x32_bf16 v[92:95], a[92:95], v[108:111], v[92:95]\n" \
    "v_cvt_pk_bf16_f32 v169, v146, v147\n" \
    "v_cvt_pk_bf16_f32 v170, v148, v149\n" \
    "s_waitcnt lgkmcnt(2)\n" \
    "v_mfma_f32_16x16x32_bf16 v[64:67], a[96:99], v[112:115], v[64:67]\n" \
    "ds_read_b128 v[96:99], v21 offset:8192\n" \
    "v_mfma_f32_16x16x32_bf16 v[80:83], a[96:99], v[116:119], v[80:83]\n" \
    "ds_read_b128 v[100:103], v21 offset:28672\n" \
    "v_mfma_f32_16x16x32_bf16 v[68:71], a[100:103], v[112:115], v[68:71]\n" \
    "s_add_u32 m0, s43, 4096\n" \
    "v_mfma_f32_16x16x32_bf16 v[84:87], a[100:103], v[116:119], v[84:87]\n" \
    "buffer_load_dwordx4 v16, s[36:39], s41 offen offset:2048 sc1 lds\n" \
    "v_mfma_f32_16x16x32_bf16 v[72:75], a[104:107], v[112:115], v[72:75]\n" \
    "v_cvt_pk_bf16_f32 v171, v150, v151\n" \
    "v_cvt_pk_bf16_f32 v172, v152, v153\n" \
    "v_mfma_f32_16x16x32_bf16 v[88:91], a[104:107], v[116:119], v[88:91]\n" \
    "v_cvt_pk_bf16_f32 v173, v154, v155\n" \
    "v_cvt_pk_bf16_f32 v174, v156, v157\n" \
    "v_mfma_f32_16x16x32_bf16 v[76:79], a[108:111], v[112:115], v[76:79]\n" \
    "v_cvt_pk_bf16_f32 v175, v158, v159\n" \
    "v_pk_max_i16 v168, v168, 0\n" \
    "v_mfma_f32_16x16x32_bf16 v[92:95], a[108:111], v[116:119], v[92:95]\n" \
    "v_pk_max_i16 v169, v169, 0\n" \
    "v_pk_max_i16 v170, v170, 0\n" \
    "s_waitcnt lgkmcnt(2)\n" \
    "v_mfma_f32_16x16x32_bf16 v[64:67], a[112:115], v[120:123], v[64:67]\n" \
    "ds_read_b128 v[104:107], v21 offset:9216\n" \
    "v_mfma_f32_16x16x32_bf16 v[80:83], a[112:115], v[124:127], v[80:83]\n" \
    "ds_read_b128 v[108:111], v21 offset:29696\n" \
    "v_mfma_f32_16x16x32_bf16 v[68:71], a[116:119], v[120:123], v[68:71]\n" \
    "s_add_u32 m0, s43, 4096\n" \
    "v_mfma_f32_16x16x32_bf16 v[84:87], a[116:119], v[124:127], v[84:87]\n" \
    "buffer_load_dwordx4 v16, s[36:39], s41 offen offset:3072 sc1 lds\n" \
    "v_mfma_f32_16x16x32_bf16 v[72:75], a[120:123], v[120:123], v[72:75]\n" \
    "v_pk_max_i16 v171, v171, 0\n" \
    "v_lshlrev_b32 v48, 16, v172\n" \
    "v_mfma_f32_16x16x32_bf16 v[88:91], a[120:123], v[124:127], v[88:91]\n" \
    "v_and_b32 v49, 0xffff0000, v172\n" \
    "v_lshlrev_b32 v50, 16, v173\n" \
    "v_mfma_f32_16x16x32_bf16 v[76:79], a[124:127], v[120:123], v[76:79]\n" \
    "v_and_b32 v51, 0xffff0000, v173\n" \
    "v_lshlrev_b32 v52, 16, v174\n" \
    "v_mfma_f32_16x16x32_bf16 v[92:95], a[124:127], v[124:127], v[92:95]\n" \
    "v_and_b32 v53, 0xffff0000, v174\n" \
    "v_lshlrev_b32 v54, 16, v175\n" \
    "s_waitcnt lgkmcnt(2)\n" \
    "v_mfma_f32_16x16x32_bf16 v[64:67], a[128:131], v[96:99], v[64:67]\n" \
    "ds_read_b128 v[112:115], v21 offset:10240\n" \
    "v_mfma_f32_16x16x32_bf16 v[80:83], a[128:131], v[100:103], v[80:83]\n" \
    "ds_read_b128 v[116:119], v21 offset:30720\n" \
    "v_mfma_f32_16x16x32_bf16 v[68:71], a[132:135], v[96:99], v[68:71]\n" \
    "s_add_u32 m0, s43, 8192\n" \
    "v_mfma_f32_16x16x32_bf16 v[84:87], a[132:135], v[100:103], v[84:87]\n" \
    "buffer_load_dwordx4 v16, s[36:39], s42 offen offset:0 sc1 lds\n" \
    "v_mfma_f32_16x16x32_bf16 v[72:75], a[136:139], v[96:99], v[72:75]\n" \
    "v_and_b32 v55, 0xffff0000, v175\n" \
    "v_lshlrev_b32 v32, 16, v168\n" \
    "v_mfma_f32_16x16x32_bf16 v[88:91], a[136:139], v[100:103], v[88:91]\n" \
    "v_and_b32 v33, 0xffff0000, v168\n" \
    "v_lshlrev_b32 v34, 16, v169\n" \
    "v_mfma_f32_16x16x32_bf16 v[76:79], a[140:143], v[96:99], v[76:79]\n" \
    "v_and_b32 v35, 0xffff0000, v169\n" \
    "v_lshlrev_b32 v36, 16, v170\n" \
    "v_mfma_f32_16x16x32_bf16 v[92:95], a[140:143], v[100:103], v[92:95]\n" \
    "v_and_b32 v37, 0xffff0000, v170\n" \
    "v_lshlrev_b32 v38, 16, v171\n" \
    "s_waitcnt lgkmcnt(2)\n" \
    "v_mfma_f32_16x16x32_bf16 v[64:67], a[144:147], v[104:107], v[64:67]\n" \
    "ds_read_b128 v[120:123], v21 offset:11264\n" \
    "v_mfma_f32_16x16x32_bf16 v[80:83], a[144:147], v[108:111], v[80:83]\n" \
    "ds_read_b128 v[124:127], v21 offset:31744\n" \
    "v_mfma_f32_16x16x32_bf16 v[68:71], a[148:151], v[104:107], v[68:71]\n" \
    "s_add_u32 m0, s43, 8192\n" \
    "v_mfma_f32_16x16x32_bf16 v[84:87], a[148:151], v[108:111], v[84:87]\n" \
    "buffer_load_dwordx4 v16, s[36:39], s42 offen offset:1024 sc1 lds\n" \
    "v_mfma_f32_16x16x32_bf16 v[72:75], a[152:155], v[104:107], v[72:75]\n" \
    "v_and_b32 v39, 0xffff0000, v171\n" \
    "v_pk_mul_f32 v[32:33], v[32:33], v[48:49]\n" \
    "v_mfma_f32_16x16x32_bf16 v[88:91], a[152:155], v[108:111], v[88:91]\n" \
    "v_pk_mul_f32 v[34:35], v[34:35], v[50:51]\n" \
    "v_pk_mul_f32 v[36:37], v[36:37], v[52:53]\n" \
    "v_mfma_f32_16x16x32_bf16 v[76:79], a[156:159], v[104:107], v[76:79]\n" \
    "v_pk_mul_f32 v[38:39], v[38:39], v[54:55]\n" \
    "v_cvt_pk_bf16_f32 v164, v32, v33\n" \
    "v_mfma_f32_16x16x32_bf16 v[92:95], a[156:159], v[108:111], v[92:95]\n" \
    "v_cvt_pk_bf16_f32 v165, v34, v35\n" \
    "v_cvt_pk_bf16_f32 v166, v36, v37\n" \
    "s_waitcnt lgkmcnt(2)\n" \
    "v_mfma_f32_16x16x32_bf16 v[64:67], a[160:163], v[112:115], v[64:67]\n" \
    "ds_read_b128 v[96:99], v21 offset:12288\n" \
    "v_mfma_f32_16x16x32_bf16 v[80:83], a[160:163], v[116:119], v[80:83]\n" \
    "ds_read_b128 v[100:103], v21 offset:32768\n" \
    "v_mfma_f32_16x16x32_bf16 v[68:71], a[164:167], v[112:115], v[68:71]\n" \
    "v_mfma_f32_16x16x32_bf16 v[84:87], a[164:167], v[116:119], v[84:87]\n" \
    "v_mfma_f32_16x16x32_bf16 v[72:75], a[168:171], v[112:115], v[72:75]\n" \
    "v_cvt_pk_bf16_f32 v167, v38, v39\n" \
    "v_mfma_f32_16x16x32_bf16 v[88:91], a[168:171], v[116:119], v[88:91]\n" \
    "s_add_u32 s74, s74, 55296\n" \
    "v_mfma_f32_16x16x32_bf16 v[76:79], a[172:175], v[112:115], v[76:79]\n" \
    "buffer_store_dwordx4 v[164:167], v18, s[36:39], s74 offen sc1\n" \
    "v_mfma_f32_16x16x32_bf16 v[92:95], a[172:175], v[116:119], v[92:95]\n" \
    "s_waitcnt lgkmcnt(2)\n" \
    "v_mfma_f32_16x16x32_bf16 v[64:67], a[176:179], v[120:123], v[64:67]\n" \
    "ds_read_b128 v[104:107], v21 offset:13312\n" \
    "v_mfma_f32_16x16x32_bf16 v[80:83], a[176:179], v[124:127], v[80:83]\n" \
    "ds_read_b128 v[108:111], v21 offset:33792\n" \
    "v_mfma_f32_16x16x32_bf16 v[68:71], a[180:183], v[120:123], v[68:71]\n" \
    "v_mfma_f32_16x16x32_bf16 v[84:87], a[180:183], v[124:127], v[84:87]\n" \
    "v_mfma_f32_16x16x32_bf16 v[72:75], a[184:187], v[120:123], v[72:75]\n" \
    "v_mfma_f32_16x16x32_bf16 v[88:91], a[184:187], v[124:127], v[88:91]\n" \
    "v_mfma_f32_16x16x32_bf16 v[76:79], a[188:191], v[120:123], v[76:79]\n" \
    "v_mfma_f32_16x16x32_bf16 v[92:95], a[188:191], v[124:127], v[92:95]\n" \
    "s_waitcnt lgkmcnt(2)\n" \
    "v_mfma_f32_16x16x32_bf16 v[64:67], a[192:195], v[96:99], v[64:67]\n" \
    "ds_read_b128 v[112:115], v21 offset:14336\n" \
    "v_mfma_f32_16x16x32_bf16 v[80:83], a[192:195], v[100:103], v[80:83]\n" \
    "ds_read_b128 v[116:119], v21 offset:34816\n" \
    "v_mfma_f32_16x16x32_bf16 v[68:71], a[196:199], v[96:99], v[68:71]\n" \
    "v_mfma_f32_16x16x32_bf16 v[84:87], a[196:199], v[100:103], v[84:87]\n" \
    "v_mfma_f32_16x16x32_bf16 v[72:75], a[200:203], v[96:99], v[72:75]\n" \
    "v_mfma_f32_16x16x32_bf16 v[88:91], a[200:203], v[100:103], v[88:91]\n" \
    "v_mfma_f32_16x16x32_bf16 v[76:79], a[204:207], v[96:99], v[76:79]\n" \
    "v_mfma_f32_16x16x32_bf16 v[92:95], a[204:207], v[100:103], v[92:95]\n" \
    "s_waitcnt lgkmcnt(2)\n" \
    "v_mfma_f32_16x16x32_bf16 v[64:67], a[208:211], v[104:107], v[64:67]\n" \
    "ds_read_b128 v[120:123], v21 offset:15360\n" \
    "v_mfma_f32_16x16x32_bf16 v[80:83], a[208:211], v[108:111], v[80:83]\n" \
    "ds_read_b128 v[124:127], v21 offset:35840\n" \
    "v_mfma_f32_16x16x32_bf16 v[68:71], a[212:215], v[104:107], v[68:71]\n" \
    "v_mfma_f32_16x16x32_bf16 v[84:87], a[212:215], v[108:111], v[84:87]\n" \
    "v_mfma_f32_16x16x32_bf16 v[72:75], a[216:219], v[104:107], v[72:75]\n" \
    "v_mfma_f32_16x16x32_bf16 v[88:91], a[216:219], v[108:111], v[88:91]\n" \
    "v_mfma_f32_16x16x32_bf16 v[76:79], a[220:223], v[104:107], v[76:79]\n" \
    "v_mfma_f32_16x16x32_bf16 v[92:95], a[220:223], v[108:111], v[92:95]\n" \
    "s_waitcnt lgkmcnt(2)\n" \
    "v_mfma_f32_16x16x32_bf16 v[64:67], a[224:227], v[112:115], v[64:67]\n" \
    "ds_read_b128 v[96:99], v21 offset:16384\n" \
    "v_mfma_f32_16x16x32_bf16 v[80:83], a[224:227], v[116:119], v[80:83]\n" \
    "ds_read_b128 v[100:103], v21 offset:36864\n" \
    "v_mfma_f32_16x16x32_bf16 v[68:71], a[228:231], v[112:115], v[68:71]\n" \
    "v_mfma_f32_16x16x32_bf16 v[84:87], a[228:231], v[116:119], v[84:87]\n" \
    "v_mfma_f32_16x16x32_bf16 v[72:75], a[232:235], v[112:115], v[72:75]\n" \
    "v_mfma_f32_16x16x32_bf16 v[88:91], a[232:235], v[116:119], v[88:91]\n" \
    "v_mfma_f32_16x16x32_bf16 v[76:79], a[236:239], v[112:115], v[76:79]\n" \
    "v_mfma_f32_16x16x32_bf16 v[92:95], a[236:239], v[116:119], v[92:95]\n" \
    "s_waitcnt lgkmcnt(2)\n" \
    "v_mfma_f32_16x16x32_bf16 v[64:67], a[240:243], v[120:123], v[64:67]\n" \
    "ds_read_b128 v[104:107], v21 offset:17408\n" \
    "v_mfma_f32_16x16x32_bf16 v[80:83], a[240:243], v[124:127], v[80:83]\n" \
    "ds_read_b128 v[108:111], v21 offset:37888\n" \
    "v_mfma_f32_16x16x32_bf16 v[68:71], a[244:247], v[120:123], v[68:71]\n" \
    "v_mfma_f32_16x16x32_bf16 v[84:87], a[244:247], v[124:127], v[84:87]\n" \
    "v_mfma_f32_16x16x32_bf16 v[72:75], a[248:251], v[120:123], v[72:75]\n" \
    "v_mfma_f32_16x16x32_bf16 v[88:91], a[248:251], v[124:127], v[88:91]\n" \
    "v_mfma_f32_16x16x32_bf16 v[76:79], a[252:255], v[120:123], v[76:79]\n" \
    "v_mfma_f32_16x16x32_bf16 v[92:95], a[252:255], v[124:127], v[92:95]\n" \
    "s_waitcnt lgkmcnt(2)\n" \
    "v_mfma_f32_16x16x32_bf16 v[64:67], v[192:195], v[96:99], v[64:67]\n" \
    "ds_read_b128 v[112:115], v21 offset:18432\n" \
    "v_mfma_f32_16x16x32_bf16 v[80:83], v[192:195], v[100:103], v[80:83]\n" \
    "ds_read_b128 v[116:119], v21 offset:38912\n" \
    "v_mfma_f32_16x16x32_bf16 v[68:71], v[196:199], v[96:99], v[68:71]\n" \
    "v_mfma_f32_16x16x32_bf16 v[84:87], v[196:199], v[100:103], v[84:87]\n" \
    "v_mfma_f32_16x16x32_bf16 v[72:75], v[200:203], v[96:99], v[72:75]\n" \
    "v_mov_b32 v22, s63\n" \
    "v_mov_b32 v23, s70\n" \
    "v_mfma_f32_16x16x32_bf16 v[88:91], v[200:203], v[100:103], v[88:91]\n" \
    "s_waitcnt vmcnt(0)\n" \
    "v_mfma_f32_16x16x32_bf16 v[76:79], v[204:207], v[96:99], v[76:79]\n" \
    "buffer_store_dwordx2 v[22:23], v17, s[36:39], 0 offen sc1\n" \
    "v_mfma_f32_16x16x32_bf16 v[92:95], v[204:207], v[100:103], v[92:95]\n" \
    "s_waitcnt lgkmcnt(2)\n" \
    "v_mfma_f32_16x16x32_bf16 v[64:67], v[208:211], v[104:107], v[64:67]\n" \
    "ds_read_b128 v[120:123], v21 offset:19456\n" \
    "v_mfma_f32_16x16x32_bf16 v[80:83], v[208:211], v[108:111], v[80:83]\n" \
    "ds_read_b128 v[124:127], v21 offset:39936\n" \
    "v_mfma_f32_16x16x32_bf16 v[68:71], v[212:215], v[104:107], v[68:71]\n" \
    "v_mfma_f32_16x16x32_bf16 v[84:87], v[212:215], v[108:111], v[84:87]\n" \
    "v_mfma_f32_16x16x32_bf16 v[72:75], v[216:219], v[104:107], v[72:75]\n" \
    "v_mfma_f32_16x16x32_bf16 v[88:91], v[216:219], v[108:111], v[88:91]\n" \
    "v_mfma_f32_16x16x32_bf16 v[76:79], v[220:223], v[104:107], v[76:79]\n" \
    "v_mfma_f32_16x16x32_bf16 v[92:95], v[220:223], v[108:111], v[92:95]\n" \
    "s_add_u32 s75, s72, s54\n" \
    "s_add_u32 s72, s72, 40960\n" \
    "s_cmp_eq_u32 s72, 81920\n" \
    "s_cselect_b32 s72, 114688, s72\n" \
    "s_cmp_eq_u32 s72, 155648\n" \
    "s_cselect_b32 s72, 0, s72\n" \
    "v_add_u32 v21, s72, v16\n" \
    "s_waitcnt lgkmcnt(0)\n" \
    "s_barrier\n" \
    "v_mfma_f32_16x16x32_bf16 v[64:67], v[224:227], v[112:115], v[64:67]\n" \
    "ds_read_b128 v[96:99], v21 offset:0\n" \
    "v_mfma_f32_16x16x32_bf16 v[80:83], v[224:227], v[116:119], v[80:83]\n" \
    "ds_read_b128 v[100:103], v21 offset:20480\n" \
    "v_mfma_f32_16x16x32_bf16 v[68:71], v[228:231], v[112:115], v[68:71]\n" \
    "v_mfma_f32_16x16x32_bf16 v[84:87], v[228:231], v[116:119], v[84:87]\n" \
    "v_mfma_f32_16x16x32_bf16 v[72:75], v[232:235], v[112:115], v[72:75]\n" \
    "v_mfma_f32_16x16x32_bf16 v[88:91], v[232:235], v[116:119], v[88:91]\n" \
    "v_mfma_f32_16x16x32_bf16 v[76:79], v[236:239], v[112:115], v[76:79]\n" \
    "v_mfma_f32_16x16x32_bf16 v[92:95], v[236:239], v[116:119], v[92:95]\n" \
    "v_mfma_f32_16x16x32_bf16 v[64:67], v[240:243], v[120:123], v[64:67]\n" \
    "ds_read_b128 v[104:107], v21 offset:1024\n" \
    "v_mfma_f32_16x16x32_bf16 v[80:83], v[240:243], v[124:127], v[80:83]\n" \
    "ds_read_b128 v[108:111], v21 offset:21504\n" \
    "v_mfma_f32_16x16x32_bf16 v[68:71], v[244:247], v[120:123], v[68:71]\n" \
    "v_mfma_f32_16x16x32_bf16 v[84:87], v[244:247], v[124:127], v[84:87]\n" \
    "v_mfma_f32_16x16x32_bf16 v[72:75], v[248:251], v[120:123], v[72:75]\n" \
    "v_mfma_f32_16x16x32_bf16 v[88:91], v[248:251], v[124:127], v[88:91]\n" \
    "v_mfma_f32_16x16x32_bf16 v[76:79], v[252:255], v[120:123], v[76:79]\n" \
    "v_mfma_f32_16x16x32_bf16 v[92:95], v[252:255], v[124:127], v[92:95]\n" \
    "s_branch P2_loop_%=\n" \
    "P2_fin_%=:\n" \
    "s_mul_i32 s68, s70, 110592\n" \
    "s_add_u32 s74, s68, s53\n" \
    "s_add_u32 s70, s70, 1\n" \
    "s_add_u32 s68, s70, 2\n" \
    "s_min_u32 s68, s68, 63\n" \
    "s_mul_i32 s68, s68, 40960\n" \
    "s_add_u32 s40, s68, s54\n" \
    "s_add_u32 s40, s40, 2097152\n" \
    "s_add_u32 s41, s40, 4096\n" \
    "s_add_u32 s42, s40, 8192\n" \
    "s_mov_b32 s43, s75\n" \
    "s_nop 7\n" \
    "v_cvt_pk_bf16_f32 v168, v128, v129\n" \
    "v_cvt_pk_bf16_f32 v169, v130, v131\n" \
    "v_cvt_pk_bf16_f32 v170, v132, v133\n" \
    "v_cvt_pk_bf16_f32 v171, v134, v135\n" \
    "v_cvt_pk_bf16_f32 v172, v136, v137\n" \
    "v_cvt_pk_bf16_f32 v173, v138, v139\n" \
    "v_cvt_pk_bf16_f32 v174, v140, v141\n" \
    "v_cvt_pk_bf16_f32 v175, v142, v143\n" \
    "v_pk_max_i16 v168, v168, 0\n" \
    "v_pk_max_i16 v169, v169, 0\n" \
    "v_pk_max_i16 v170, v170, 0\n" \
    "v_pk_max_i16 v171, v171, 0\n" \
    "v_lshlrev_b32 v48, 16, v172\n" \
    "v_and_b32 v49, 0xffff0000, v172\n" \
    "v_lshlrev_b32 v50, 16, v173\n" \
    "v_and_b32 v51, 0xffff0000, v173\n" \
    "v_lshlrev_b32 v52, 16, v174\n" \
    "v_and_b32 v53, 0xffff0000, v174\n" \
    "v_lshlrev_b32 v54, 16, v175\n" \
    "v_and_b32 v55, 0xffff0000, v175\n" \
    "v_lshlrev_b32 v32, 16, v168\n" \
    "v_and_b32 v33, 0xffff0000, v168\n" \
    "v_lshlrev_b32 v34, 16, v169\n" \
    "v_and_b32 v35, 0xffff0000, v169\n" \
    "v_lshlrev_b32 v36, 16, v170\n" \
    "v_and_b32 v37, 0xffff0000, v170\n" \
    "v_lshlrev_b32 v38, 16, v171\n" \
    "v_and_b32 v39, 0xffff0000, v171\n" \
    "v_pk_mul_f32 v[32:33], v[32:33], v[48:49]\n" \
    "v_pk_mul_f32 v[34:35], v[34:35], v[50:51]\n" \
    "v_pk_mul_f32 v[36:37], v[36:37], v[52:53]\n" \
    "v_pk_mul_f32 v[38:39], v[38:39], v[54:55]\n" \
    "v_cvt_pk_bf16_f32 v160, v32, v33\n" \
    "v_cvt_pk_bf16_f32 v161, v34, v35\n" \
    "v_cvt_pk_bf16_f32 v162, v36, v37\n" \
    "v_cvt_pk_bf16_f32 v163, v38, v39\n" \
    "buffer_store_dwordx4 v[160:163], v18, s[36:39], s74 offen sc1\n" \
    "v_cvt_pk_bf16_f32 v168, v144, v145\n" \
    "v_cvt_pk_bf16_f32 v169, v146, v147\n" \
    "v_cvt_pk_bf16_f32 v170, v148, v149\n" \
    "v_cvt_pk_bf16_f32 v171, v150, v151\n" \
    "v_cvt_pk_bf16_f32 v172, v152, v153\n" \
    "v_cvt_pk_bf16_f32 v173, v154, v155\n" \
    "v_cvt_pk_bf16_f32 v174, v156, v157\n" \
    "v_cvt_pk_bf16_f32 v175, v158, v159\n" \
    "v_pk_max_i16 v168, v168, 0\n" \
    "v_pk_max_i16 v169, v169, 0\n" \
    "v_pk_max_i16 v170, v170, 0\n" \
    "v_pk_max_i16 v171, v171, 0\n" \
    "v_lshlrev_b32 v48, 16, v172\n" \
    "v_and_b32 v49, 0xffff0000, v172\n" \
    "v_lshlrev_b32 v50, 16, v173\n" \
    "v_and_b32 v51, 0xffff0000, v173\n" \
    "v_lshlrev_b32 v52, 16, v174\n" \
    "v_and_b32 v53, 0xffff0000, v174\n" \
    "v_lshlrev_b32 v54, 16, v175\n" \
    "v_and_b32 v55, 0xffff0000, v175\n" \
    "v_lshlrev_b32 v32, 16, v168\n" \
    "v_and_b32 v33, 0xffff0000, v168\n" \
    "v_lshlrev_b32 v34, 16, v169\n" \
    "v_and_b32 v35, 0xffff0000, v169\n" \
    "v_lshlrev_b32 v36, 16, v170\n" \
    "v_and_b32 v37, 0xffff0000, v170\n" \
    "v_lshlrev_b32 v38, 16, v171\n" \
    "v_and_b32 v39, 0xffff0000, v171\n" \
    "v_pk_mul_f32 v[32:33], v[32:33], v[48:49]\n" \
    "v_pk_mul_f32 v[34:35], v[34:35], v[50:51]\n" \
    "v_pk_mul_f32 v[36:37], v[36:37], v[52:53]\n" \
    "v_pk_mul_f32 v[38:39], v[38:39], v[54:55]\n" \
    "v_cvt_pk_bf16_f32 v164, v32, v33\n" \
    "v_cvt_pk_bf16_f32 v165, v34, v35\n" \
    "v_cvt_pk_bf16_f32 v166, v36, v37\n" \
    "v_cvt_pk_bf16_f32 v167, v38, v39\n" \
    "s_add_u32 s74, s74, 55296\n" \
    "buffer_store_dwordx4 v[164:167], v18, s[36:39], s74 offen sc1\n" \
    "v_mov_b32 v22, s63\n" \
    "v_mov_b32 v23, 64\n" \
    "s_waitcnt vmcnt(0)\n" \
    "buffer_store_dwordx2 v[22:23], v17, s[36:39], 0 offen sc1\n" \
    "s_endpgm\n" \
    "P2_ln_no_%=:\n" \
    "s_add_u32 s82, s82, 1\n" \
    "s_and_b32 s85, s82, 63\n" \
    "s_cmp_lg_u32 s85, 0\n" \
    "s_cbranch_scc1 P2_g0_ok_%=\n" \
    "s_mov_b32 s85, 1048832\n" \
    "buffer_load_dword v22, off, s[36:39], s85 sc1\n" \
    "s_waitcnt vmcnt(0)\n" \
    "v_readfirstlane_b32 s85, v22\n" \
    "s_cmp_eq_u32 s85, s63\n" \
    "s_cbranch_scc1 P2_end_%=\n" \
    "s_memrealtime s[80:81]\n" \
    "s_waitcnt lgkmcnt(0)\n" \
    "s_sub_u32 s85, s80, s83\n" \
    "s_cmp_gt_u32 s85, 3000000\n" \
    "s_cbranch_scc1 P2_giveup_%=\n" \
    "P2_g0_ok_%=:\n" \
    "s_sleep 1\n" \
    "s_branch P2_ln_%=\n" \
    "P2_la_no_%=:\n" \
    "s_add_u32 s82, s82, 1\n" \
    "s_and_b32 s85, s82, 63\n" \
    "s_cmp_lg_u32 s85, 0\n" \
    "s_cbranch_scc1 P2_g1_ok_%=\n" \
    "s_mov_b32 s85, 1048832\n" \
    "buffer_load_dword v22, off, s[36:39], s85 sc1\n" \
    "s_waitcnt vmcnt(0)\n" \
    "v_readfirstlane_b32 s85, v22\n" \
    "s_cmp_eq_u32 s85, s63\n" \
    "s_cbranch_scc1 P2_end_%=\n" \
    "s_memrealtime s[80:81]\n" \
    "s_waitcnt lgkmcnt(0)\n" \
    "s_sub_u32 s85, s80, s83\n" \
    "s_cmp_gt_u32 s85, 3000000\n" \
    "s_cbranch_scc1 P2_giveup_%=\n" \
    "P2_g1_ok_%=:\n" \
    "s_sleep 1\n" \
    "s_branch P2_la_%=\n" \
    "P2_giveup_%=:\n" \
    "s_mov_b32 s85, 1048832\n" \
    "v_mov_b32 v22, s63\n" \
    "buffer_store_dword v22, off, s[36:39], s85 sc1\n" \
    "P2_end_%=:\n" \
    "s_endpgm\n"

#define OUT_ASM \
    "s_nop 4\n" \
    "s_mov_b32 s88, s58\n" \
    "s_mov_b32 s89, s59\n" \
    "s_mov_b32 s90, s50\n" \
    "s_mov_b32 s91, s51\n" \
    "s_add_u32 s68, s53, 0\n" \
    "s_add_u32 m0, s52, 0\n" \
    "s_nop 0\n" \
    "buffer_load_dwordx4 v0, s[44:47], s68 offen lds\n" \
    "s_add_u32 m0, s52, 1024\n" \
    "s_nop 0\n" \
    "buffer_load_dwordx4 v1, s[44:47], s68 offen lds\n" \
    "s_add_u32 s68, s53, 163840\n" \
    "s_add_u32 m0, s52, 2048\n" \
    "s_nop 0\n" \
    "buffer_load_dwordx4 v0, s[44:47], s68 offen lds\n" \
    "s_add_u32 m0, s52, 3072\n" \
    "s_nop 0\n" \
    "buffer_load_dwordx4 v1, s[44:47], s68 offen lds\n" \
    "s_add_u32 s68, s53, 327680\n" \
    "s_add_u32 m0, s52, 4096\n" \
    "s_nop 0\n" \
    "buffer_load_dwordx4 v0, s[44:47], s68 offen lds\n" \
    "s_add_u32 m0, s52, 5120\n" \
    "s_nop 0\n" \
    "buffer_load_dwordx4 v1, s[44:47], s68 offen lds\n" \
    "s_add_u32 s68, s53, 491520\n" \
    "s_add_u32 m0, s52, 6144\n" \
    "s_nop 0\n" \
    "buffer_load_dwordx4 v0, s[44:47], s68 offen lds\n" \
    "s_add_u32 m0, s52, 7168\n" \
    "s_nop 0\n" \
    "buffer_load_dwordx4 v1, s[44:47], s68 offen lds\n" \
    "s_add_u32 s68, s53, 655360\n" \
    "s_add_u32 m0, s52, 8192\n" \
    "s_nop 0\n" \
    "buffer_load_dwordx4 v0, s[44:47], s68 offen lds\n" \
    "s_add_u32 m0, s52, 9216\n" \
    "s_nop 0\n" \
    "buffer_load_dwordx4 v1, s[44:47], s68 offen lds\n" \
    "s_add_u32 s68, s53, 819200\n" \
    "s_add_u32 m0, s52, 10240\n" \
    "s_nop 0\n" \
    "buffer_load_dwordx4 v0, s[44:47], s68 offen lds\n" \
    "s_add_u32 m0, s52, 11264\n" \
    "s_nop 0\n" \
    "buffer_load_dwordx4 v1, s[44:47], s68 offen lds\n" \
    "s_add_u32 s68, s53, 983040\n" \
    "s_add_u32 m0, s52, 12288\n" \
    "s_nop 0\n" \
    "buffer_load_dwordx4 v0, s[44:47], s68 offen lds\n" \
    "s_add_u32 m0, s52, 13312\n" \
    "s_nop 0\n" \
    "buffer_load_dwordx4 v1, s[44:47], s68 offen lds\n" \
    "s_add_u32 s68, s53, 1146880\n" \
    "s_add_u32 m0, s52, 14336\n" \
    "s_nop 0\n" \
    "buffer_load_dwordx4 v0, s[44:47], s68 offen lds\n" \
    "s_add_u32 m0, s52, 15360\n" \
    "s_nop 0\n" \
    "buffer_load_dwordx4 v1, s[44:47], s68 offen lds\n" \
    "s_add_u32 s68, s53, 1310720\n" \
    "s_add_u32 m0, s52, 16384\n" \
    "s_nop 0\n" \
    "buffer_load_dwordx4 v0, s[44:47], s68 offen lds\n" \
    "s_add_u32 m0, s52, 17408\n" \
    "s_nop 0\n" \
    "buffer_load_dwordx4 v1, s[44:47], s68 offen lds\n" \
    "s_add_u32 s68, s53, 1474560\n" \
    "s_add_u32 m0, s52, 18432\n" \
    "s_nop 0\n" \
    "buffer_load_dwordx4 v0, s[44:47], s68 offen lds\n" \
    "s_add_u32 m0, s52, 19456\n" \
    "s_nop 0\n" \
    "buffer_load_dwordx4 v1, s[44:47], s68 offen lds\n" \
    "s_add_u32 s68, s53, 1638400\n" \
    "s_add_u32 m0, s52, 20480\n" \
    "s_nop 0\n" \
    "buffer_load_dwordx4 v0, s[44:47], s68 offen lds\n" \
    "s_add_u32 m0, s52, 21504\n" \
    "s_nop 0\n" \
    "buffer_load_dwordx4 v1, s[44:47], s68 offen lds\n" \
    "s_add_u32 s68, s53, 1802240\n" \
    "s_add_u32 m0, s52, 22528\n" \
    "s_nop 0\n" \
    "buffer_load_dwordx4 v0, s[44:47], s68 offen lds\n" \
    "s_add_u32 m0, s52, 23552\n" \
    "s_nop 0\n" \
    "buffer_load_dwordx4 v1, s[44:47], s68 offen lds\n" \
    "s_add_u32 s68, s53, 1966080\n" \
    "s_add_u32 m0, s52, 24576\n" \
    "s_nop 0\n" \
    "buffer_load_dwordx4 v0, s[44:47], s68 offen lds\n" \
    "s_add_u32 m0, s52, 25600\n" \
    "s_nop 0\n" \
    "buffer_load_dwordx4 v1, s[44:47], s68 offen lds\n" \
    "s_add_u32 s68, s53, 2129920\n" \
    "s_add_u32 m0, s52, 26624\n" \
    "s_nop 0\n" \
    "buffer_load_dwordx4 v0, s[44:47], s68 offen lds\n" \
    "s_add_u32 m0, s52, 27648\n" \
    "s_nop 0\n" \
    "buffer_load_dwordx4 v1, s[44:47], s68 offen lds\n" \
    "s_add_u32 s68, s54, 0\n" \
    "s_add_u32 m0, s52, 28672\n" \
    "s_nop 0\n" \
    "buffer_load_dwordx4 v0, s[40:43], s68 offen lds\n" \
    "s_add_u32 m0, s52, 29696\n" \
    "s_nop 0\n" \
    "buffer_load_dwordx4 v1, s[40:43], s68 offen lds\n" \
    "s_add_u32 s68, s54, 40960\n" \
    "s_add_u32 m0, s52, 30720\n" \
    "s_nop 0\n" \
    "buffer_load_dwordx4 v0, s[40:43], s68 offen lds\n" \
    "s_add_u32 m0, s52, 31744\n" \
    "s_nop 0\n" \
    "buffer_load_dwordx4 v1, s[40:43], s68 offen lds\n" \
    "s_waitcnt vmcnt(30)\n" \
    "ds_read_b64_tr_b16 v[144:145], v4 offset:0\n" \
    "ds_read_b64_tr_b16 v[146:147], v5 offset:0\n" \
    "ds_read_b64_tr_b16 v[148:149], v6 offset:0\n" \
    "ds_read_b64_tr_b16 v[150:151], v7 offset:0\n" \
    "s_waitcnt lgkmcnt(0)\n" \
    "s_add_u32 s68, s54, 81920\n" \
    "s_add_u32 m0, s52, 0\n" \
    "s_nop 0\n" \
    "buffer_load_dwordx4 v0, s[40:43], s68 offen lds\n" \
    "s_add_u32 m0, s52, 1024\n" \
    "s_nop 0\n" \
    "buffer_load_dwordx4 v1, s[40:43], s68 offen lds\n" \
    "s_waitcnt vmcnt(30)\n" \
    "ds_read_b64_tr_b16 v[152:153], v4 offset:2048\n" \
    "ds_read_b64_tr_b16 v[154:155], v5 offset:2048\n" \
    "ds_read_b64_tr_b16 v[156:157], v6 offset:2048\n" \
    "ds_read_b64_tr_b16 v[158:159], v7 offset:2048\n" \
    "s_waitcnt lgkmcnt(0)\n" \
    "s_add_u32 s68, s54, 122880\n" \
    "s_add_u32 m0, s52, 2048\n" \
    "s_nop 0\n" \
    "buffer_load_dwordx4 v0, s[40:43], s68 offen lds\n" \
    "s_add_u32 m0, s52, 3072\n" \
    "s_nop 0\n" \
    "buffer_load_dwordx4 v1, s[40:43], s68 offen lds\n" \
    "s_waitcnt vmcnt(30)\n" \
    "ds_read_b64_tr_b16 v[160:161], v4 offset:4096\n" \
    "ds_read_b64_tr_b16 v[162:163], v5 offset:4096\n" \
    "ds_read_b64_tr_b16 v[164:165], v6 offset:4096\n" \
    "ds_read_b64_tr_b16 v[166:167], v7 offset:4096\n" \
    "s_waitcnt lgkmcnt(0)\n" \
    "s_add_u32 s68, s54, 163840\n" \
    "s_add_u32 m0, s52, 4096\n" \
    "s_nop 0\n" \
    "buffer_load_dwordx4 v0, s[40:43], s68 offen lds\n" \
    "s_add_u32 m0, s52, 5120\n" \
    "s_nop 0\n" \
    "buffer_load_dwordx4 v1, s[40:43], s68 offen lds\n" \
    "s_waitcnt vmcnt(30)\n" \
    "ds_read_b64_tr_b16 v[168:169], v4 offset:6144\n" \
    "ds_read_b64_tr_b16 v[170:171], v5 offset:6144\n" \
    "ds_read_b64_tr_b16 v[172:173], v6 offset:6144\n" \
    "ds_read_b64_tr_b16 v[174:175], v7 offset:6144\n" \
    "s_waitcnt lgkmcnt(0)\n" \
    "s_add_u32 s68, s54, 204800\n" \
    "s_add_u32 m0, s52, 6144\n" \
    "s_nop 0\n" \
    "buffer_load_dwordx4 v0, s[40:43], s68 offen lds\n" \
    "s_add_u32 m0, s52, 7168\n" \
    "s_nop 0\n" \
    "buffer_load_dwordx4 v1, s[40:43], s68 offen lds\n" \
    "s_waitcnt vmcnt(30)\n" \
    "ds_read_b64_tr_b16 v[176:177], v4 offset:8192\n" \
    "ds_read_b64_tr_b16 v[178:179], v5 offset:8192\n" \
    "ds_read_b64_tr_b16 v[180:181], v6 offset:8192\n" \
    "ds_read_b64_tr_b16 v[182:183], v7 offset:8192\n" \
    "s_waitcnt lgkmcnt(0)\n" \
    "s_add_u32 s68, s54, 245760\n" \
    "s_add_u32 m0, s52, 8192\n" \
    "s_nop 0\n" \
    "buffer_load_dwordx4 v0, s[40:43], s68 offen lds\n" \
    "s_add_u32 m0, s52, 9216\n" \
    "s_nop 0\n" \
    "buffer_load_dwordx4 v1, s[40:43], s68 offen lds\n" \
    "s_waitcnt vmcnt(30)\n" \
    "ds_read_b64_tr_b16 v[184:185], v4 offset:10240\n" \
    "ds_read_b64_tr_b16 v[186:187], v5 offset:10240\n" \
    "ds_read_b64_tr_b16 v[188:189], v6 offset:10240\n" \
    "ds_read_b64_tr_b16 v[190:191], v7 offset:10240\n" \
    "s_waitcnt lgkmcnt(0)\n" \
    "s_add_u32 s68, s54, 286720\n" \
    "s_add_u32 m0, s52, 10240\n" \
    "s_nop 0\n" \
    "buffer_load_dwordx4 v0, s[40:43], s68 offen lds\n" \
    "s_add_u32 m0, s52, 11264\n" \
    "s_nop 0\n" \
    "buffer_load_dwordx4 v1, s[40:43], s68 offen lds\n" \
    "s_waitcnt vmcnt(30)\n" \
    "ds_read_b64_tr_b16 v[192:193], v4 offset:12288\n" \
    "ds_read_b64_tr_b16 v[194:195], v5 offset:12288\n" \
    "ds_read_b64_tr_b16 v[196:197], v6 offset:12288\n" \
    "ds_read_b64_tr_b16 v[198:199], v7 offset:12288\n" \
    "s_waitcnt lgkmcnt(0)\n" \
    "s_add_u32 s68, s54, 327680\n" \
    "s_add_u32 m0, s52, 12288\n" \
    "s_nop 0\n" \
    "buffer_load_dwordx4 v0, s[40:43], s68 offen lds\n" \
    "s_add_u32 m0, s52, 13312\n" \
    "s_nop 0\n" \
    "buffer_load_dwordx4 v1, s[40:43], s68 offen lds\n" \
    "s_waitcnt vmcnt(30)\n" \
    "ds_read_b64_tr_b16 v[200:201], v4 offset:14336\n" \
    "ds_read_b64_tr_b16 v[202:203], v5 offset:14336\n" \
    "ds_read_b64_tr_b16 v[204:205], v6 offset:14336\n" \
    "ds_read_b64_tr_b16 v[206:207], v7 offset:14336\n" \
    "s_waitcnt lgkmcnt(0)\n" \
    "s_add_u32 s68, s54, 368640\n" \
    "s_add_u32 m0, s52, 14336\n" \
    "s_nop 0\n" \
    "buffer_load_dwordx4 v0, s[40:43], s68 offen lds\n" \
    "s_add_u32 m0, s52, 15360\n" \
    "s_nop 0\n" \
    "buffer_load_dwordx4 v1, s[40:43], s68 offen lds\n" \
    "s_waitcnt vmcnt(30)\n" \
    "ds_read_b64_tr_b16 v[208:209], v4 offset:16384\n" \
    "ds_read_b64_tr_b16 v[210:211], v5 offset:16384\n" \
    "ds_read_b64_tr_b16 v[212:213], v6 offset:16384\n" \
    "ds_read_b64_tr_b16 v[214:215], v7 offset:16384\n" \
    "s_waitcnt lgkmcnt(0)\n" \
    "s_add_u32 s68, s54, 409600\n" \
    "s_add_u32 m0, s52, 16384\n" \
    "s_nop 0\n" \
    "buffer_load_dwordx4 v0, s[40:43], s68 offen lds\n" \
    "s_add_u32 m0, s52, 17408\n" \
    "s_nop 0\n" \
    "buffer_load_dwordx4 v1, s[40:43], s68 offen lds\n" \
    "s_waitcnt vmcnt(30)\n" \
    "ds_read_b64_tr_b16 v[216:217], v4 offset:18432\n" \
    "ds_read_b64_tr_b16 v[218:219], v5 offset:18432\n" \
    "ds_read_b64_tr_b16 v[220:221], v6 offset:18432\n" \
    "ds_read_b64_tr_b16 v[222:223], v7 offset:18432\n" \
    "s_waitcnt lgkmcnt(0)\n" \
    "s_add_u32 s68, s54, 450560\n" \
    "s_add_u32 m0, s52, 18432\n" \
    "s_nop 0\n" \
    "buffer_load_dwordx4 v0, s[40:43], s68 offen lds\n" \
    "s_add_u32 m0, s52, 19456\n" \
    "s_nop 0\n" \
    "buffer_load_dwordx4 v1, s[40:43], s68 offen lds\n" \
    "s_waitcnt vmcnt(30)\n" \
    "ds_read_b64_tr_b16 v[224:225], v4 offset:20480\n" \
    "ds_read_b64_tr_b16 v[226:227], v5 offset:20480\n" \
    "ds_read_b64_tr_b16 v[228:229], v6 offset:20480\n" \
    "ds_read_b64_tr_b16 v[230:231], v7 offset:20480\n" \
    "s_waitcnt lgkmcnt(0)\n" \
    "s_add_u32 s68, s54, 491520\n" \
    "s_add_u32 m0, s52, 20480\n" \
    "s_nop 0\n" \
    "buffer_load_dwordx4 v0, s[40:43], s68 offen lds\n" \
    "s_add_u32 m0, s52, 21504\n" \
    "s_nop 0\n" \
    "buffer_load_dwordx4 v1, s[40:43], s68 offen lds\n" \
    "s_waitcnt vmcnt(30)\n" \
    "ds_read_b64_tr_b16 v[232:233], v4 offset:22528\n" \
    "ds_read_b64_tr_b16 v[234:235], v5 offset:22528\n" \
    "ds_read_b64_tr_b16 v[236:237], v6 offset:22528\n" \
    "ds_read_b64_tr_b16 v[238:239], v7 offset:22528\n" \
    "s_waitcnt lgkmcnt(0)\n" \
    "s_add_u32 s68, s54, 532480\n" \
    "s_add_u32 m0, s52, 22528\n" \
    "s_nop 0\n" \
    "buffer_load_dwordx4 v0, s[40:43], s68 offen lds\n" \
    "s_add_u32 m0, s52, 23552\n" \
    "s_nop 0\n" \
    "buffer_load_dwordx4 v1, s[40:43], s68 offen lds\n" \
    "s_waitcnt vmcnt(30)\n" \
    "ds_read_b64_tr_b16 v[240:241], v4 offset:24576\n" \
    "ds_read_b64_tr_b16 v[242:243], v5 offset:24576\n" \
    "ds_read_b64_tr_b16 v[244:245], v6 offset:24576\n" \
    "ds_read_b64_tr_b16 v[246:247], v7 offset:24576\n" \
    "s_waitcnt lgkmcnt(0)\n" \
    "s_add_u32 s68, s54, 573440\n" \
    "s_add_u32 m0, s52, 24576\n" \
    "s_nop 0\n" \
    "buffer_load_dwordx4 v0, s[40:43], s68 offen lds\n" \
    "s_add_u32 m0, s52, 25600\n" \
    "s_nop 0\n" \
    "buffer_load_dwordx4 v1, s[40:43], s68 offen lds\n" \
    "s_waitcnt vmcnt(30)\n" \
    "ds_read_b64_tr_b16 v[248:249], v4 offset:26624\n" \
    "ds_read_b64_tr_b16 v[250:251], v5 offset:26624\n" \
    "ds_read_b64_tr_b16 v[252:253], v6 offset:26624\n" \
    "ds_read_b64_tr_b16 v[254:255], v7 offset:26624\n" \
    "s_waitcnt lgkmcnt(0)\n" \
    "s_add_u32 s68, s54, 614400\n" \
    "s_add_u32 m0, s52, 26624\n" \
    "s_nop 0\n" \
    "buffer_load_dwordx4 v0, s[40:43], s68 offen lds\n" \
    "s_add_u32 m0, s52, 27648\n" \
    "s_nop 0\n" \
    "buffer_load_dwordx4 v1, s[40:43], s68 offen lds\n" \
    "s_waitcnt vmcnt(30)\n" \
    "ds_read_b64_tr_b16 a[0:1], v4 offset:28672\n" \
    "ds_read_b64_tr_b16 a[2:3], v5 offset:28672\n" \
    "ds_read_b64_tr_b16 a[4:5], v6 offset:28672\n" \
    "ds_read_b64_tr_b16 a[6:7], v7 offset:28672\n" \
    "s_waitcnt lgkmcnt(0)\n" \
    "s_add_u32 s68, s54, 655360\n" \
    "s_add_u32 m0, s52, 28672\n" \
    "s_nop 0\n" \
    "buffer_load_dwordx4 v0, s[40:43], s68 offen lds\n" \
    "s_add_u32 m0, s52, 29696\n" \
    "s_nop 0\n" \
    "buffer_load_dwordx4 v1, s[40:43], s68 offen lds\n" \
    "s_waitcnt vmcnt(30)\n" \
    "ds_read_b64_tr_b16 a[8:9], v4 offset:30720\n" \
    "ds_read_b64_tr_b16 a[10:11], v5 offset:30720\n" \
    "ds_read_b64_tr_b16 a[12:13], v6 offset:30720\n" \
    "ds_read_b64_tr_b16 a[14:15], v7 offset:30720\n" \
    "s_waitcnt lgkmcnt(0)\n" \
    "s_add_u32 s68, s54, 696320\n" \
    "s_add_u32 m0, s52, 30720\n" \
    "s_nop 0\n" \
    "buffer_load_dwordx4 v0, s[40:43], s68 offen lds\n" \
    "s_add_u32 m0, s52, 31744\n" \
    "s_nop 0\n" \
    "buffer_load_dwordx4 v1, s[40:43], s68 offen lds\n" \
    "s_waitcnt vmcnt(30)\n" \
    "ds_read_b64_tr_b16 a[16:17], v4 offset:0\n" \
    "ds_read_b64_tr_b16 a[18:19], v5 offset:0\n" \
    "ds_read_b64_tr_b16 a[20:21], v6 offset:0\n" \
    "ds_read_b64_tr_b16 a[22:23], v7 offset:0\n" \
    "s_waitcnt lgkmcnt(0)\n" \
    "s_add_u32 s68, s54, 737280\n" \
    "s_add_u32 m0, s52, 0\n" \
    "s_nop 0\n" \
    "buffer_load_dwordx4 v0, s[40:43], s68 offen lds\n" \
    "s_add_u32 m0, s52, 1024\n" \
    "s_nop 0\n" \
    "buffer_load_dwordx4 v1, s[40:43], s68 offen lds\n" \
    "s_waitcnt vmcnt(30)\n" \
    "ds_read_b64_tr_b16 a[24:25], v4 offset:2048\n" \
    "ds_read_b64_tr_b16 a[26:27], v5 offset:2048\n" \
    "ds_read_b64_tr_b16 a[28:29], v6 offset:2048\n" \
    "ds_read_b64_tr_b16 a[30:31], v7 offset:2048\n" \
    "s_waitcnt lgkmcnt(0)\n" \
    "s_add_u32 s68, s54, 778240\n" \
    "s_add_u32 m0, s52, 2048\n" \
    "s_nop 0\n" \
    "buffer_load_dwordx4 v0, s[40:43], s68 offen lds\n" \
    "s_add_u32 m0, s52, 3072\n" \
    "s_nop 0\n" \
    "buffer_load_dwordx4 v1, s[40:43], s68 offen lds\n" \
    "s_waitcnt vmcnt(30)\n" \
    "ds_read_b64_tr_b16 a[32:33], v4 offset:4096\n" \
    "ds_read_b64_tr_b16 a[34:35], v5 offset:4096\n" \
    "ds_read_b64_tr_b16 a[36:37], v6 offset:4096\n" \
    "ds_read_b64_tr_b16 a[38:39], v7 offset:4096\n" \
    "s_waitcnt lgkmcnt(0)\n" \
    "s_add_u32 s68, s54, 819200\n" \
    "s_add_u32 m0, s52, 4096\n" \
    "s_nop 0\n" \
    "buffer_load_dwordx4 v0, s[40:43], s68 offen lds\n" \
    "s_add_u32 m0, s52, 5120\n" \
    "s_nop 0\n" \
    "buffer_load_dwordx4 v1, s[40:43], s68 offen lds\n" \
    "s_waitcnt vmcnt(30)\n" \
    "ds_read_b64_tr_b16 a[40:41], v4 offset:6144\n" \
    "ds_read_b64_tr_b16 a[42:43], v5 offset:6144\n" \
    "ds_read_b64_tr_b16 a[44:45], v6 offset:6144\n" \
    "ds_read_b64_tr_b16 a[46:47], v7 offset:6144\n" \
    "s_waitcnt lgkmcnt(0)\n" \
    "s_add_u32 s68, s54, 860160\n" \
    "s_add_u32 m0, s52, 6144\n" \
    "s_nop 0\n" \
    "buffer_load_dwordx4 v0, s[40:43], s68 offen lds\n" \
    "s_add_u32 m0, s52, 7168\n" \
    "s_nop 0\n" \
    "buffer_load_dwordx4 v1, s[40:43], s68 offen lds\n" \
    "s_waitcnt vmcnt(30)\n" \
    "ds_read_b64_tr_b16 a[48:49], v4 offset:8192\n" \
    "ds_read_b64_tr_b16 a[50:51], v5 offset:8192\n" \
    "ds_read_b64_tr_b16 a[52:53], v6 offset:8192\n" \
    "ds_read_b64_tr_b16 a[54:55], v7 offset:8192\n" \
    "s_waitcnt lgkmcnt(0)\n" \
    "s_add_u32 s68, s54, 901120\n" \
    "s_add_u32 m0, s52, 8192\n" \
    "s_nop 0\n" \
    "buffer_load_dwordx4 v0, s[40:43], s68 offen lds\n" \
    "s_add_u32 m0, s52, 9216\n" \
    "s_nop 0\n" \
    "buffer_load_dwordx4 v1, s[40:43], s68 offen lds\n" \
    "s_waitcnt vmcnt(30)\n" \
    "ds_read_b64_tr_b16 a[56:57], v4 offset:10240\n" \
    "ds_read_b64_tr_b16 a[58:59], v5 offset:10240\n" \
    "ds_read_b64_tr_b16 a[60:61], v6 offset:10240\n" \
    "ds_read_b64_tr_b16 a[62:63], v7 offset:10240\n" \
    "s_waitcnt lgkmcnt(0)\n" \
    "s_add_u32 s68, s54, 942080\n" \
    "s_add_u32 m0, s52, 10240\n" \
    "s_nop 0\n" \
    "buffer_load_dwordx4 v0, s[40:43], s68 offen lds\n" \
    "s_add_u32 m0, s52, 11264\n" \
    "s_nop 0\n" \
    "buffer_load_dwordx4 v1, s[40:43], s68 offen lds\n" \
    "s_waitcnt vmcnt(30)\n" \
    "ds_read_b64_tr_b16 a[64:65], v4 offset:12288\n" \
    "ds_read_b64_tr_b16 a[66:67], v5 offset:12288\n" \
    "ds_read_b64_tr_b16 a[68:69], v6 offset:12288\n" \
    "ds_read_b64_tr_b16 a[70:71], v7 offset:12288\n" \
    "s_waitcnt lgkmcnt(0)\n" \
    "s_add_u32 s68, s54, 983040\n" \
    "s_add_u32 m0, s52, 12288\n" \
    "s_nop 0\n" \
    "buffer_load_dwordx4 v0, s[40:43], s68 offen lds\n" \
    "s_add_u32 m0, s52, 13312\n" \
    "s_nop 0\n" \
    "buffer_load_dwordx4 v1, s[40:43], s68 offen lds\n" \
    "s_waitcnt vmcnt(30)\n" \
    "ds_read_b64_tr_b16 a[72:73], v4 offset:14336\n" \
    "ds_read_b64_tr_b16 a[74:75], v5 offset:14336\n" \
    "ds_read_b64_tr_b16 a[76:77], v6 offset:14336\n" \
    "ds_read_b64_tr_b16 a[78:79], v7 offset:14336\n" \
    "s_waitcnt lgkmcnt(0)\n" \
    "s_add_u32 s68, s54, 1024000\n" \
    "s_add_u32 m0, s52, 14336\n" \
    "s_nop 0\n" \
    "buffer_load_dwordx4 v0, s[40:43], s68 offen lds\n" \
    "s_add_u32 m0, s52, 15360\n" \
    "s_nop 0\n" \
    "buffer_load_dwordx4 v1, s[40:43], s68 offen lds\n" \
    "s_waitcnt vmcnt(30)\n" \
    "ds_read_b64_tr_b16 a[80:81], v4 offset:16384\n" \
    "ds_read_b64_tr_b16 a[82:83], v5 offset:16384\n" \
    "ds_read_b64_tr_b16 a[84:85], v6 offset:16384\n" \
    "ds_read_b64_tr_b16 a[86:87], v7 offset:16384\n" \
    "s_waitcnt lgkmcnt(0)\n" \
    "s_add_u32 s68, s54, 1064960\n" \
    "s_add_u32 m0, s52, 16384\n" \
    "s_nop 0\n" \
    "buffer_load_dwordx4 v0, s[40:43], s68 offen lds\n" \
    "s_add_u32 m0, s52, 17408\n" \
    "s_nop 0\n" \
    "buffer_load_dwordx4 v1, s[40:43], s68 offen lds\n" \
    "s_waitcnt vmcnt(30)\n" \
    "ds_read_b64_tr_b16 a[88:89], v4 offset:18432\n" \
    "ds_read_b64_tr_b16 a[90:91], v5 offset:18432\n" \
    "ds_read_b64_tr_b16 a[92:93], v6 offset:18432\n" \
    "ds_read_b64_tr_b16 a[94:95], v7 offset:18432\n" \
    "s_waitcnt lgkmcnt(0)\n" \
    "s_add_u32 s68, s54, 1105920\n" \
    "s_add_u32 m0, s52, 18432\n" \
    "s_nop 0\n" \
    "buffer_load_dwordx4 v0, s[40:43], s68 offen lds\n" \
    "s_add_u32 m0, s52, 19456\n" \
    "s_nop 0\n" \
    "buffer_load_dwordx4 v1, s[40:43], s68 offen lds\n" \
    "s_waitcnt vmcnt(30)\n" \
    "ds_read_b64_tr_b16 a[96:97], v4 offset:20480\n" \
    "ds_read_b64_tr_b16 a[98:99], v5 offset:20480\n" \
    "ds_read_b64_tr_b16 a[100:101], v6 offset:20480\n" \
    "ds_read_b64_tr_b16 a[102:103], v7 offset:20480\n" \
    "s_waitcnt lgkmcnt(0)\n" \
    "s_add_u32 s68, s54, 1146880\n" \
    "s_add_u32 m0, s52, 20480\n" \
    "s_nop 0\n" \
    "buffer_load_dwordx4 v0, s[40:43], s68 offen lds\n" \
    "s_add_u32 m0, s52, 21504\n" \
    "s_nop 0\n" \
    "buffer_load_dwordx4 v1, s[40:43], s68 offen lds\n" \
    "s_waitcnt vmcnt(30)\n" \
    "ds_read_b64_tr_b16 a[104:105], v4 offset:22528\n" \
    "ds_read_b64_tr_b16 a[106:107], v5 offset:22528\n" \
    "ds_read_b64_tr_b16 a[108:109], v6 offset:22528\n" \
    "ds_read_b64_tr_b16 a[110:111], v7 offset:22528\n" \
    "s_waitcnt lgkmcnt(0)\n" \
    "s_add_u32 s68, s54, 1187840\n" \
    "s_add_u32 m0, s52, 22528\n" \
    "s_nop 0\n" \
    "buffer_load_dwordx4 v0, s[40:43], s68 offen lds\n" \
    "s_add_u32 m0, s52, 23552\n" \
    "s_nop 0\n" \
    "buffer_load_dwordx4 v1, s[40:43], s68 offen lds\n" \
    "s_waitcnt vmcnt(30)\n" \
    "ds_read_b64_tr_b16 a[112:113], v4 offset:24576\n" \
    "ds_read_b64_tr_b16 a[114:115], v5 offset:24576\n" \
    "ds_read_b64_tr_b16 a[116:117], v6 offset:24576\n" \
    "ds_read_b64_tr_b16 a[118:119], v7 offset:24576\n" \
    "s_waitcnt lgkmcnt(0)\n" \
    "s_add_u32 s68, s54, 1228800\n" \
    "s_add_u32 m0, s52, 24576\n" \
    "s_nop 0\n" \
    "buffer_load_dwordx4 v0, s[40:43], s68 offen lds\n" \
    "s_add_u32 m0, s52, 25600\n" \
    "s_nop 0\n" \
    "buffer_load_dwordx4 v1, s[40:43], s68 offen lds\n" \
    "s_waitcnt vmcnt(30)\n" \
    "ds_read_b64_tr_b16 a[120:121], v4 offset:26624\n" \
    "ds_read_b64_tr_b16 a[122:123], v5 offset:26624\n" \
    "ds_read_b64_tr_b16 a[124:125], v6 offset:26624\n" \
    "ds_read_b64_tr_b16 a[126:127], v7 offset:26624\n" \
    "s_waitcnt lgkmcnt(0)\n" \
    "s_add_u32 s68, s54, 1269760\n" \
    "s_add_u32 m0, s52, 26624\n" \
    "s_nop 0\n" \
    "buffer_load_dwordx4 v0, s[40:43], s68 offen lds\n" \
    "s_add_u32 m0, s52, 27648\n" \
    "s_nop 0\n" \
    "buffer_load_dwordx4 v1, s[40:43], s68 offen lds\n" \
    "s_waitcnt vmcnt(30)\n" \
    "ds_read_b64_tr_b16 a[128:129], v4 offset:28672\n" \
    "ds_read_b64_tr_b16 a[130:131], v5 offset:28672\n" \
    "ds_read_b64_tr_b16 a[132:133], v6 offset:28672\n" \
    "ds_read_b64_tr_b16 a[134:135], v7 offset:28672\n" \
    "s_waitcnt lgkmcnt(0)\n" \
    "s_waitcnt vmcnt(28)\n" \
    "ds_read_b64_tr_b16 a[136:137], v4 offset:30720\n" \
    "ds_read_b64_tr_b16 a[138:139], v5 offset:30720\n" \
    "ds_read_b64_tr_b16 a[140:141], v6 offset:30720\n" \
    "ds_read_b64_tr_b16 a[142:143], v7 offset:30720\n" \
    "s_waitcnt lgkmcnt(0)\n" \
    "s_waitcnt vmcnt(26)\n" \
    "ds_read_b64_tr_b16 a[144:145], v4 offset:0\n" \
    "ds_read_b64_tr_b16 a[146:147], v5 offset:0\n" \
    "ds_read_b64_tr_b16 a[148:149], v6 offset:0\n" \
    "ds_read_b64_tr_b16 a[150:151], v7 offset:0\n" \
    "s_waitcnt lgkmcnt(0)\n" \
    "s_waitcnt vmcnt(24)\n" \
    "ds_read_b64_tr_b16 a[152:153], v4 offset:2048\n" \
    "ds_read_b64_tr_b16 a[154:155], v5 offset:2048\n" \
    "ds_read_b64_tr_b16 a[156:157], v6 offset:2048\n" \
    "ds_read_b64_tr_b16 a[158:159], v7 offset:2048\n" \
    "s_waitcnt lgkmcnt(0)\n" \
    "s_waitcnt vmcnt(22)\n" \
    "ds_read_b64_tr_b16 a[160:161], v4 offset:4096\n" \
    "ds_read_b64_tr_b16 a[162:163], v5 offset:4096\n" \
    "ds_read_b64_tr_b16 a[164:165], v6 offset:4096\n" \
    "ds_read_b64_tr_b16 a[166:167], v7 offset:4096\n" \
    "s_waitcnt lgkmcnt(0)\n" \
    "s_waitcnt vmcnt(20)\n" \
    "ds_read_b64_tr_b16 a[168:169], v4 offset:6144\n" \
    "ds_read_b64_tr_b16 a[170:171], v5 offset:6144\n" \
    "ds_read_b64_tr_b16 a[172:173], v6 offset:6144\n" \
    "ds_read_b64_tr_b16 a[174:175], v7 offset:6144\n" \
    "s_waitcnt lgkmcnt(0)\n" \
    "s_waitcnt vmcnt(18)\n" \
    "ds_read_b64_tr_b16 a[176:177], v4 offset:8192\n" \
    "ds_read_b64_tr_b16 a[178:179], v5 offset:8192\n" \
    "ds_read_b64_tr_b16 a[180:181], v6 offset:8192\n" \
    "ds_read_b64_tr_b16 a[182:183], v7 offset:8192\n" \
    "s_waitcnt lgkmcnt(0)\n" \
    "s_waitcnt vmcnt(16)\n" \
    "ds_read_b64_tr_b16 a[184:185], v4 offset:10240\n" \
    "ds_read_b64_tr_b16 a[186:187], v5 offset:10240\n" \
    "ds_read_b64_tr_b16 a[188:189], v6 offset:10240\n" \
    "ds_read_b64_tr_b16 a[190:191], v7 offset:10240\n" \
    "s_waitcnt lgkmcnt(0)\n" \
    "s_waitcnt vmcnt(14)\n" \
    "ds_read_b64_tr_b16 a[192:193], v4 offset:12288\n" \
    "ds_read_b64_tr_b16 a[194:195], v5 offset:12288\n" \
    "ds_read_b64_tr_b16 a[196:197], v6 offset:12288\n" \
    "ds_read_b64_tr_b16 a[198:199], v7 offset:12288\n" \
    "s_waitcnt lgkmcnt(0)\n" \
    "s_waitcnt vmcnt(12)\n" \
    "ds_read_b64_tr_b16 a[200:201], v4 offset:14336\n" \
    "ds_read_b64_tr_b16 a[202:203], v5 offset:14336\n" \
    "ds_read_b64_tr_b16 a[204:205], v6 offset:14336\n" \
    "ds_read_b64_tr_b16 a[206:207], v7 offset:14336\n" \
    "s_waitcnt lgkmcnt(0)\n" \
    "s_waitcnt vmcnt(10)\n" \
    "ds_read_b64_tr_b16 a[208:209], v4 offset:16384\n" \
    "ds_read_b64_tr_b16 a[210:211], v5 offset:16384\n" \
    "ds_read_b64_tr_b16 a[212:213], v6 offset:16384\n" \
    "ds_read_b64_tr_b16 a[214:215], v7 offset:16384\n" \
    "s_waitcnt lgkmcnt(0)\n" \
    "s_waitcnt vmcnt(8)\n" \
    "ds_read_b64_tr_b16 a[216:217], v4 offset:18432\n" \
    "ds_read_b64_tr_b16 a[218:219], v5 offset:18432\n" \
    "ds_read_b64_tr_b16 a[220:221], v6 offset:18432\n" \
    "ds_read_b64_tr_b16 a[222:223], v7 offset:18432\n" \
    "s_waitcnt lgkmcnt(0)\n" \
    "s_waitcnt vmcnt(6)\n" \
    "ds_read_b64_tr_b16 a[224:225], v4 offset:20480\n" \
    "ds_read_b64_tr_b16 a[226:227], v5 offset:20480\n" \
    "ds_read_b64_tr_b16 a[228:229], v6 offset:20480\n" \
    "ds_read_b64_tr_b16 a[230:231], v7 offset:20480\n" \
    "s_waitcnt lgkmcnt(0)\n" \
    "s_waitcnt vmcnt(4)\n" \
    "ds_read_b64_tr_b16 a[232:233], v4 offset:22528\n" \
    "ds_read_b64_tr_b16 a[234:235], v5 offset:22528\n" \
    "ds_read_b64_tr_b16 a[236:237], v6 offset:22528\n" \
    "ds_read_b64_tr_b16 a[238:239], v7 offset:22528\n" \
    "s_waitcnt lgkmcnt(0)\n" \
    "s_waitcnt vmcnt(2)\n" \
    "ds_read_b64_tr_b16 a[240:241], v4 offset:24576\n" \
    "ds_read_b64_tr_b16 a[242:243], v5 offset:24576\n" \
    "ds_read_b64_tr_b16 a[244:245], v6 offset:24576\n" \
    "ds_read_b64_tr_b16 a[246:247], v7 offset:24576\n" \
    "s_waitcnt lgkmcnt(0)\n" \
    "s_waitcnt vmcnt(0)\n" \
    "ds_read_b64_tr_b16 a[248:249], v4 offset:26624\n" \
    "ds_read_b64_tr_b16 a[250:251], v5 offset:26624\n" \
    "ds_read_b64_tr_b16 a[252:253], v6 offset:26624\n" \
    "ds_read_b64_tr_b16 a[254:255], v7 offset:26624\n" \
    "s_waitcnt lgkmcnt(0)\n" \
    "s_mov_b32 s72, s60\n" \
    "s_mov_b32 s77, 0\n" \
    "s_mov_b32 s76, 0\n" \
    "v_mov_b32 v26, 0\n" \
    "v_mov_b32 v27, 0\n" \
    "s_barrier\n" \
    "s_lshr_b32 s69, s72, 1\n" \
    "s_add_u32 s69, s69, 1\n" \
    "s_cmp_lt_u32 s77, s69\n" \
    "s_cbranch_scc1 O_ff0_%=\n" \
    "O_ff0_d_%=:\n" \
    "s_mul_i32 s68, s72, 55296\n" \
    "s_add_u32 s68, s68, 73400320\n" \
    "s_add_u32 s40, s68, s56\n" \
    "s_add_u32 s41, s68, s55\n" \
    "buffer_load_dwordx4 v[48:51], v16, s[36:39], s40 offen sc1\n" \
    "s_add_u32 s40, s40, 4096\n" \
    "buffer_load_dwordx4 v[52:55], v16, s[36:39], s40 offen sc1\n" \
    "s_add_u32 s40, s40, 4096\n" \
    "buffer_load_dwordx4 v[56:59], v16, s[36:39], s40 offen sc1\n" \
    "s_add_u32 s40, s40, 4096\n" \
    "buffer_load_dwordx4 v[60:63], v16, s[36:39], s40 offen sc1\n" \
    "s_add_u32 s40, s40, 4096\n" \
    "buffer_load_dwordx4 v[64:67], v16, s[36:39], s40 offen sc1\n" \
    "s_add_u32 s40, s40, 4096\n" \
    "buffer_load_dwordx4 v[68:71], v16, s[36:39], s40 offen sc1\n" \
    "s_add_u32 s40, s40, 4096\n" \
    "buffer_load_dwordx4 v[72:75], v16, s[36:39], s40 offen sc1\n" \
    "s_add_u32 s40, s40, 4096\n" \
    "buffer_load_dwordx4 v[76:79], v16, s[36:39], s40 offen sc1\n" \
    "s_add_u32 s40, s40, 4096\n" \
    "buffer_load_dwordx4 v[80:83], v16, s[36:39], s40 offen sc1\n" \
    "s_add_u32 s40, s40, 4096\n" \
    "buffer_load_dwordx4 v[84:87], v16, s[36:39], s40 offen sc1\n" \
    "s_add_u32 s40, s40, 4096\n" \
    "buffer_load_dwordx4 v[88:91], v16, s[36:39], s40 offen sc1\n" \
    "s_add_u32 s40, s40, 4096\n" \
    "buffer_load_dwordx4 v[92:95], v16, s[36:39], s40 offen sc1\n" \
    "s_add_u32 s40, s40, 4096\n" \
    "buffer_load_dwordx4 v[96:99], v16, s[36:39], s40 offen sc1\n" \
    "s_add_u32 s40, s40, 4096\n" \
    "buffer_load_dwordx4 v[100:103], v16, s[36:39], s41 offen sc1\n" \
    "O_loop_%=:\n" \
    "s_and_b32 s68, s72, 1\n" \
    "s_lshl_b32 s78, s68, 4\n" \
    "buffer_load_dwordx2 v[28:29], v18, s[36:39], s78 offen sc1\n" \
    "s_mul_i32 s79, s72, 20480\n" \
    "buffer_load_dwordx2 v[30:31], v19, s[48:51], s79 offen\n" \
    "s_waitcnt vmcnt(15)\n" \
    "v_mfma_f32_16x16x32_bf16 v[120:123], v[144:147], v[48:51], 0\n" \
    "v_mfma_f32_16x16x32_bf16 v[124:127], v[148:151], v[48:51], 0\n" \
    "s_waitcnt vmcnt(14)\n" \
    "v_mfma_f32_16x16x32_bf16 v[120:123], v[152:155], v[52:55], v[120:123]\n" \
    "v_mfma_f32_16x16x32_bf16 v[124:127], v[156:159], v[52:55], v[124:127]\n" \
    "s_waitcnt vmcnt(13)\n" \
    "v_mfma_f32_16x16x32_bf16 v[120:123], v[160:163], v[56:59], v[120:123]\n" \
    "v_mfma_f32_16x16x32_bf16 v[124:127], v[164:167], v[56:59], v[124:127]\n" \
    "s_waitcnt vmcnt(12)\n" \
    "v_mfma_f32_16x16x32_bf16 v[120:123], v[168:171], v[60:63], v[120:123]\n" \
    "v_mfma_f32_16x16x32_bf16 v[124:127], v[172:175], v[60:63], v[124:127]\n" \
    "s_waitcnt vmcnt(11)\n" \
    "v_mfma_f32_16x16x32_bf16 v[120:123], v[176:179], v[64:67], v[120:123]\n" \
    "v_mfma_f32_16x16x32_bf16 v[124:127], v[180:183], v[64:67], v[124:127]\n" \
    "s_waitcnt vmcnt(10)\n" \
    "v_mfma_f32_16x16x32_bf16 v[120:123], v[184:187], v[68:71], v[120:123]\n" \
    "v_mfma_f32_16x16x32_bf16 v[124:127], v[188:191], v[68:71], v[124:127]\n" \
    "s_waitcnt vmcnt(9)\n" \
    "v_mfma_f32_16x16x32_bf16 v[120:123], v[192:195], v[72:75], v[120:123]\n" \
    "v_mfma_f32_16x16x32_bf16 v[124:127], v[196:199], v[72:75], v[124:127]\n" \
    "s_waitcnt vmcnt(8)\n" \
    "v_mfma_f32_16x16x32_bf16 v[120:123], v[200:203], v[76:79], v[120:123]\n" \
    "v_mfma_f32_16x16x32_bf16 v[124:127], v[204:207], v[76:79], v[124:127]\n" \
    "s_waitcnt vmcnt(7)\n" \
    "v_mfma_f32_16x16x32_bf16 v[120:123], v[208:211], v[80:83], v[120:123]\n" \
    "v_mfma_f32_16x16x32_bf16 v[124:127], v[212:215], v[80:83], v[124:127]\n" \
    "s_waitcnt vmcnt(6)\n" \
    "v_mfma_f32_16x16x32_bf16 v[120:123], v[216:219], v[84:87], v[120:123]\n" \
    "v_mfma_f32_16x16x32_bf16 v[124:127], v[220:223], v[84:87], v[124:127]\n" \
    "s_waitcnt vmcnt(5)\n" \
    "v_mfma_f32_16x16x32_bf16 v[120:123], v[224:227], v[88:91], v[120:123]\n" \
    "v_mfma_f32_16x16x32_bf16 v[124:127], v[228:231], v[88:91], v[124:127]\n" \
    "s_waitcnt vmcnt(4)\n" \
    "v_mfma_f32_16x16x32_bf16 v[120:123], v[232:235], v[92:95], v[120:123]\n" \
    "v_mfma_f32_16x16x32_bf16 v[124:127], v[236:239], v[92:95], v[124:127]\n" \
    "s_waitcnt vmcnt(3)\n" \
    "v_mfma_f32_16x16x32_bf16 v[120:123], v[240:243], v[96:99], v[120:123]\n" \
    "v_mfma_f32_16x16x32_bf16 v[124:127], v[244:247], v[96:99], v[124:127]\n" \
    "s_waitcnt vmcnt(2)\n" \
    "v_mfma_f32_16x16x32_bf16 v[120:123], v[248:251], v[100:103], v[120:123]\n" \
    "v_mfma_f32_16x16x32_bf16 v[124:127], v[252:255], v[100:103], v[124:127]\n" \
    "s_lshr_b32 s69, s72, 1\n" \
    "s_add_u32 s69, s69, 1\n" \
    "s_waitcnt vmcnt(0)\n" \
    "s_branch O_of_%=\n" \
    "O_of_d_%=:\n" \
    "s_lshl_b32 s68, s72, 12\n" \
    "s_add_u32 s40, s68, s57\n" \
    "buffer_load_dwordx4 v[48:51], v16, s[36:39], s40 offen offset:0 sc1\n" \
    "buffer_load_dwordx4 v[52:55], v16, s[36:39], s40 offen offset:1024 sc1\n" \
    "buffer_load_dwordx4 v[56:59], v16, s[36:39], s40 offen offset:2048 sc1\n" \
    "buffer_load_dwordx4 v[60:63], v16, s[36:39], s40 offen offset:3072 sc1\n" \
    "s_add_u32 s40, s40, 2097152\n" \
    "buffer_load_dwordx4 v[64:67], v16, s[36:39], s40 offen offset:0 sc1\n" \
    "buffer_load_dwordx4 v[68:71], v16, s[36:39], s40 offen offset:1024 sc1\n" \
    "buffer_load_dwordx4 v[72:75], v16, s[36:39], s40 offen offset:2048 sc1\n" \
    "buffer_load_dwordx4 v[76:79], v16, s[36:39], s40 offen offset:3072 sc1\n" \
    "s_add_u32 s40, s40, 2097152\n" \
    "buffer_load_dwordx4 v[80:83], v16, s[36:39], s40 offen offset:0 sc1\n" \
    "buffer_load_dwordx4 v[84:87], v16, s[36:39], s40 offen offset:1024 sc1\n" \
    "buffer_load_dwordx4 v[88:91], v16, s[36:39], s40 offen offset:2048 sc1\n" \
    "buffer_load_dwordx4 v[92:95], v16, s[36:39], s40 offen offset:3072 sc1\n" \
    "s_add_u32 s40, s40, 2097152\n" \
    "buffer_load_dwordx4 v[96:99], v16, s[36:39], s40 offen offset:0 sc1\n" \
    "buffer_load_dwordx4 v[100:103], v16, s[36:39], s40 offen offset:1024 sc1\n" \
    "buffer_load_dwordx4 v[104:107], v16, s[36:39], s40 offen offset:2048 sc1\n" \
    "buffer_load_dwordx4 v[108:111], v16, s[36:39], s40 offen offset:3072 sc1\n" \
    "s_add_u32 s40, s40, 2097152\n" \
    "s_waitcnt vmcnt(15)\n" \
    "v_mfma_f32_16x16x32_bf16 v[112:115], a[0:3], v[48:51], 0\n" \
    "v_mfma_f32_16x16x32_bf16 v[116:119], a[4:7], v[48:51], 0\n" \
    "buffer_load_dwordx4 v[48:51], v16, s[36:39], s40 offen offset:0 sc1\n" \
    "s_waitcnt vmcnt(15)\n" \
    "v_mfma_f32_16x16x32_bf16 v[112:115], a[8:11], v[52:55], v[112:115]\n" \
    "v_mfma_f32_16x16x32_bf16 v[116:119], a[12:15], v[52:55], v[116:119]\n" \
    "buffer_load_dwordx4 v[52:55], v16, s[36:39], s40 offen offset:1024 sc1\n" \
    "s_waitcnt vmcnt(15)\n" \
    "v_mfma_f32_16x16x32_bf16 v[112:115], a[16:19], v[56:59], v[112:115]\n" \
    "v_mfma_f32_16x16x32_bf16 v[116:119], a[20:23], v[56:59], v[116:119]\n" \
    "buffer_load_dwordx4 v[56:59], v16, s[36:39], s40 offen offset:2048 sc1\n" \
    "s_waitcnt vmcnt(15)\n" \
    "v_mfma_f32_16x16x32_bf16 v[112:115], a[24:27], v[60:63], v[112:115]\n" \
    "v_mfma_f32_16x16x32_bf16 v[116:119], a[28:31], v[60:63], v[116:119]\n" \
    "buffer_load_dwordx4 v[60:63], v16, s[36:39], s40 offen offset:3072 sc1\n" \
    "s_add_u32 s40, s40, 2097152\n" \
    "s_waitcnt vmcnt(15)\n" \
    "v_mfma_f32_16x16x32_bf16 v[112:115], a[32:35], v[64:67], v[112:115]\n" \
    "v_mfma_f32_16x16x32_bf16 v[116:119], a[36:39], v[64:67], v[116:119]\n" \
    "buffer_load_dwordx4 v[64:67], v16, s[36:39], s40 offen offset:0 sc1\n" \
    "s_waitcnt vmcnt(15)\n" \
    "v_mfma_f32_16x16x32_bf16 v[112:115], a[40:43], v[68:71], v[112:115]\n" \
    "v_mfma_f32_16x16x32_bf16 v[116:119], a[44:47], v[68:71], v[116:119]\n" \
    "buffer_load_dwordx4 v[68:71], v16, s[36:39], s40 offen offset:1024 sc1\n" \
    "s_waitcnt vmcnt(15)\n" \
    "v_mfma_f32_16x16x32_bf16 v[112:115], a[48:51], v[72:75], v[112:115]\n" \
    "v_mfma_f32_16x16x32_bf16 v[116:119], a[52:55], v[72:75], v[116:119]\n" \
    "buffer_load_dwordx4 v[72:75], v16, s[36:39], s40 offen offset:2048 sc1\n" \
    "s_waitcnt vmcnt(15)\n" \
    "v_mfma_f32_16x16x32_bf16 v[112:115], a[56:59], v[76:79], v[112:115]\n" \
    "v_mfma_f32_16x16x32_bf16 v[116:119], a[60:63], v[76:79], v[116:119]\n" \
    "buffer_load_dwordx4 v[76:79], v16, s[36:39], s40 offen offset:3072 sc1\n" \
    "s_add_u32 s40, s40, 2097152\n" \
    "s_waitcnt vmcnt(15)\n" \
    "v_mfma_f32_16x16x32_bf16 v[112:115], a[64:67], v[80:83], v[112:115]\n" \
    "v_mfma_f32_16x16x32_bf16 v[116:119], a[68:71], v[80:83], v[116:119]\n" \
    "buffer_load_dwordx4 v[80:83], v16, s[36:39], s40 offen offset:0 sc1\n" \
    "s_waitcnt vmcnt(15)\n" \
    "v_mfma_f32_16x16x32_bf16 v[112:115], a[72:75], v[84:87], v[112:115]\n" \
    "v_mfma_f32_16x16x32_bf16 v[116:119], a[76:79], v[84:87], v[116:119]\n" \
    "buffer_load_dwordx4 v[84:87], v16, s[36:39], s40 offen offset:1024 sc1\n" \
    "s_waitcnt vmcnt(15)\n" \
    "v_mfma_f32_16x16x32_bf16 v[112:115], a[80:83], v[88:91], v[112:115]\n" \
    "v_mfma_f32_16x16x32_bf16 v[116:119], a[84:87], v[88:91], v[116:119]\n" \
    "buffer_load_dwordx4 v[88:91], v16, s[36:39], s40 offen offset:2048 sc1\n" \
    "s_waitcnt vmcnt(15)\n" \
    "v_mfma_f32_16x16x32_bf16 v[112:115], a[88:91], v[92:95], v[112:115]\n" \
    "v_mfma_f32_16x16x32_bf16 v[116:119], a[92:95], v[92:95], v[116:119]\n" \
    "buffer_load_dwordx4 v[92:95], v16, s[36:39], s40 offen offset:3072 sc1\n" \
    "s_add_u32 s40, s40, 2097152\n" \
    "s_waitcnt vmcnt(15)\n" \
    "v_mfma_f32_16x16x32_bf16 v[112:115], a[96:99], v[96:99], v[112:115]\n" \
    "v_mfma_f32_16x16x32_bf16 v[116:119], a[100:103], v[96:99], v[116:119]\n" \
    "buffer_load_dwordx4 v[96:99], v16, s[36:39], s40 offen offset:0 sc1\n" \
    "s_waitcnt vmcnt(15)\n" \
    "v_mfma_f32_16x16x32_bf16 v[112:115], a[104:107], v[100:103], v[112:115]\n" \
    "v_mfma_f32_16x16x32_bf16 v[116:119], a[108:111], v[100:103], v[116:119]\n" \
    "buffer_load_dwordx4 v[100:103], v16, s[36:39], s40 offen offset:1024 sc1\n" \
    "s_waitcnt vmcnt(15)\n" \
    "v_mfma_f32_16x16x32_bf16 v[112:115], a[112:115], v[104:107], v[112:115]\n" \
    "v_mfma_f32_16x16x32_bf16 v[116:119], a[116:119], v[104:107], v[116:119]\n" \
    "buffer_load_dwordx4 v[104:107], v16, s[36:39], s40 offen offset:2048 sc1\n" \
    "s_waitcnt vmcnt(15)\n" \
    "v_mfma_f32_16x16x32_bf16 v[112:115], a[120:123], v[108:111], v[112:115]\n" \
    "v_mfma_f32_16x16x32_bf16 v[116:119], a[124:127], v[108:111], v[116:119]\n" \
    "buffer_load_dwordx4 v[108:111], v16, s[36:39], s40 offen offset:3072 sc1\n" \
    "s_add_u32 s40, s40, 2097152\n" \
    "s_waitcnt vmcnt(15)\n" \
    "v_mfma_f32_16x16x32_bf16 v[112:115], a[128:131], v[48:51], v[112:115]\n" \
    "v_mfma_f32_16x16x32_bf16 v[116:119], a[132:135], v[48:51], v[116:119]\n" \
    "s_waitcnt vmcnt(14)\n" \
    "v_mfma_f32_16x16x32_bf16 v[112:115], a[136:139], v[52:55], v[112:115]\n" \
    "v_mfma_f32_16x16x32_bf16 v[116:119], a[140:143], v[52:55], v[116:119]\n" \
    "s_waitcnt vmcnt(13)\n" \
    "v_mfma_f32_16x16x32_bf16 v[112:115], a[144:147], v[56:59], v[112:115]\n" \
    "v_mfma_f32_16x16x32_bf16 v[116:119], a[148:151], v[56:59], v[116:119]\n" \
    "s_waitcnt vmcnt(12)\n" \
    "v_mfma_f32_16x16x32_bf16 v[112:115], a[152:155], v[60:63], v[112:115]\n" \
    "v_mfma_f32_16x16x32_bf16 v[116:119], a[156:159], v[60:63], v[116:119]\n" \
    "s_waitcnt vmcnt(11)\n" \
    "v_mfma_f32_16x16x32_bf16 v[112:115], a[160:163], v[64:67], v[112:115]\n" \
    "v_mfma_f32_16x16x32_bf16 v[116:119], a[164:167], v[64:67], v[116:119]\n" \
    "s_waitcnt vmcnt(10)\n" \
    "v_mfma_f32_16x16x32_bf16 v[112:115], a[168:171], v[68:71], v[112:115]\n" \
    "v_mfma_f32_16x16x32_bf16 v[116:119], a[172:175], v[68:71], v[116:119]\n" \
    "s_waitcnt vmcnt(9)\n" \
    "v_mfma_f32_16x16x32_bf16 v[112:115], a[176:179], v[72:75], v[112:115]\n" \
    "v_mfma_f32_16x16x32_bf16 v[116:119], a[180:183], v[72:75], v[116:119]\n" \
    "s_waitcnt vmcnt(8)\n" \
    "v_mfma_f32_16x16x32_bf16 v[112:115], a[184:187], v[76:79], v[112:115]\n" \
    "v_mfma_f32_16x16x32_bf16 v[116:119], a[188:191], v[76:79], v[116:119]\n" \
    "s_waitcnt vmcnt(7)\n" \
    "v_mfma_f32_16x16x32_bf16 v[112:115], a[192:195], v[80:83], v[112:115]\n" \
    "v_mfma_f32_16x16x32_bf16 v[116:119], a[196:199], v[80:83], v[116:119]\n" \
    "s_waitcnt vmcnt(6)\n" \
    "v_mfma_f32_16x16x32_bf16 v[112:115], a[200:203], v[84:87], v[112:115]\n" \
    "v_mfma_f32_16x16x32_bf16 v[116:119], a[204:207], v[84:87], v[116:119]\n" \
    "s_waitcnt vmcnt(5)\n" \
    "v_mfma_f32_16x16x32_bf16 v[112:115], a[208:211], v[88:91], v[112:115]\n" \
    "v_mfma_f32_16x16x32_bf16 v[116:119], a[212:215], v[88:91], v[116:119]\n" \
    "s_waitcnt vmcnt(4)\n" \
    "v_mfma_f32_16x16x32_bf16 v[112:115], a[216:219], v[92:95], v[112:115]\n" \
    "v_mfma_f32_16x16x32_bf16 v[116:119], a[220:223], v[92:95], v[116:119]\n" \
    "s_waitcnt vmcnt(3)\n" \
    "v_mfma_f32_16x16x32_bf16 v[112:115], a[224:227], v[96:99], v[112:115]\n" \
    "v_mfma_f32_16x16x32_bf16 v[116:119], a[228:231], v[96:99], v[116:119]\n" \
    "s_waitcnt vmcnt(2)\n" \
    "v_mfma_f32_16x16x32_bf16 v[112:115], a[232:235], v[100:103], v[112:115]\n" \
    "v_mfma_f32_16x16x32_bf16 v[116:119], a[236:239], v[100:103], v[116:119]\n" \
    "s_waitcnt vmcnt(1)\n" \
    "v_mfma_f32_16x16x32_bf16 v[112:115], a[240:243], v[104:107], v[112:115]\n" \
    "v_mfma_f32_16x16x32_bf16 v[116:119], a[244:247], v[104:107], v[116:119]\n" \
    "s_waitcnt vmcnt(0)\n" \
    "v_mfma_f32_16x16x32_bf16 v[112:115], a[248:251], v[108:111], v[112:115]\n" \
    "v_mfma_f32_16x16x32_bf16 v[116:119], a[252:255], v[108:111], v[116:119]\n" \
    "v_add_u32 v0, s76, v20\n" \
    "v_add_u32 v1, s76, v21\n" \
    "ds_write_b128 v0, v[120:123] offset:2048\n" \
    "ds_write_b128 v0, v[124:127] offset:3072\n" \
    "s_add_u32 s72, s72, 3\n" \
    "s_xor_b32 s76, s76, 16384\n" \
    "s_nop 2\n" \
    "ds_write_b128 v0, v[112:115]\n" \
    "ds_write_b128 v0, v[116:119] offset:1024\n" \
    "s_cmp_ge_u32 s72, 128\n" \
    "s_cbranch_scc1 O_nopre_%=\n" \
    "s_lshr_b32 s69, s72, 1\n" \
    "s_add_u32 s69, s69, 1\n" \
    "s_cmp_lt_u32 s77, s69\n" \
    "s_cbranch_scc1 O_ff1_%=\n" \
    "O_ff1_d_%=:\n" \
    "s_mul_i32 s68, s72, 55296\n" \
    "s_add_u32 s68, s68, 73400320\n" \
    "s_add_u32 s40, s68, s56\n" \
    "s_add_u32 s41, s68, s55\n" \
    "buffer_load_dwordx4 v[48:51], v16, s[36:39], s40 offen sc1\n" \
    "s_add_u32 s40, s40, 4096\n" \
    "buffer_load_dwordx4 v[52:55], v16, s[36:39], s40 offen sc1\n" \
    "s_add_u32 s40, s40, 4096\n" \
    "buffer_load_dwordx4 v[56:59], v16, s[36:39], s40 offen sc1\n" \
    "s_add_u32 s40, s40, 4096\n" \
    "buffer_load_dwordx4 v[60:63], v16, s[36:39], s40 offen sc1\n" \
    "s_add_u32 s40, s40, 4096\n" \
    "buffer_load_dwordx4 v[64:67], v16, s[36:39], s40 offen sc1\n" \
    "s_add_u32 s40, s40, 4096\n" \
    "buffer_load_dwordx4 v[68:71], v16, s[36:39], s40 offen sc1\n" \
    "s_add_u32 s40, s40, 4096\n" \
    "buffer_load_dwordx4 v[72:75], v16, s[36:39], s40 offen sc1\n" \
    "s_add_u32 s40, s40, 4096\n" \
    "buffer_load_dwordx4 v[76:79], v16, s[36:39], s40 offen sc1\n" \
    "s_add_u32 s40, s40, 4096\n" \
    "buffer_load_dwordx4 v[80:83], v16, s[36:39], s40 offen sc1\n" \
    "s_add_u32 s40, s40, 4096\n" \
    "buffer_load_dwordx4 v[84:87], v16, s[36:39], s40 offen sc1\n" \
    "s_add_u32 s40, s40, 4096\n" \
    "buffer_load_dwordx4 v[88:91], v16, s[36:39], s40 offen sc1\n" \
    "s_add_u32 s40, s40, 4096\n" \
    "buffer_load_dwordx4 v[92:95], v16, s[36:39], s40 offen sc1\n" \
    "s_add_u32 s40, s40, 4096\n" \
    "buffer_load_dwordx4 v[96:99], v16, s[36:39], s40 offen sc1\n" \
    "s_add_u32 s40, s40, 4096\n" \
    "buffer_load_dwordx4 v[100:103], v16, s[36:39], s41 offen sc1\n" \
    "O_nopre_%=:\n" \
    "s_cmp_gt_u32 s62, 1\n" \
    "s_waitcnt lgkmcnt(0)\n" \
    "s_barrier\n" \
    "s_cbranch_scc1 O_skip_%=\n" \
    "ds_read_b128 v[112:115], v1 offset:0\n" \
    "ds_read_b128 v[116:119], v1 offset:4096\n" \
    "ds_read_b128 v[120:123], v1 offset:8192\n" \
    "ds_read_b128 v[124:127], v1 offset:12288\n" \
    "ds_read_b128 v[128:131], v1 offset:2048\n" \
    "ds_read_b128 v[132:135], v1 offset:6144\n" \
    "ds_read_b128 v[136:139], v1 offset:10240\n" \
    "ds_read_b128 v[140:143], v1 offset:14336\n" \
    "v_lshlrev_b32 v2, 16, v30\n" \
    "v_and_b32 v3, 0xffff0000, v30\n" \
    "v_lshlrev_b32 v4, 16, v31\n" \
    "v_and_b32 v5, 0xffff0000, v31\n" \
    "s_waitcnt lgkmcnt(4)\n" \
    "v_pk_add_f32 v[112:113], v[112:113], v[116:117]\n" \
    "v_pk_add_f32 v[120:121], v[120:121], v[124:125]\n" \
    "v_pk_add_f32 v[114:115], v[114:115], v[118:119]\n" \
    "v_pk_add_f32 v[122:123], v[122:123], v[126:127]\n" \
    "v_pk_add_f32 v[112:113], v[112:113], v[120:121]\n" \
    "v_pk_add_f32 v[114:115], v[114:115], v[122:123]\n" \
    "v_cvt_pk_bf16_f32 v116, v112, v113\n" \
    "v_cvt_pk_bf16_f32 v117, v114, v115\n" \
    "v_lshlrev_b32 v112, 16, v116\n" \
    "v_and_b32 v113, 0xffff0000, v116\n" \
    "v_lshlrev_b32 v114, 16, v117\n" \
    "v_and_b32 v115, 0xffff0000, v117\n" \
    "s_waitcnt lgkmcnt(0)\n" \
    "v_pk_add_f32 v[128:129], v[128:129], v[132:133]\n" \
    "v_pk_add_f32 v[136:137], v[136:137], v[140:141]\n" \
    "v_pk_add_f32 v[130:131], v[130:131], v[134:135]\n" \
    "v_pk_add_f32 v[138:139], v[138:139], v[142:143]\n" \
    "v_pk_add_f32 v[128:129], v[128:129], v[136:137]\n" \
    "v_pk_add_f32 v[130:131], v[130:131], v[138:139]\n" \
    "v_cvt_pk_bf16_f32 v132, v128, v129\n" \
    "v_cvt_pk_bf16_f32 v133, v130, v131\n" \
    "v_lshlrev_b32 v128, 16, v132\n" \
    "v_and_b32 v129, 0xffff0000, v132\n" \
    "v_lshlrev_b32 v130, 16, v133\n" \
    "v_and_b32 v131, 0xffff0000, v133\n" \
    "v_pk_add_f32 v[2:3], v[2:3], v[112:113]\n" \
    "v_pk_add_f32 v[4:5], v[4:5], v[114:115]\n" \
    "v_cvt_pk_bf16_f32 v6, v2, v3\n" \
    "v_cvt_pk_bf16_f32 v7, v4, v5\n" \
    "v_lshlrev_b32 v2, 16, v6\n" \
    "v_and_b32 v3, 0xffff0000, v6\n" \
    "v_lshlrev_b32 v4, 16, v7\n" \
    "v_and_b32 v5, 0xffff0000, v7\n" \
    "v_pk_add_f32 v[2:3], v[2:3], v[128:129]\n" \
    "v_pk_add_f32 v[4:5], v[4:5], v[130:131]\n" \
    "v_cvt_pk_bf16_f32 v6, v2, v3\n" \
    "v_cvt_pk_bf16_f32 v7, v4, v5\n" \
    "O_skip_%=:\n" \
    "buffer_store_dwordx2 v[6:7], v19, s[88:91], s79 offen\n" \
    "s_cmp_lt_u32 s72, 128\n" \
    "s_cbranch_scc1 O_loop_%=\n" \
    "s_endpgm\n" \
    "O_ff0_%=:\n" \
    "s_waitcnt vmcnt(0)\n" \
    "s_memrealtime s[80:81]\n" \
    "s_waitcnt lgkmcnt(0)\n" \
    "s_mov_b32 s83, s80\n" \
    "s_mov_b32 s82, 0\n" \
    "O_ff0_l_%=:\n" \
    "v_cmp_eq_u32 vcc, s63, v26\n" \
    "s_nop 1\n" \
    "v_cndmask_b32 v27, 0, v27, vcc\n" \
    "v_max_u32 v27, v27, v22\n" \
    "v_cmp_le_u32 vcc, s69, v27\n" \
    "s_cmp_eq_u64 vcc, exec\n" \
    "s_cbranch_scc1 O_ff0_up_%=\n" \
    "s_add_u32 s82, s82, 1\n" \
    "s_and_b32 s85, s82, 63\n" \
    "s_cmp_lg_u32 s85, 0\n" \
    "s_cbranch_scc1 O_ff0_g_ok_%=\n" \
    "s_mov_b32 s85, 1048832\n" \
    "buffer_load_dword v32, off, s[36:39], s85 sc1\n" \
    "s_waitcnt vmcnt(0)\n" \
    "v_readfirstlane_b32 s85, v32\n" \
    "s_cmp_eq_u32 s85, s63\n" \
    "s_cbranch_scc1 O_end_%=\n" \
    "s_memrealtime s[80:81]\n" \
    "s_waitcnt lgkmcnt(0)\n" \
    "s_sub_u32 s85, s80, s83\n" \
    "s_cmp_gt_u32 s85, 3000000\n" \
    "s_cbranch_scc1 O_giveup_%=\n" \
    "O_ff0_g_ok_%=:\n" \
    "s_sleep 1\n" \
    "buffer_load_dwordx2 v[26:27], v17, s[36:39], 0 offen sc1\n" \
    "s_waitcnt vmcnt(0)\n" \
    "s_branch O_ff0_l_%=\n" \
    "O_ff0_up_%=:\n" \
    "s_mov_b32 s77, s69\n" \
    "s_add_u32 s85, s77, 16\n" \
    "v_cmp_le_u32 vcc, s85, v27\n" \
    "s_cmp_eq_u64 vcc, exec\n" \
    "s_cselect_b32 s77, s85, s77\n" \
    "s_add_u32 s85, s77, 8\n" \
    "v_cmp_le_u32 vcc, s85, v27\n" \
    "s_cmp_eq_u64 vcc, exec\n" \
    "s_cselect_b32 s77, s85, s77\n" \
    "s_add_u32 s85, s77, 4\n" \
    "v_cmp_le_u32 vcc, s85, v27\n" \
    "s_cmp_eq_u64 vcc, exec\n" \
    "s_cselect_b32 s77, s85, s77\n" \
    "s_add_u32 s85, s77, 2\n" \
    "v_cmp_le_u32 vcc, s85, v27\n" \
    "s_cmp_eq_u64 vcc, exec\n" \
    "s_cselect_b32 s77, s85, s77\n" \
    "s_add_u32 s85, s77, 1\n" \
    "v_cmp_le_u32 vcc, s85, v27\n" \
    "s_cmp_eq_u64 vcc, exec\n" \
    "s_cselect_b32 s77, s85, s77\n" \
    "s_branch O_ff0_d_%=\n" \
    "O_ff1_%=:\n" \
    "s_waitcnt vmcnt(0)\n" \
    "s_memrealtime s[80:81]\n" \
    "s_waitcnt lgkmcnt(0)\n" \
    "s_mov_b32 s83, s80\n" \
    "s_mov_b32 s82, 0\n" \
    "O_ff1_l_%=:\n" \
    "v_cmp_eq_u32 vcc, s63, v26\n" \
    "s_nop 1\n" \
    "v_cndmask_b32 v27, 0, v27, vcc\n" \
    "v_max_u32 v27, v27, v22\n" \
    "v_cmp_le_u32 vcc, s69, v27\n" \
    "s_cmp_eq_u64 vcc, exec\n" \
    "s_cbranch_scc1 O_ff1_up_%=\n" \
    "s_add_u32 s82, s82, 1\n" \
    "s_and_b32 s85, s82, 63\n" \
    "s_cmp_lg_u32 s85, 0\n" \
    "s_cbranch_scc1 O_ff1_g_ok_%=\n" \
    "s_mov_b32 s85, 1048832\n" \
    "buffer_load_dword v32, off, s[36:39], s85 sc1\n" \
    "s_waitcnt vmcnt(0)\n" \
    "v_readfirstlane_b32 s85, v32\n" \
    "s_cmp_eq_u32 s85, s63\n" \
    "s_cbranch_scc1 O_end_%=\n" \
    "s_memrealtime s[80:81]\n" \
    "s_waitcnt lgkmcnt(0)\n" \
    "s_sub_u32 s85, s80, s83\n" \
    "s_cmp_gt_u32 s85, 3000000\n" \
    "s_cbranch_scc1 O_giveup_%=\n" \
    "O_ff1_g_ok_%=:\n" \
    "s_sleep 1\n" \
    "buffer_load_dwordx2 v[26:27], v17, s[36:39], 0 offen sc1\n" \
    "s_waitcnt vmcnt(0)\n" \
    "s_branch O_ff1_l_%=\n" \
    "O_ff1_up_%=:\n" \
    "s_mov_b32 s77, s69\n" \
    "s_add_u32 s85, s77, 16\n" \
    "v_cmp_le_u32 vcc, s85, v27\n" \
    "s_cmp_eq_u64 vcc, exec\n" \
    "s_cselect_b32 s77, s85, s77\n" \
    "s_add_u32 s85, s77, 8\n" \
    "v_cmp_le_u32 vcc, s85, v27\n" \
    "s_cmp_eq_u64 vcc, exec\n" \
    "s_cselect_b32 s77, s85, s77\n" \
    "s_add_u32 s85, s77, 4\n" \
    "v_cmp_le_u32 vcc, s85, v27\n" \
    "s_cmp_eq_u64 vcc, exec\n" \
    "s_cselect_b32 s77, s85, s77\n" \
    "s_add_u32 s85, s77, 2\n" \
    "v_cmp_le_u32 vcc, s85, v27\n" \
    "s_cmp_eq_u64 vcc, exec\n" \
    "s_cselect_b32 s77, s85, s77\n" \
    "s_add_u32 s85, s77, 1\n" \
    "v_cmp_le_u32 vcc, s85, v27\n" \
    "s_cmp_eq_u64 vcc, exec\n" \
    "s_cselect_b32 s77, s85, s77\n" \
    "s_branch O_ff1_d_%=\n" \
    "O_of_%=:\n" \
    "s_waitcnt vmcnt(0)\n" \
    "s_memrealtime s[80:81]\n" \
    "s_waitcnt lgkmcnt(0)\n" \
    "s_mov_b32 s83, s80\n" \
    "s_mov_b32 s82, 0\n" \
    "O_of_l_%=:\n" \
    "v_cmp_eq_u32 vcc, s63, v28\n" \
    "s_nop 1\n" \
    "v_cndmask_b32 v29, 0, v29, vcc\n" \
    "v_max_u32 v29, v29, v23\n" \
    "v_cmp_le_u32 vcc, s69, v29\n" \
    "s_cmp_eq_u64 vcc, exec\n" \
    "s_cbranch_scc1 O_of_up_%=\n" \
    "s_add_u32 s82, s82, 1\n" \
    "s_and_b32 s85, s82, 63\n" \
    "s_cmp_lg_u32 s85, 0\n" \
    "s_cbranch_scc1 O_of_g_ok_%=\n" \
    "s_mov_b32 s85, 1048832\n" \
    "buffer_load_dword v32, off, s[36:39], s85 sc1\n" \
    "s_waitcnt vmcnt(0)\n" \
    "v_readfirstlane_b32 s85, v32\n" \
    "s_cmp_eq_u32 s85, s63\n" \
    "s_cbranch_scc1 O_end_%=\n" \
    "s_memrealtime s[80:81]\n" \
    "s_waitcnt lgkmcnt(0)\n" \
    "s_sub_u32 s85, s80, s83\n" \
    "s_cmp_gt_u32 s85, 3000000\n" \
    "s_cbranch_scc1 O_giveup_%=\n" \
    "O_of_g_ok_%=:\n" \
    "s_sleep 1\n" \
    "buffer_load_dwordx2 v[28:29], v18, s[36:39], s78 offen sc1\n" \
    "s_waitcnt vmcnt(0)\n" \
    "s_branch O_of_l_%=\n" \
    "O_of_up_%=:\n" \
    "s_branch O_of_d_%=\n" \
    "O_giveup_%=:\n" \
    "s_mov_b32 s85, 1048832\n" \
    "v_mov_b32 v32, s63\n" \
    "buffer_store_dword v32, off, s[36:39], s85 sc1\n" \
    "O_end_%=:\n" \
    "s_endpgm\n"

DI void put_rsrc(v16u& s, int i, const void* p, unsigned bytes) {
  const uint64_t u = (uint64_t)p;
  s[i] = (unsigned)u; s[i + 1] = (unsigned)(u >> 32) & 0xffffu; s[i + 2] = bytes; s[i + 3] = 0x00020000u;
}
#define RUN_ROLE(TEXT, VB)                                                                                     \
  s1[11] = e;                                                                                                   \
  _Pragma("unroll") for (int i = 0; i < 16; ++i) { s0[i] = uni(s0[i]); s1[i] = uni(s1[i]); }                   \
  asm volatile(TEXT :: "{v[0:15]}"(va), VB(vb), "{s[36:51]}"(s0), "{s[52:67]}"(s1) : "v255", "a255", "s101", "vcc", "memory")
DI unsigned dbg_addr(unsigned lane, int role, int w) { return lane == 0 ? W_DBG + (role * 4 + w) * 4096 : DROP; }

DI void role_head(const Args& a, int x, int h, int eta, unsigned lane, int w, unsigned e) {
  const unsigned g = lane >> 4, c16 = lane & 15, base = w * 40960u, t = w & 1, u = w >> 1;
  const unsigned NEG = 0xf149f2cau;
  v16u va = {}, s0 = {}, s1 = {}; v32u vb = {};
#pragma unroll
  for (int i = 0; i < 4; ++i) {
    va[i] = img_src<128, MAP_SP>(lane, i, 0, 8192, (h * 128 + 64 * t) * 2);
    va[5 + 2 * i] = base + tr_addr<128>(lane, i, 0); va[6 + 2 * i] = base + tr_addr<128>(lane, i, 1);
  }
  va[4] = img_src<32, MAP_SP>(lane, 0, 0, 8192, (h * 128 + 64 * eta + 16 * w) * 2);
  va[13] = base + tr_addr<32>(lane, 0, 0); va[14] = base + tr_addr<32>(lane, 0, 1);
  vb[28] = 69632 + lane * 16 + w * 8192;
  vb[29] = (lane == 0 && w == 0) ? C_RDY + (2 * h + eta) * 8 : DROP; vb[30] = W_DBG + ((2 * h + eta) * 4 + w) * 4096;
  vb[0] = lane * 16;
  vb[1] = (lane & 4) ? 0xEEEE : 0x4444;
  vb[2] = !(c16 & 4) ? EX_O + (h >> 3) * 32768 + (c16 >> 3) * 16384 + (h & 7) * 1024 + (2 * w + eta) * 128 + (g >> 1) * 64 + (c16 & 3) * 16 + (g & 1) * 8 : DROP;
  vb[3] = lane == 0 ? C_OF + (2 * h + eta) * 128 + w * 16 : DROP;
  vb[4] = C_PF + ((lane < 4 ? lane >> 1 : 2) * 16 + (h >> 1)) * 128 + (2 * (h & 1) + (lane < 4 ? (lane & 1) : eta)) * 8;
  vb[5] = 61440 + (64 * t + 16 * (g & 1) + c16) * 32 + (g >> 1) * 16 + u * 8;
  vb[6] = 61440 + (4 * g + (c16 >> 2)) * 32 + (c16 & 3) * 8;
  vb[7] = 65536 + lane * 8 + w * 512; vb[8] = 65536 + lane * 8 + u * 1024;
  const int lim = 16 * (int)t + (int)c16 - 4 * (int)g;
#pragma unroll
  for (int r = 0; r < 4; ++r) {
    vb[12 + r] = (g & 1) ? 0u : NEG;
    vb[16 + r] = r <= lim ? 0u : NEG; vb[20 + r] = r + 16 <= lim ? 0u : NEG; vb[24 + r] = 0x3f803f80u;
  }
  put_rsrc(s0, 0, a.ws, W_END); put_rsrc(s0, 4, u ? a.wk : a.wq, DM * 4096 * 2); put_rsrc(s0, 12, a.wv, DM * 4096 * 2);
  s1[0] = base; s1[1] = C_START + (4 * x + w) * 128; s1[7] = C_MH + (4 * x + w) * 128; s1[2] = W_HB + (h << 21) + w * 2048u; s1[3] = W_HB + (h << 21) + 16384 + eta * 4096u + w * 1024u;
  s1[4] = w * 2048; s1[5] = 16384 + w * 1024; s1[6] = W_HB + (h << 21) + H_OT + (t * 4 + 2 * eta + u) * 1024u;
  s1[9] = (x & 1) * (256u * 8192u); s1[10] = t * 4096; s1[14] = u * 2048;
  RUN_ROLE(HEAD_ASM, "{v[16:47]}");
}

DI void role_col(const Args& a, rsrc_t ws, int x, int cg, int rho, unsigned lane, int w, unsigned e, unsigned t_in) {
  const unsigned g = lane >> 4, c16 = lane & 15, base = w * 40960u;
  const unsigned prow = (lane >> 1) & 3, pcol = 16 * (g & 1) + 8 * ((lane >> 3) & 1) + 4 * (lane & 1);
  const unsigned mpub = lane < 32 ? (cg & 7) * 512 + (g & 1) * 256 + rho * 128 + c16 * 8 : DROP;
  if (w == 0) {
    const v2u v = *(const v2u*)(a.mem0 + (4 * rho + prow) * DM + 32 * cg + pcol);
#pragma unroll
    for (int k = 0; k < 24; ++k) st8(v2u{~0u, ~0u}, ws, mpub + (k >> 3) * EX_BUF + (k & 7) * 4096);
    st8(v2u{min(v.x, 0xfffffffeu), min(v.y, 0xfffffffeu)}, ws, mpub + C_M0 + (cg >> 3) * 4096);
  }
  until(t_in + DLY_COL);
  vm0();
  v16u va = {}, vb = {}, s0 = {}, s1 = {};
#pragma unroll
  for (int hf = 0; hf < 2; ++hf) {
    va[2 * hf] = img_src<64, MAP_SP>(lane, 0, hf, 1280, cg * 64); va[2 * hf + 1] = img_src<64, MAP_SP>(lane, 1, hf, 1280, cg * 64);
    va[6 + hf] = base + tr_addr<64>(lane, 0, hf); va[8 + hf] = base + tr_addr<64>(lane, 1, hf);
  }
  va[13] = (lane == 0 && w == 0) ? C_RDY + (64 + 2 * cg + rho) * 8 : DROP;
  vb[0] = lane * 16; vb[1] = (lane & 4) ? 0xEEEE : 0x4444; vb[2] = mpub;
  vb[3] = ((w * 4 + (g & 1)) * 32 + 16 * (g >> 1) + c16) * 4; vb[4] = (prow * 32 + pcol) * 4;
  vb[5] = dbg_addr(lane, 64 + 2 * cg + rho, w);
  vb[6] = (lane == 0 && w == 0) ? C_CD + (2 * cg + rho) * 64 : DROP;
  put_rsrc(s0, 0, a.ws, W_END); put_rsrc(s0, 4, a.wo, 4096 * DM * 2);
  s1[0] = base; s1[1] = C_START + (4 * x + w) * 128;
  s1[2] = EX_O + w * 32768 + rho * 16384;
  s1[6] = cg >> 3; s1[4] = w * 1024u * 1280u; s1[5] = C_MC + (4 * x + w) * 128; s1[7] = C_MH + (4 * x + w) * 128; s1[10] = w;
  RUN_ROLE(COL_ASM, "{v[16:31]}");
}

template <bool FFN> DI void role_proj(const Args& a, int p, unsigned lane, int w, unsigned e) {
  const unsigned base = w * 28672u;
  v16u va = {}, vb = {}, s0 = {}, s1 = {};
  put_rsrc(s0, 0, a.ws, W_END);
  vb[0] = lane * 16; vb[1] = lane == 0 ? C_PF + p * 128 + w * 8 : DROP; vb[2] = lane * 16; vb[3] = C_LN + lane * 8;
  vb[4] = dbg_addr(lane, 104 + p, w);
  s1[0] = base; s1[2] = w * 10240;
  if constexpr (FFN) {
    const int fks = 4 * (p - 48) + w;
    const bool live = fks < 54;
#pragma unroll
    for (int i = 0; i < 2; ++i) {
      va[i] = img_src<64, MAP_NAT>(lane, i, 0, FF * 2, (live ? fks : 0) * 64);
      va[4 + i] = base + tr_addr<64>(lane, 0, i); va[6 + i] = base + tr_addr<64>(lane, 1, i);
    }
    if (!live) vb[2] = DROP;
    put_rsrc(s0, 4, a.wg, DM * FF * 2); put_rsrc(s0, 8, a.wu, DM * FF * 2);
    s1[1] = W_HD + fks * 1024;
    RUN_ROLE(PROJ2_ASM, "{v[16:31]}");
  } else {
    const int m = p >> 4, pair = p & 15, hh = 2 * pair + (w >> 1), eh = w & 1;
#pragma unroll
    for (int i = 0; i < 4; ++i) {
      va[i] = img_src<128, MAP_NAT>(lane, i, 0, 8192, (pair * 256 + w * 64) * 2);
      va[4 + 2 * i] = base + tr_addr<128>(lane, i, 0); va[5 + 2 * i] = base + tr_addr<128>(lane, i, 1);
    }
    put_rsrc(s0, 4, m == 0 ? a.wk : (m == 1 ? a.wq : a.wv), DM * 4096 * 2);
    if (m < 2) {
      s1[1] = W_HB + (hh << 21) + m * 8192u + eh * 2048u;
      RUN_ROLE(PROJ0_ASM, "{v[16:31]}");
    } else {
      s1[1] = W_HB + (hh << 21) + 16384 + eh * 4096u;
      RUN_ROLE(PROJ1_ASM, "{v[16:31]}");
    }
  }
}

DI void role_out(const Args& a, int repl, int cg, unsigned lane, int w, unsigned e) {
  const unsigned g = lane >> 4, c16 = lane & 15, base = w * 40960u;
  v16u va = {}, vb = {}, s0 = {}, s1 = {};
#pragma unroll
  for (int i = 0; i < 2; ++i) {
    va[i] = img_src<64, MAP_PERM>(lane, i, 0, 1280, cg * 64);
    va[4 + i] = base + tr_addr<64>(lane, 0, i); va[6 + i] = base + tr_addr<64>(lane, 1, i);
  }

  vb[0] = lane * 16;
  vb[1] = C_PF + (48 + min(lane, 13u)) * 128 + w * 8;
  vb[2] = C_OF + 64 + (16 * w + ((lane >> 1) & 15)) * 128 + (lane & 1) * 32;
  vb[3] = w < 2 ? (c16 * DM + 32 * cg + 16 * w + 4 * g) * 2 : DROP;
  vb[4] = w * 4096 + lane * 16; vb[5] = w * 1024 + lane * 16;
  vb[6] = lane < (w < 2 ? 14u : 13u) ? 0u : ~0u; vb[7] = lane < 32 ? 0u : ~0u;
  vb[8] = dbg_addr(lane, 166 + repl * 20 + cg, w);
  put_rsrc(s0, 0, a.ws, W_END); put_rsrc(s0, 4, a.wo, 4096 * DM * 2); put_rsrc(s0, 8, a.wd, FF * DM * 2); put_rsrc(s0, 12, a.x, SEQ * DM * 2);
  s1[0] = base; s1[1] = w * 32u * 1280u; s1[2] = w * 1024u * 1280u; s1[3] = (w < 2 ? 52 + w : w) * 1024; s1[4] = w * 1024;
  s1[5] = W_HB + (w << 24) + H_OT; s1[6] = (unsigned)(uint64_t)a.out; s1[7] = (unsigned)((uint64_t)a.out >> 32) & 0xffffu; s1[8] = repl; s1[10] = w;
  RUN_ROLE(OUT_ASM, "{v[16:31]}");
}

DI void normalise(const Args& a, rsrc_t ws, int u0, int u1, int pf, unsigned lane, int w, unsigned e, float* red) {
  const unsigned g = lane >> 4, c16 = lane & 15;
  DG(const unsigned dstart = ticks();)
  v4u v[2][5];
#pragma unroll
  for (int k = 0; k < 2; ++k) {
    const uint16_t* row = a.x + (size_t)(16 * (k ? (u1 < 128 ? u1 : u0) : u0) + c16) * DM + 8 * g + 160 * w;
#pragma unroll
    for (int j = 0; j < 5; ++j) v[k][j] = *(const v4u*)(row + 32 * j);
  }
  if (pf >= 0) {
    const rsrc_t r = __builtin_amdgcn_make_buffer_rsrc((void*)a.wo, 0, 4096 * DM * 2, 0x00020000);
    const unsigned o = (pf * 4 + w) * 1024 + lane * 16;
    for (int i = 0; i < 18; ++i) asm volatile("s_mov_b32 m0, %0\n\ts_nop 0\n\tbuffer_load_dwordx4 %1, %2, %3 offen lds" :: "s"(4096 + w * 1024), "v"(o), "s"(r), "s"(i * 74 * 4096) : "memory");
    if (PFQ) {
#pragma unroll
      for (int m = 0; m < 3; ++m) {
        const rsrc_t q = __builtin_amdgcn_make_buffer_rsrc((void*)(m == 0 ? a.wk : m == 1 ? a.wv : a.wq), 0, DM * 4096 * 2, 0x00020000);
        for (int i = 0; i < PFQ; ++i) asm volatile("s_mov_b32 m0, %0\n\ts_nop 0\n\tbuffer_load_dwordx4 %1, %2, %3 offen lds" :: "s"(4096 + w * 1024), "v"(o), "s"(q), "s"(DM * 8192 - (PFQ - i) * 74 * 4096) : "memory");
      }
    }
  }
  float mean[2];
#pragma unroll
  for (int k = 0; k < 2; ++k) {
    float s = 0.f;
#pragma unroll
    for (int j = 0; j < 20; ++j) s += bflo(v[k][j >> 2][j & 3]) + bfhi(v[k][j >> 2][j & 3]);
    s += __shfl_xor(s, 16); s += __shfl_xor(s, 32);
    if (lane < 16) red[(k * 4 + w) * 16 + lane] = s;
  }
  __syncthreads();
#pragma unroll
  for (int k = 0; k < 2; ++k) {
    const float* r = red + k * 64 + c16;
    mean[k] = ((r[0] + r[16]) + (r[32] + r[48])) * (1.0f / DM);
    float s = 0.f;
#pragma unroll
    for (int j = 0; j < 20; ++j) { const float p = bflo(v[k][j >> 2][j & 3]) - mean[k], q = bfhi(v[k][j >> 2][j & 3]) - mean[k]; s += p * p + q * q; }
    s += __shfl_xor(s, 16); s += __shfl_xor(s, 32);
    if (lane < 16) red[128 + (k * 4 + w) * 16 + lane] = s;
  }
  __syncthreads();
#pragma unroll
  for (int k = 0; k < 2; ++k) {
    const float* r = red + 128 + k * 64 + c16;
    const float var = ((r[0] + r[16]) + (r[32] + r[48])) * (1.0f / DM);
    const float mb = rbf(mean[k]), inv = 1.0f / rbf(sqrtf(rbf(rbf(var) + 1e-5f)));
    const int u = k ? u1 : u0;
#pragma unroll
    for (int j = 0; j < 5; ++j) {
      v4u o;
#pragma unroll
      for (int c = 0; c < 4; ++c) {
        const unsigned d = pk2(bflo(v[k][j][c]) - mb, bfhi(v[k][j][c]) - mb);
        o[c] = pk2(bflo(d) * inv, bfhi(d) * inv);
      }
      st16(o, ws, u < 128 ? W_XN + (u * 20 + 5 * w + j) * 1024 + lane * 16 : DROP);
    }
  }
  vm0();
  __syncthreads();
  if (w == 0) st4(e, ws, lane == 0 ? C_LN + u0 * 4 : (lane == 1 && u1 < 128 ? C_LN + u1 * 4 : DROP));
  DG(st16(v4u{dstart, ticks(), 0, 0}, ws, lane == 0 && u0 < 25 ? W_DBG + ((226 + u0) * 4 + w) * 4096 + 128 * 16 : DROP);)
  vm0();
}

DI unsigned wmax(unsigned v) {
#pragma unroll
  for (int m = 32; m >= 1; m >>= 1) v = max(v, (unsigned)__shfl_xor((int)v, m));
  return uni(v);
}
DI void role_conduct(rsrc_t ws, unsigned lane, int w, unsigned e, volatile unsigned* lat) {
  if (threadIdx.x < 9) lat[threadIdx.x] = threadIdx.x < 8 ? 0xffffu : 0u;
  __syncthreads();
  if (w >= 2) {
    for (unsigned rep = 0; rep < 256 && lat[8] == 0; ++rep)
#pragma unroll
      for (int k = 0; k < 4; ++k) {
        const unsigned c0 = cycles();
        st4(0u, ws, lane == 0 ? PROBE + (4 * (w - 2) + k) * 4096 + (rep & 31) * 128 : DROP);
        vm0();
        const unsigned d = cycles() - c0;
        if (d < lat[4 * (w - 2) + k]) lat[4 * (w - 2) + k] = d;
      }
  }
  unsigned n = 0; const unsigned t0 = ticks();
  if (w == 0) {
    v2u r0;
    for (;;) {
      unsigned r1, p0, p1, p2;
      const unsigned pf = C_PF + (lane >> 2) * 128 + (lane & 3) * 8;
      asm volatile("buffer_load_dwordx2 %0, %5, %10, 0 offen sc1\n\tbuffer_load_dword %1, %6, %10, 0 offen sc1\n\tbuffer_load_dword %2, %7, %10, 0 offen sc1\n\t"
                   "buffer_load_dword %3, %8, %10, 0 offen sc1\n\tbuffer_load_dword %4, %9, %10, 0 offen sc1\n\ts_waitcnt vmcnt(0)"
                   : "=&v"(r0), "=&v"(r1), "=&v"(p0), "=&v"(p1), "=&v"(p2)
                   : "v"(C_RDY + lane * 8), "v"(C_RDY + 512 + min(lane, 39u) * 8), "v"(pf), "v"(pf + 2048), "v"(pf + 4096), "s"(ws) : "memory");
      if (__all(r0.x == e && r1 == e && p0 == e && p1 == e && p2 == e)) break;
      spin_guard(n, t0, ws, e);
    }
    lat[8] = 1;
    const unsigned a0 = min(lat[0], lat[1]), a1 = min(lat[2], lat[3]), a2 = min(lat[4], lat[5]), a3 = min(lat[6], lat[7]);
    const unsigned cfg = min(a0, a2) < min(a1, a3) ? 8192u : 0u;
    const unsigned pages = a2 + a3 < a0 + a1 ? 16384u : 0u;
    float per = (float)(r0.y >> 20) / (float)((r0.y & 0xfffffu) + 1u);
#pragma unroll
    for (int m = 32; m >= 1; m >>= 1) per = fmaxf(per, __shfl_xor(per, m));
    per = fminf(fmaxf(per, 0.040f), 0.080f);
    unsigned ch = CH ? CH : uni((unsigned)(HA + HB * per)), cc = CC ? CC : uni((unsigned)(CA + CB * per));
    st16(v4u{e, ticks() + LEAD, ch | (cc << 16), cfg | pages}, ws, lane < 16 ? C_START + lane * 128 : DROP);
    DG(st16(v4u{a0 | (a1 << 16), a2 | (a3 << 16), ch | (cc << 16), ticks()}, ws, lane == 0 ? W_DBG + 255 * 16384 : DROP);)
    bool calm = true;
    unsigned dh = DH, dc = DC, hmin = 999, cmin = 999, nch = ch, ncc = cc, eff = 0;
    for (unsigned s = 0; s < 63; ++s) {
      v4u v;
      for (;;) { v = ld16(ws, C_OF + lane * 128); if (__all(v.x == e && v.y >= s)) break; spin_guard(n, t0, ws, e); }
      st16(v4u{e, s, ticks() + LEADM, 0}, ws, lane < 16 ? C_MC + cfg + lane * 128 : DROP);
      const bool hoff = __any(v.y == s && (v.z & 3)), hfail = __any(v.y == s && (v.z & 1));
      const unsigned hd = wmax(v.y == s ? v.z >> 8 : 0);
      for (;;) { v = ld16(ws, C_CD + (cfg ^ 8192u) + min(lane, 39u) * 64); if (__all(v.x == e && v.y > s)) break; spin_guard(n, t0, ws, e); }
      const bool coff = __any(v.z != 0);
      if (s + 1 == eff) { ch = nch; cc = ncc; }
      if (calm && s > 3) { if (hfail) { dc += BUMP; cc += BUMP; ncc += BUMP; } else if (coff && !hoff) { dh += BUMP; ch += BUMP; nch += BUMP; } }
      calm = !hoff && !coff;
      st16(v4u{e, s + 1, ticks() + LEADM, (unsigned)__all(v.z != 0) | (ch << 8) | (cc << 20)}, ws, lane < 16 ? C_MH + cfg + lane * 128 : DROP);
      if (calm && s >= 3) { hmin = min(hmin, hd); cmin = min(cmin, wmax(v.w)); }
      if (!CH && hmin < 999 && (s & 7) == FIRST) {
        nch = hmin + dh; ncc = cmin + dc; hmin = cmin = 999;
        if (nch + 1 - ch <= 2 && ncc + 1 - cc <= 2) continue;
        eff = s + 4;
        st8(v2u{e, eff | (nch << 8) | (ncc << 20)}, ws, lane < 16 ? C_CD + cfg + lane * 128 : DROP);
        DG(st4(nch | (ncc << 16), ws, lane == 0 ? W_DBG + 255 * 16384 + 32 + (s >> 3) * 4 : DROP);)
      }
    }
    st4(ch | (cc << 16), ws, lane == 0 ? W_DBG + 255 * 16384 + 16 : DROP);
  } else if (w == 1) {
    for (;;) {
      bool ok = true;
#pragma unroll
      for (int k = 0; k < 4; ++k) ok = ok && ld4(ws, C_IN + (64 * k + lane) * 4) == e;
      if (__all(ok)) break;
      spin_guard(n, t0, ws, e);
      __builtin_amdgcn_s_sleep(32);
    }
    st4(e, ws, lane == 0 ? C_EPOCH : DROP);
  }
}

__global__ void __launch_bounds__(256, 1) rmt_kernel(Args a) {
  extern __shared__ __attribute__((aligned(16))) unsigned char lds[];
  const unsigned lane = threadIdx.x & 63;
  const int w = (int)uni(threadIdx.x >> 6), t = blockIdx.x >> 3;
  const rsrc_t ws = __builtin_amdgcn_make_buffer_rsrc(a.ws, 0, W_END, 0x00020000);
  if (w == 0) {
    const unsigned e = uni(ld4(ws, C_EPOCH)) + 1;
    unsigned x = (unsigned)__builtin_amdgcn_s_getreg(20 | (3 << 11)) & 7u;
    for (int i = 0; i < 8; ++i, x = (x + 1) & 7u) {
      unsigned old = e;
      asm volatile("buffer_atomic_swap %0, %1, %2, 0 offen sc0 sc1\n\ts_waitcnt vmcnt(0)" : "+v"(old) : "v"(lane == 0 ? C_IN + (8 * t + x) * 4 : DROP), "s"(ws) : "memory");
      if (uni(old) != e) break;
    }
    if (lane == 0) { ((volatile unsigned*)lds)[0] = x; ((volatile unsigned*)lds)[1] = e; }
  } else if (CODEPF) {
    uint64_t pc; unsigned len, junk;
    asm volatile("s_getpc_b64 %0\nrmt_code_here:\n\ts_mov_b32 %1, rmt_code_end-rmt_code_here" : "=s"(pc), "=s"(len));
    const unsigned o = min(((t & 3) * 192 + (w - 1) * 64 + lane) * 128u, len - 4u) & ~3u;
    asm volatile("global_load_dword %0, %1, %2\n\ts_waitcnt vmcnt(0)" : "=&v"(junk) : "v"(o), "s"(pc) : "memory");
  }
  __syncthreads();
  const int x = (int)uni(((volatile unsigned*)lds)[0]);
  const unsigned e = uni(((volatile unsigned*)lds)[1]);
  __syncthreads();
  const int y = x - 4;
  int u0 = 128;
  if (x < 4) { if (t >= 26) u0 = 6 * x + t - 26; }
  else if (t >= 12 && !(y == 3 && t == 31)) u0 = y < 3 ? 29 + 20 * y + t - 12 : (t < 26 ? 89 + t - 12 : t - 2);
  const unsigned t_in = ticks();
  if (u0 < 128) normalise(a, ws, u0, u0 < 25 ? 103 + u0 : 128, PFWO && u0 >= 29 ? u0 - 29 : -1, lane, w, e, (float*)lds);
  if (t >= 26 && w == 0) (void)ld4(ws, lane < 39 ? lane * (2u << 20) + 4096 * x : DROP);
  if (x == 7 && t == 31) role_conduct(ws, lane, w, e, (volatile unsigned*)lds);
  if (x < 4) {
    if (t < 16) role_head(a, x, 8 * x + (t >> 1), t & 1, lane, w, e);
    else if (t < 26) role_col(a, ws, x, 10 * (x >> 1) + t - 16, x & 1, lane, w, e, t_in);
  } else {
    if (t < 12) role_proj<false>(a, 4 * t + y, lane, w, e);
    else if (y < 3) {
      until(t_in + DLY_OUT); role_out(a, y, t - 12, lane, w, e); }
    else if (t < 26) { until(t_in + DLY_FFN); role_proj<true>(a, 36 + t, lane, w, e); }
  }
}

extern "C" void kernel_launch(void* const* d_in, const int* in_sizes, int n_in, void* d_out, int out_size, void* d_ws, size_t ws_size,
                              hipStream_t stream) {
  Args a;
  a.x = (const uint16_t*)d_in[0]; a.mem0 = (const uint16_t*)d_in[1]; a.wq = (const uint16_t*)d_in[2]; a.wk = (const uint16_t*)d_in[3];
  a.wv = (const uint16_t*)d_in[4]; a.wo = (const uint16_t*)d_in[5]; a.wg = (const uint16_t*)d_in[8]; a.wu = (const uint16_t*)d_in[9];
  a.wd = (const uint16_t*)d_in[10]; a.out = (uint16_t*)d_out;
  a.ws = (unsigned char*)(((uintptr_t)d_ws + (2u << 20) - 1) & ~(uintptr_t)((2u << 20) - 1));
  (void)hipFuncSetAttribute((const void*)rmt_kernel, hipFuncAttributeMaxDynamicSharedMemorySize, LDS_BYTES);
  rmt_kernel<<<256, 256, LDS_BYTES, stream>>>(a);
}
```
